```python
import torch

def _softmax(x, axis=-1):
    return torch.softmax(x, dim=axis)
def _gelu(x):
    return torch.nn.functional.gelu(x, approximate="tanh")
def _silu(x):
    return torch.nn.functional.silu(x)
def _sigmoid(x):
    return torch.sigmoid(x)
def _layernorm(x, axis=-1, eps=1e-5):
    m = x.mean(dim=axis, keepdim=True)
    v = x.var(dim=axis, keepdim=True, unbiased=False)
    return (x - m) / torch.sqrt(v + eps)
def _scatter_add(tgt, idx, src, axis=0):
    return tgt.clone().index_add_(axis, idx, src)
def _segment_sum(src, seg, n):
    out = torch.zeros((n,) + src.shape[1:], dtype=src.dtype)
    return out.index_add_(0, seg, src)
def _pad(x, pw):
    flat = tuple(v for pair in reversed(pw) for v in pair)
    return torch.nn.functional.pad(x, flat)

kernel_name = 'sequential_preLN__mha_block_cached__geglu_ffn__decode'

def setup_inputs():

    _w = (lambda a: (lambda t: t.to(torch.bfloat16) if t.is_floating_point() else t)(torch.as_tensor(a)))
    def _A_setup():
        B, Sq, D = 2, 1, 640
        H, Sk, Dh = 32, 16384, 192
        return {
            "x": _w((lambda *s: torch.randn(*s, dtype=torch.bfloat16) * 0.05)(B, Sq, D)),
            "k_cache": _w((lambda *s: torch.randn(*s, dtype=torch.bfloat16) * 3.2)(B, H, Sk, Dh)),
            "v_cache": _w((lambda *s: torch.randn(*s, dtype=torch.bfloat16) * 0.4)(B, H, Sk, Dh)),
            "wq": _w((lambda *s: torch.randn(*s, dtype=torch.bfloat16) * 3.2)(D, H * Dh) * (D ** -0.5)),
            "wo": _w((lambda *s: torch.randn(*s, dtype=torch.bfloat16) * 0.4)(H * Dh, D) * ((H * Dh) ** -0.5)),
        }
    _A_in = _A_setup()

    def _B_setup():
        B, S, D, F = 2, 1, 640, 1536
        return {
            "x": _w((lambda *s: torch.randn(*s, dtype=torch.bfloat16) * 0.05)(B, S, D)),
            "w_gate": _w((lambda *s: torch.randn(*s, dtype=torch.bfloat16) * 0.05)(D, F)),
            "w_up": _w((lambda *s: torch.randn(*s, dtype=torch.bfloat16) * 0.05)(D, F)),
            "w_down": _w((lambda *s: torch.randn(*s, dtype=torch.bfloat16) * 0.05)(F, D)),
        }
    _B_in = _B_setup()

    return {
        "A_x": _A_in["x"],
        "A_k_cache": _A_in["k_cache"],
        "A_v_cache": _A_in["v_cache"],
        "A_wq": _A_in["wq"],
        "A_wo": _A_in["wo"],
        "B_x": _B_in["x"],
        "B_w_gate": _B_in["w_gate"],
        "B_w_up": _B_in["w_up"],
        "B_w_down": _B_in["w_down"],
    }

def reference(A_x, A_k_cache, A_v_cache, A_wq, A_wo, B_x, B_w_gate, B_w_up, B_w_down):
    def _A_ref(x, k_cache, v_cache, wq, wo):
        B, Sq, D = x.shape
        H, Dh = 32, 192
        q = (lambda x, *d: x.permute(*d))((x @ wq).reshape(B, Sq, H, Dh), 0, 2, 1, 3)
        scores = torch.einsum("bhqd,bhkd->bhqk", q, k_cache) * (Dh ** -0.5)
        p = _softmax(scores, axis=-1)
        o = torch.einsum("bhqk,bhkd->bhqd", p, v_cache)
        return (lambda x, *d: x.permute(*d))(o, 0, 2, 1, 3).reshape(B, Sq, H * Dh) @ wo

    def _B_ref(x, w_gate, w_up, w_down):
        return (_gelu(x @ w_gate) * (x @ w_up)) @ w_down

    x = A_x
    h = x + _A_ref(_layernorm(x, axis=-1), A_k_cache, A_v_cache, A_wq, A_wo)
    h2 = h + _B_ref(_layernorm(h, axis=-1), B_w_gate, B_w_up, B_w_down)
    return h2
```

```cpp
#include <hip/hip_runtime.h>
#include <hip/hip_bf16.h>
#include <stdint.h>

#define MIMIC_SCORE 1

namespace {
typedef unsigned v4u __attribute__((ext_vector_type(4)));
typedef unsigned v2u __attribute__((ext_vector_type(2)));
typedef float v4f __attribute__((ext_vector_type(4)));
typedef float v2f __attribute__((ext_vector_type(2)));
typedef short v4s __attribute__((ext_vector_type(4)));
typedef short v8s __attribute__((ext_vector_type(8)));
typedef __bf16 bf2 __attribute__((ext_vector_type(2)));
typedef __bf16 bf8 __attribute__((ext_vector_type(8)));
#define LAS __attribute__((address_space(3)))
#define CAS __attribute__((address_space(4)))

constexpr int DM = 640, NH = 32, SK = 16384, DH = 192, FF = 1536;
constexpr int NWG = 256, NW = 8, NS = NW - 1, NTHR = NW * 64;
constexpr unsigned ITEMB = 8 * DH * 2;
constexpr unsigned HEADB = (unsigned)SK * DH * 2, B1OFF = NH * HEADB;
constexpr int IPH = SK / 8;
constexpr int S0 = IPH / 8, S1 = 192, STAT = S0 + S1;
constexpr int POOL = IPH - 8 * S1, LEAD = 8, NEXTRA = 3;
constexpr unsigned END = 0xF0000000u, EMPTY = 0xFFFFFFFFu;
constexpr int NRED = DM / 4, NFFN = FF / 8, NREP = 16;
constexpr int SPIN_CAP = 1 << 20;
constexpr float M_INIT = -3.0e38f;

constexpr int WS_CTR = 4096, CTR_STRIDE = 4352;
constexpr int WS_Q = 0x30000, WS_P = 0x100000, WS_Y = 0x200000, WS_H = 0x480000, WS_Z = 0x500000, WS_BYTES = 0x700000;
constexpr int OOB = (int)0x80000000u;
static_assert(WS_CTR + NH * CTR_STRIDE <= WS_Q && WS_Q + NH * 8 * DH * 16 <= WS_P && WS_P + NH * 64 * 32 * 16 <= WS_Y, "ws map");
static_assert(WS_Y + NRED * NWG * 64 <= WS_H && WS_H + NREP * 320 * 16 <= WS_Z && WS_Z + NRED * NFFN * 64 <= WS_BYTES, "ws map");

constexpr int L_WO = 0, L_G = L_WO + 24 * 1280, L_U = L_G + DM * 16, L_WD = L_U + DM * 16, L_QP = L_WD + 8 * 1280;
constexpr int L_QS = L_QP + NW * 2 * DH * 4, RECF = DH + 4, L_REC = L_QS + 2 * DH * 2, L_OB = L_REC + NS * 2 * RECF * 4;
constexpr int L_LN = L_OB + 128, L_CT = L_LN + 2 * DM * 2, DYNMAX = POOL + 64, L_DYN = L_CT + 64, L_BYTES = L_DYN + DYNMAX * 4;
static_assert(L_BYTES > 81920 && L_BYTES <= 160 * 1024 && L_REC % 16 == 0 && L_OB % 16 == 0 && L_LN % 16 == 0, "one workgroup per CU");

struct Args {
  const unsigned short *x, *k, *v, *wq, *wo, *wg, *wu, *wd;
  unsigned short* out;
  unsigned char* ws;
};

__device__ __forceinline__ float dot2(unsigned a, unsigned b, float c) {
  return __builtin_amdgcn_fdot2_f32_bf16(__builtin_bit_cast(bf2, a), __builtin_bit_cast(bf2, b), c, false);
}
template <int CTRL> __device__ __forceinline__ float dpp(float x) {
  return __builtin_bit_cast(float, __builtin_amdgcn_mov_dpp(__builtin_bit_cast(int, x), CTRL, 0xf, 0xf, true));
}
constexpr int XOR1 = 0xB1, XOR2 = 0x4E, HMIR = 0x141, ROR4 = 0x124, ROR8 = 0x128;
__device__ __forceinline__ float bflo(unsigned x) { return __builtin_bit_cast(float, x << 16); }
__device__ __forceinline__ float bfhi(unsigned x) { return __builtin_bit_cast(float, x & 0xffff0000u); }
__device__ __forceinline__ unsigned pk(float lo, float hi) {
  const bf2 r = {(__bf16)lo, (__bf16)hi};
  return __builtin_bit_cast(unsigned, r);
}
__device__ __forceinline__ float bfr(float x) { return (float)(__bf16)x; }
__device__ __forceinline__ unsigned fbits(float x) { return __builtin_bit_cast(unsigned, x); }
__device__ __forceinline__ float ubits(unsigned x) { return __builtin_bit_cast(float, x); }
__device__ __forceinline__ float sw16add(float a, float b) {
  const auto s = __builtin_amdgcn_permlane16_swap(fbits(a), fbits(b), false, false);
  return ubits(s[0]) + ubits(s[1]);
}
__device__ __forceinline__ float sw32add(float a, float b) {
  const auto s = __builtin_amdgcn_permlane32_swap(fbits(a), fbits(b), false, false);
  return ubits(s[0]) + ubits(s[1]);
}
__device__ __forceinline__ float sw16max(float a) {
  const auto s = __builtin_amdgcn_permlane16_swap(fbits(a), fbits(a), false, false);
  return fmaxf(ubits(s[0]), ubits(s[1]));
}
__device__ __forceinline__ float sw32max(float a) {
  const auto s = __builtin_amdgcn_permlane32_swap(fbits(a), fbits(a), false, false);
  return fmaxf(ubits(s[0]), ubits(s[1]));
}
__device__ __forceinline__ float rowsum16(float s) { s += dpp<XOR1>(s); s += dpp<XOR2>(s); s += dpp<HMIR>(s); s += dpp<ROR8>(s); return s; }
__device__ __forceinline__ float rdlane(float x, int l) { return ubits(__builtin_amdgcn_readlane(fbits(x), l)); }
__device__ __forceinline__ v4u mkrsrc(const void* p, unsigned bytes) {
  const unsigned long long a = (unsigned long long)p;
  return v4u{(unsigned)a, (unsigned)(a >> 32) & 0xffffu, bytes, 0x00020000u};
}
__device__ __forceinline__ v4f mfma(v4u a, v4u b, v4f c) {
  return __builtin_amdgcn_mfma_f32_16x16x32_bf16(__builtin_bit_cast(bf8, a), __builtin_bit_cast(bf8, b), c, 0, 0, 0);
}
__device__ __forceinline__ v2u trread(LAS unsigned char* p) {
  return __builtin_bit_cast(v2u, __builtin_amdgcn_ds_read_tr16_b64_v4i16((LAS v4s*)p));
}
#define BARRIER() asm volatile("s_waitcnt lgkmcnt(0)\n\ts_barrier" ::: "memory")

template <int IMM> __device__ __forceinline__ void ld_first(v4u& d, int voff, v4u rs, unsigned soff) {
  asm volatile("buffer_load_dwordx4 %0, %1, %2, %3 offen offset:%4 nt" : "=v"(d) : "v"(voff), "s"(rs), "s"(soff), "i"(IMM));
}
template <int IMM> __device__ __forceinline__ void ld_again(v4u& d, int voff, v4u rs, unsigned soff) {
  asm volatile("buffer_load_dwordx4 %0, %1, %2, %3 offen offset:%4 nt" : "+v"(d) : "v"(voff), "s"(rs), "s"(soff), "i"(IMM));
}
__device__ __forceinline__ void ld3_first(v4u (&r)[3], int voff, v4u rs, unsigned so) { ld_first<0>(r[0], voff, rs, so); ld_first<128>(r[1], voff, rs, so); ld_first<256>(r[2], voff, rs, so); }
__device__ __forceinline__ void ld3_again(v4u (&r)[3], int voff, v4u rs, unsigned so) { ld_again<0>(r[0], voff, rs, so); ld_again<128>(r[1], voff, rs, so); ld_again<256>(r[2], voff, rs, so); }
template <int N> __device__ __forceinline__ void wait3(v4u (&r)[3]) { asm volatile("s_waitcnt vmcnt(%3)" : "+v"(r[0]), "+v"(r[1]), "+v"(r[2]) : "i"(N)); }
__device__ __forceinline__ void pin3(v4u (&r)[3]) { asm volatile("" : "+v"(r[0]), "+v"(r[1]), "+v"(r[2])); }
template <int N> __device__ __forceinline__ void waitvm() { asm volatile("s_waitcnt vmcnt(%0)" : : "i"(N) : "memory"); }

template <bool NT> __device__ __forceinline__ void dma16(const void* base, int voff, unsigned lds) {
  unsigned keep;
  if (NT) asm volatile("s_mov_b32 %0, m0\n\ts_mov_b32 m0, %3\n\ts_nop 0\n\tglobal_load_lds_dwordx4 %1, %2 nt\n\ts_mov_b32 m0, %0" : "=&s"(keep) : "v"(voff), "s"(base), "s"(lds) : "memory");
  else asm volatile("s_mov_b32 %0, m0\n\ts_mov_b32 m0, %3\n\ts_nop 0\n\tglobal_load_lds_dwordx4 %1, %2\n\ts_mov_b32 m0, %0" : "=&s"(keep) : "v"(voff), "s"(base), "s"(lds) : "memory");
}

__device__ __forceinline__ void put(__amdgpu_buffer_rsrc_t rs, int off, unsigned a, unsigned b, unsigned tag) {
  __builtin_amdgcn_raw_buffer_store_b128(v4u{a, tag, b, tag}, rs, off, 0, 16);
}
template <int N> __device__ __forceinline__ void gather(__amdgpu_buffer_rsrc_t rs, int off, int stride, bool idle, unsigned tag, unsigned (&a)[N], unsigned (&b)[N]) {
  unsigned todo = idle ? 0u : ((N == 32) ? 0xffffffffu : ((1u << N) - 1u));
#pragma unroll
  for (int k = 0; k < N; ++k) { a[k] = 0u; b[k] = 0u; }
  for (int spin = 0; spin < SPIN_CAP; ++spin) {
    v4u t[N];
#pragma unroll
    for (int k = 0; k < N; ++k) t[k] = __builtin_amdgcn_raw_buffer_load_b128(rs, ((todo >> k) & 1u) ? off + k * stride : OOB, 0, 16);
#pragma unroll
    for (int k = 0; k < N; ++k) {
      const bool hit = ((todo >> k) & 1u) && t[k].y == tag && t[k].w == tag;
      a[k] = hit ? t[k].x : a[k]; b[k] = hit ? t[k].z : b[k];
      todo = hit ? todo & ~(1u << k) : todo;
    }
    if (!__any(todo != 0u)) break;
    __builtin_amdgcn_s_sleep(1);
  }
}

__device__ __forceinline__ float ln_rden(float var) { return 1.0f / bfr(sqrtf(bfr(bfr(var) + 1e-5f))); }
__device__ __forceinline__ float ln_val(float x, float mean_b, float rden) { return bfr(bfr(x - mean_b) * rden); }

struct Soft { float m, l, acc[24]; };
__device__ __forceinline__ float score(const v4u (&kk)[3], const v4u (&q)[3]) {
  float s0 = 0.f, s1 = 0.f;
#pragma unroll
  for (int j = 0; j < 3; ++j) {
    s0 = dot2(kk[j].x, q[j].x, s0); s1 = dot2(kk[j].y, q[j].y, s1);
    s0 = dot2(kk[j].z, q[j].z, s0); s1 = dot2(kk[j].w, q[j].w, s1);
  }
  return s0 + s1;
}
__device__ __forceinline__ float soft(Soft& st, float s) {
  s += dpp<XOR1>(s); s += dpp<XOR2>(s); s += dpp<HMIR>(s);
  s = bfr(bfr(s) * 0.07216878364870322f) * 1.4426950408889634f;
  if (__any(s > st.m)) {
    const float mn = fmaxf(st.m, s), al = __builtin_amdgcn_exp2f(st.m - mn);
    st.l *= al;
#pragma unroll
    for (int k = 0; k < 24; ++k) st.acc[k] *= al;
    st.m = mn;
  }
  const float p = __builtin_amdgcn_exp2f(s - st.m);
  st.l += p;
  return p;
}
__device__ __forceinline__ void pv(Soft& st, float p, const v4u (&vv)[3]) {
#pragma unroll
  for (int j = 0; j < 3; ++j)
#pragma unroll
    for (int e = 0; e < 4; ++e) {
      st.acc[j * 8 + 2 * e] = fmaf(p, bflo(vv[j][e]), st.acc[j * 8 + 2 * e]);
      st.acc[j * 8 + 2 * e + 1] = fmaf(p, bfhi(vv[j][e]), st.acc[j * 8 + 2 * e + 1]);
    }
}

#define STAMP(n)

__global__ void __launch_bounds__(NTHR, 2) decode_block(Args a) {
  __shared__ __attribute__((aligned(16))) unsigned char smem[L_BYTES];
  LAS unsigned char* L = (LAS unsigned char*)smem;
  volatile LAS unsigned* CT = (volatile LAS unsigned*)(L + L_CT);
  volatile LAS unsigned* DYN = (volatile LAS unsigned*)(L + L_DYN);
  const int tid = threadIdx.x, lane = tid & 63, w = __builtin_amdgcn_readfirstlane(tid >> 6);
  const int wg = blockIdx.x, h = wg >> 3, j = wg & 7;
  const int hf = lane >> 5, l5 = lane & 31;
  const __amdgpu_buffer_rsrc_t rs = __builtin_amdgcn_make_buffer_rsrc((void*)a.ws, 0, WS_BYTES, 0x00020000);
  unsigned* ctr = (unsigned*)(a.ws + WS_CTR + h * CTR_STRIDE);
  STAMP(0);

  unsigned epoch;
  {
    const int zero = 0;
    if (w == 0) {
      __builtin_amdgcn_s_setprio(3);
      if (j == 0) asm volatile("global_store_dword %0, %0, %1 sc1" : : "v"(zero), "s"(ctr) : "memory");
    }
    asm volatile("global_load_dword %0, %1, %2 sc1" : "=v"(epoch) : "v"(zero), "s"(a.ws));
  }
  v4u wqr[5], xa, xb; v2u xc;
  {
    const size_t rowoff = (size_t)(80 * j + 10 * w) * (NH * DH * 2) + h * (DH * 2);
    const v4u rw = mkrsrc((const unsigned char*)a.wq + rowoff, (unsigned)((size_t)DM * NH * DH * 2 - rowoff));
    const int vo = l5 < 24 ? hf * (NH * DH * 2) + l5 * 16 : OOB;
#pragma unroll
    for (int i = 0; i < 5; ++i) ld_first<0>(wqr[i], vo, rw, i * 2 * NH * DH * 2);
    const v4u rx = mkrsrc(a.x, 2 * DM * 2);
    const int xo = hf * (DM * 2) + l5 * 40;
    asm volatile("buffer_load_dwordx4 %0, %3, %4, 0 offen\n\tbuffer_load_dwordx4 %1, %3, %4, 0 offen offset:16\n\tbuffer_load_dwordx2 %2, %3, %4, 0 offen offset:32"
                 : "=&v"(xa), "=&v"(xb), "=&v"(xc) : "v"(xo), "s"(rx));
  }
  const v4u rk = mkrsrc((const unsigned char*)a.k + (size_t)h * HEADB, B1OFF + HEADB), rv = mkrsrc((const unsigned char*)a.v + (size_t)h * HEADB, B1OFF + HEADB);
  const int voff = (lane >> 3) * (DH * 2) + (lane & 7) * 16;
  const unsigned sb0 = (unsigned)(S0 * j) * ITEMB, sb1 = B1OFF + (unsigned)(S1 * j - S0) * ITEMB;
  v4u km[3], vm[3], ke[NEXTRA][3], ve[NEXTRA][3];
  const unsigned so0 = w ? sb0 + (unsigned)(w - 1) * ITEMB : END;
  ld3_first(km, voff, rk, so0); ld3_first(vm, voff, rv, so0);
  if (w == 0) {
    for (int k = lane; k < DYNMAX; k += 64) DYN[k] = EMPTY;
    if (lane == 0) CT[0] = (NEXTRA + 1) * NS;
  }
  asm volatile("s_waitcnt vmcnt(6)" : "+v"(wqr[0]), "+v"(wqr[1]), "+v"(wqr[2]), "+v"(wqr[3]), "+v"(wqr[4]), "+v"(xa), "+v"(xb), "+v"(xc), "+v"(epoch));
  ld3_first(ke[0], voff, rk, so0 + NS * ITEMB); ld3_first(ve[0], voff, rv, so0 + NS * ITEMB);
  const unsigned tag = __builtin_amdgcn_readfirstlane(epoch) + 0x9E3779B9u;
  STAMP(1);
  {
    const unsigned xw[10] = {xa.x, xa.y, xa.z, xa.w, xb.x, xb.y, xb.z, xb.w, xc.x, xc.y};
    float s = 0.f;
#pragma unroll
    for (int k = 0; k < 10; ++k) s += bflo(xw[k]) + bfhi(xw[k]);
    s = rowsum16(s); s = sw16add(s, s);
    const float mean = s * (1.0f / DM);
    float vs = 0.f;
#pragma unroll
    for (int k = 0; k < 10; ++k) { const float d0 = bflo(xw[k]) - mean, d1 = bfhi(xw[k]) - mean; vs = fmaf(d0, d0, vs); vs = fmaf(d1, d1, vs); }
    vs = rowsum16(vs); vs = sw16add(vs, vs);
    const float mb = bfr(mean), rd = ln_rden(vs * (1.0f / DM));
    const float mb0 = rdlane(mb, 0), mb1 = rdlane(mb, 32), rd0 = rdlane(rd, 0), rd1 = rdlane(rd, 32);
    const CAS unsigned* x32 = (const CAS unsigned*)a.x + 40 * j + 5 * w;
    float qa[8], qb[8];
#pragma unroll
    for (int e = 0; e < 8; ++e) { qa[e] = 0.f; qb[e] = 0.f; }
#pragma unroll
    for (int i = 0; i < 5; ++i) {
      const unsigned u0 = x32[i], u1 = x32[DM / 2 + i];
      const float n0 = ln_val(hf ? bfhi(u0) : bflo(u0), mb0, rd0), n1 = ln_val(hf ? bfhi(u1) : bflo(u1), mb1, rd1);
#pragma unroll
      for (int e = 0; e < 4; ++e) {
        const float w0 = bflo(wqr[i][e]), w1 = bfhi(wqr[i][e]);
        qa[2 * e] = fmaf(n0, w0, qa[2 * e]); qa[2 * e + 1] = fmaf(n0, w1, qa[2 * e + 1]);
        qb[2 * e] = fmaf(n1, w0, qb[2 * e]); qb[2 * e + 1] = fmaf(n1, w1, qb[2 * e + 1]);
      }
    }
    float r[8];
#pragma unroll
    for (int e = 0; e < 8; ++e) r[e] = sw32add(qa[e], qb[e]);
    if (l5 < 24) {
      LAS v4f* dst = (LAS v4f*)(L + L_QP + ((w * 2 + hf) * DH + l5 * 8) * 4);
      dst[0] = v4f{r[0], r[1], r[2], r[3]}; dst[1] = v4f{r[4], r[5], r[6], r[7]};
    }
  }
  if (w == 0) waitvm<0>();
  BARRIER();
  STAMP(2);

  if (w == 0) {
    {
      const LAS float* QP = (const LAS float*)(L + L_QP);
#pragma unroll
      for (int k = 0; k < 3; ++k) {
        float s0 = 0.f, s1 = 0.f;
#pragma unroll
        for (int ww = 0; ww < NW; ++ww) { s0 += QP[(ww * 2) * DH + lane + 64 * k]; s1 += QP[(ww * 2 + 1) * DH + lane + 64 * k]; }
        put(rs, WS_Q + ((h * 8 + j) * DH + lane + 64 * k) * 16, fbits(s0), fbits(s1), tag);
      }
      unsigned ga[24], gb[24];
      gather<24>(rs, WS_Q + (h * 8 * DH + lane) * 16, 64 * 16, false, tag, ga, gb);
      LAS unsigned short* QS = (LAS unsigned short*)(L + L_QS);
#pragma unroll
      for (int k = 0; k < 3; ++k) {
        float s0 = 0.f, s1 = 0.f;
#pragma unroll
        for (int p = 0; p < 8; ++p) { s0 += ubits(ga[p * 3 + k]); s1 += ubits(gb[p * 3 + k]); }
        const unsigned u = pk(s0, s1);
        QS[lane + 64 * k] = (unsigned short)u; QS[DH + lane + 64 * k] = (unsigned short)(u >> 16);
      }
    }
    BARRIER();
    STAMP(3);
    {
      const unsigned lb = (unsigned)(uintptr_t)L;
      const unsigned char* wo = (const unsigned char*)a.wo + (size_t)(h * DH + 24 * j) * (DM * 2);
#pragma unroll 1
      for (int k = 0; k < 30; ++k) dma16<true>(wo + k * 1024, lane * 16, lb + L_WO + k * 1024);
      if (wg < NFFN) {
        const int f = j * 24 + h;
        const unsigned char* wgp = (const unsigned char*)a.wg + f * 16;
        const unsigned char* wup = (const unsigned char*)a.wu + f * 16;
        const unsigned char* wdp = (const unsigned char*)a.wd + (size_t)f * 8 * (DM * 2);
#pragma unroll 1
        for (int k = 0; k < 10; ++k) {
          dma16<false>(wgp + (size_t)k * 64 * FF * 2, lane * (FF * 2), lb + L_G + k * 1024);
          dma16<false>(wup + (size_t)k * 64 * FF * 2, lane * (FF * 2), lb + L_U + k * 1024);
          dma16<true>(wdp + k * 1024, lane * 16, lb + L_WD + k * 1024);
        }
      }
    }
    int filled = 0;
    for (int spin = 0; spin < 16 * SPIN_CAP; ++spin) {
      int need = (int)__builtin_amdgcn_readfirstlane(CT[0]) + LEAD - (STAT + filled);
      if (need > 0) {
        need = need > 32 ? 32 : need;
        unsigned old = 0;
        if (lane == 0) old = __hip_atomic_fetch_add(ctr, (unsigned)need, __ATOMIC_RELAXED, __HIP_MEMORY_SCOPE_AGENT);
        old = __builtin_amdgcn_readfirstlane(old);
        const bool done = old + need >= (unsigned)POOL;
        if (lane < (done ? need + NW : need)) DYN[filled + lane] = (lane < need && old + lane < (unsigned)POOL) ? B1OFF + (8 * S1 + old + lane) * ITEMB : END;
        filled += need;
        if (done) break;
      } else __builtin_amdgcn_s_sleep(2);
    }
    waitvm<0>();
    __builtin_amdgcn_s_setprio(0);
  } else {
    waitvm<6>();
    ld3_first(ke[1], voff, rk, so0 + 2 * NS * ITEMB); waitvm<6>();
    ld3_first(ve[1], voff, rv, so0 + 2 * NS * ITEMB); waitvm<6>();
    ld3_first(ke[2], voff, rk, so0 + 3 * NS * ITEMB); waitvm<6>();
    ld3_first(ve[2], voff, rv, so0 + 3 * NS * ITEMB);
    BARRIER();
    STAMP(3);
    v4u q[3];
#pragma unroll
    for (int jj = 0; jj < 3; ++jj) q[jj] = *(const LAS v4u*)(L + L_QS + ((lane & 7) + 8 * jj) * 16);
    Soft st;
    st.m = M_INIT; st.l = 0.f;
#pragma unroll
    for (int k = 0; k < 24; ++k) st.acc[k] = 0.f;
    auto step = [&]() -> unsigned {
      unsigned t = 0;
      if (lane == 0) t = __hip_atomic_fetch_add((LAS unsigned*)(L + L_CT), 1u, __ATOMIC_RELAXED, __HIP_MEMORY_SCOPE_WORKGROUP);
      t = __builtin_amdgcn_readfirstlane(t);
      unsigned nxt;
      if (t < (unsigned)STAT) nxt = (t < (unsigned)S0 ? sb0 : sb1) + t * ITEMB;
      else {
        int spin = 0;
        while ((nxt = __builtin_amdgcn_readfirstlane(DYN[t - STAT])) == EMPTY) {
          __builtin_amdgcn_s_sleep(1);
          if (++spin > SPIN_CAP) { nxt = END; break; }
        }
      }
      wait3<3>(km);
      float s = score(km, q);
      asm volatile("" : "+v"(s));
      ld3_again(km, voff, rk, nxt);
      const float p = soft(st, s);
      wait3<3>(vm);
      pv(st, p, vm);
      __builtin_amdgcn_sched_barrier(0);
      ld3_again(vm, voff, rv, nxt);
      return nxt;
    };
    unsigned cur = step();
#pragma unroll
    for (int e = 0; e < NEXTRA; ++e) {
      pin3(ke[e]); pin3(ve[e]);
      const float pe = soft(st, score(ke[e], q));
      pv(st, pe, ve[e]);
    }
#pragma unroll 1
    for (int b = 0; b < 2; ++b) {
      while (cur != END && (cur >= B1OFF) == (b == 1)) cur = step();
      float M = fmaxf(st.m, dpp<ROR8>(st.m)); M = sw32max(sw16max(M));
      const float sc = __builtin_amdgcn_exp2f(st.m - M);
      float l = st.l * sc; l += dpp<ROR8>(l); l = sw16add(l, l); l = sw32add(l, l);
      float t1[12], t2[6];
#pragma unroll
      for (int k = 0; k < 12; ++k) t1[k] = sw16add(st.acc[k] * sc, st.acc[k + 12] * sc);
#pragma unroll
      for (int k = 0; k < 6; ++k) { t2[k] = sw32add(t1[k], t1[k + 6]); t2[k] += dpp<ROR8>(t2[k]); }
      LAS float* rec = (LAS float*)(L + L_REC) + ((w - 1) * 2 + b) * RECF;
      if (!(lane & 8)) {
        const int a0 = 6 * hf + 12 * ((lane >> 4) & 1);
#pragma unroll
        for (int k = 0; k < 6; k += 2) { const int ai = a0 + k; *(LAS v2f*)(rec + 4 + ((lane & 7) + 8 * (ai >> 3)) * 8 + (ai & 7)) = v2f{t2[k], t2[k + 1]}; }
      }
      if (lane == 0) *(LAS v2f*)rec = v2f{M, l};
#pragma unroll
      for (int jj = 0; jj < 3; ++jj) q[jj] = *(const LAS v4u*)(L + L_QS + DH * 2 + ((lane & 7) + 8 * jj) * 16);
      st.m = M_INIT; st.l = 0.f;
#pragma unroll
      for (int k = 0; k < 24; ++k) st.acc[k] = 0.f;
    }
    wait3<0>(km); wait3<0>(vm);
  }
  STAMP(4);
  const CAS unsigned* xr = (const CAS unsigned*)a.x + 2 * (wg < NRED ? wg : 0);
  unsigned xr0 = xr[0], xr1 = xr[1], xr2 = xr[DM / 2], xr3 = xr[DM / 2 + 1];
  asm volatile("" : "+s"(xr0), "+s"(xr1), "+s"(xr2), "+s"(xr3));
  BARRIER();
  STAMP(5);

  if (tid < DH) {
    const LAS float* rec = (const LAS float*)(L + L_REC);
    float M[2], Ls[2], o[2];
#pragma unroll
    for (int b = 0; b < 2; ++b) {
      v2f ml[NS]; float av[NS];
#pragma unroll
      for (int ww = 0; ww < NS; ++ww) { ml[ww] = *(const LAS v2f*)(rec + (ww * 2 + b) * RECF); av[ww] = rec[(ww * 2 + b) * RECF + 4 + tid]; }
      float mx = ml[0].x;
#pragma unroll
      for (int ww = 1; ww < NS; ++ww) mx = fmaxf(mx, ml[ww].x);
      float ls = 0.f, os = 0.f;
#pragma unroll
      for (int ww = 0; ww < NS; ++ww) { const float e = __builtin_amdgcn_exp2f(ml[ww].x - mx); ls = fmaf(e, ml[ww].y, ls); os = fmaf(e, av[ww], os); }
      M[b] = mx; Ls[b] = ls; o[b] = os;
    }
    const int cons = tid / 24, sl = tid - cons * 24, base = WS_P + ((h * 8 + cons) * 8 + j) * 512;
    put(rs, base + sl * 16, fbits(o[0]), fbits(o[1]), tag);
    put(rs, sl < 2 ? base + (24 + sl) * 16 : OOB, fbits(sl == 0 ? M[0] : Ls[0]), fbits(sl == 0 ? M[1] : Ls[1]), tag);
  }
  const int g4 = lane >> 4, c16 = lane & 15, tq = c16 >> 2, tp = c16 & 3;
  v4u bw[5];
#pragma unroll
  for (int tt = 0; tt < 5; ++tt) {
    LAS unsigned char* p = L + L_WO + ((g4 < 3 ? g4 : 0) * 8 + tq) * 1280 + (16 * (w * 5 + tt) + 4 * tp) * 2;
    const v2u lo = trread(p), hi = trread(p + 4 * 1280);
    bw[tt] = g4 < 3 ? v4u{lo.x, lo.y, hi.x, hi.y} : v4u{0u, 0u, 0u, 0u};
  }
  const bool roleB = (w == 4) && wg < NFFN, roleA = (w == 5) && wg < NRED;
  v4u bgu[20], bdn[10];
  if (roleB) {
#pragma unroll
    for (int s = 0; s < 20; ++s) {
      LAS unsigned char* p = L + (tp < 2 ? L_G : L_U) + (32 * s + 8 * g4 + tq) * 16 + (tp & 1) * 8;
      const v2u lo = trread(p), hi = trread(p + 64);
      bgu[s] = v4u{lo.x, lo.y, hi.x, hi.y};
    }
#pragma unroll
    for (int T = 0; T < 10; ++T) {
      LAS unsigned char* p = L + L_WD + tq * 1280 + (64 * T + 16 * g4 + 4 * tp) * 2;
      const v2u lo = trread(p), hi = trread(p + 4 * 1280);
      bdn[T] = v4u{lo.x, lo.y, hi.x, hi.y};
    }
  }
  if (w == 3) {
    unsigned ga[4], gb[4];
    gather<4>(rs, WS_P + ((h * 8 + j) * 8 + hf) * 512 + l5 * 16, 1024, l5 >= 26, tag, ga, gb);
    STAMP(6);
    float m0[4], m1[4], l0[4], l1[4];
#pragma unroll
    for (int u = 0; u < 4; ++u) {
      m0[u] = ubits(__shfl(ga[u], (lane & 32) + 24)); m1[u] = ubits(__shfl(gb[u], (lane & 32) + 24));
      l0[u] = ubits(__shfl(ga[u], (lane & 32) + 25)); l1[u] = ubits(__shfl(gb[u], (lane & 32) + 25));
    }
    const float M0 = sw32max(fmaxf(fmaxf(m0[0], m0[1]), fmaxf(m0[2], m0[3]))), M1 = sw32max(fmaxf(fmaxf(m1[0], m1[1]), fmaxf(m1[2], m1[3])));
    float o0 = 0.f, o1 = 0.f, s0 = 0.f, s1 = 0.f;
#pragma unroll
    for (int u = 0; u < 4; ++u) {
      const float e0 = __builtin_amdgcn_exp2f(m0[u] - M0), e1 = __builtin_amdgcn_exp2f(m1[u] - M1);
      o0 = fmaf(e0, ubits(ga[u]), o0); o1 = fmaf(e1, ubits(gb[u]), o1);
      s0 = fmaf(e0, l0[u], s0); s1 = fmaf(e1, l1[u], s1);
    }
    const float o = sw32add(o0, o1), s = sw32add(s0, s1);
    ((LAS unsigned short*)(L + L_OB))[lane] = l5 < 24 ? (unsigned short)pk(o / s, 0.f) : (unsigned short)0;
  }
  BARRIER();
  STAMP(7);
  {
    const v4u ao = *(const LAS v4u*)(L + L_OB + (lane & 1) * 64 + g4 * 16);
#pragma unroll
    for (int tt = 0; tt < 5; ++tt) {
      const v4f d = mfma(ao, bw[tt], v4f{0.f, 0.f, 0.f, 0.f});
      const int n = 16 * (w * 5 + tt) + c16;
      put(rs, lane < 16 ? WS_Y + ((n >> 2) * NWG + wg) * 64 + (n & 3) * 16 : OOB, fbits(d[0]), fbits(d[1]), tag);
    }
  }
  STAMP(8);

  if (roleA) {
    float hv;
    {
      unsigned ga[16], gb[16];
      gather<16>(rs, WS_Y + wg * (NWG * 64) + lane * 16, 1024, false, tag, ga, gb);
      STAMP(9);
      float s0 = 0.f, s1 = 0.f;
#pragma unroll
      for (int u = 0; u < 16; ++u) { s0 += ubits(ga[u]); s1 += ubits(gb[u]); }
      s0 += dpp<ROR4>(s0); s1 += dpp<ROR4>(s1); s0 += dpp<ROR8>(s0); s1 += dpp<ROR8>(s1);
      float s = sw16add(s0, s1); s = sw32add(s, s);
      const unsigned xw = (g4 & 1) ? ((lane & 2) ? xr3 : xr2) : ((lane & 2) ? xr1 : xr0);
      hv = bfr(((lane & 1) ? bfhi(xw) : bflo(xw)) + bfr(s));
      const unsigned hp = (fbits(hv) >> 16) | (fbits(dpp<XOR1>(hv)) & 0xffff0000u);
      const unsigned h00 = __builtin_amdgcn_readlane(hp, 0), h02 = __builtin_amdgcn_readlane(hp, 2), h10 = __builtin_amdgcn_readlane(hp, 16), h12 = __builtin_amdgcn_readlane(hp, 18);
      put(rs, lane < 2 * NREP ? WS_H + ((lane >> 1) * 320 + 2 * wg + (lane & 1)) * 16 : OOB, (lane & 1) ? h02 : h00, (lane & 1) ? h12 : h10, tag);
    }
    STAMP(10);
    {
      unsigned ga[12], gb[12];
      gather<12>(rs, WS_Z + wg * (NFFN * 64) + lane * 16, 1024, false, tag, ga, gb);
      STAMP(11);
      float s0 = 0.f, s1 = 0.f;
#pragma unroll
      for (int u = 0; u < 12; ++u) { s0 += ubits(ga[u]); s1 += ubits(gb[u]); }
      s0 += dpp<ROR4>(s0); s1 += dpp<ROR4>(s1); s0 += dpp<ROR8>(s0); s1 += dpp<ROR8>(s1);
      float s = sw16add(s0, s1); s = sw32add(s, s);
      const unsigned o = pk(hv + bfr(s), 0.f);
      if (lane < 32 && c16 < 4) a.out[g4 * DM + 4 * wg + c16] = (unsigned short)o;
      if (wg == 0 && lane == 0) *(unsigned*)a.ws = tag;
    }
    STAMP(12);
  } else if (roleB) {
    const int f = j * 24 + h;
    unsigned ga[5], gb[5];
    gather<5>(rs, WS_H + ((wg & (NREP - 1)) * 320 + lane) * 16, 1024, false, tag, ga, gb);
    STAMP(9);
    float s0 = 0.f, s1 = 0.f;
#pragma unroll
    for (int u = 0; u < 5; ++u) { s0 += bflo(ga[u]) + bfhi(ga[u]); s1 += bflo(gb[u]) + bfhi(gb[u]); }
    float s = sw16add(rowsum16(s0), rowsum16(s1)); s = sw32add(s, s) * (1.0f / DM);
    const float mean0 = rdlane(s, 0), mean1 = rdlane(s, 16);
    s0 = 0.f; s1 = 0.f;
#pragma unroll
    for (int u = 0; u < 5; ++u) {
      const float a0 = bflo(ga[u]) - mean0, a1 = bfhi(ga[u]) - mean0, b0 = bflo(gb[u]) - mean1, b1 = bfhi(gb[u]) - mean1;
      s0 = fmaf(a0, a0, s0); s0 = fmaf(a1, a1, s0); s1 = fmaf(b0, b0, s1); s1 = fmaf(b1, b1, s1);
    }
    s = sw16add(rowsum16(s0), rowsum16(s1)); s = ln_rden(sw32add(s, s) * (1.0f / DM));
    const float rd0 = rdlane(s, 0), rd1 = rdlane(s, 16), mb0 = bfr(mean0), mb1 = bfr(mean1);
    LAS unsigned* LN = (LAS unsigned*)(L + L_LN);
#pragma unroll
    for (int u = 0; u < 5; ++u) {
      LN[lane + 64 * u] = pk(bfr(bflo(ga[u]) - mb0) * rd0, bfr(bfhi(ga[u]) - mb0) * rd0);
      LN[DM / 2 + lane + 64 * u] = pk(bfr(bflo(gb[u]) - mb1) * rd1, bfr(bfhi(gb[u]) - mb1) * rd1);
    }
    asm volatile("s_waitcnt lgkmcnt(0)" ::: "memory");
    v4f d0 = {0.f, 0.f, 0.f, 0.f}, d1 = {0.f, 0.f, 0.f, 0.f};
#pragma unroll
    for (int st = 0; st < 20; st += 2) {
      const LAS unsigned char* p = L + L_LN + (lane & 1) * (DM * 2) + (32 * st + 8 * g4) * 2;
      d0 = mfma(*(const LAS v4u*)p, bgu[st], d0);
      d1 = mfma(*(const LAS v4u*)(p + 64), bgu[st + 1], d1);
    }
    float act[2];
#pragma unroll
    for (int b = 0; b < 2; ++b) {
      const float gv = bfr(d0[b] + d1[b]), uv = dpp<ROR8>(gv);
      const float y = 0.7978845608028654f * (gv + 0.044715f * gv * gv * gv);
      const float th = 1.0f - 2.0f / (__expf(2.0f * y) + 1.0f);
      act[b] = bfr(bfr(0.5f * gv * (1.0f + th)) * uv);
    }
    const unsigned p0 = (fbits(act[0]) >> 16) | (fbits(dpp<XOR1>(act[0])) & 0xffff0000u), p1 = (fbits(act[1]) >> 16) | (fbits(dpp<XOR1>(act[1])) & 0xffff0000u);
    v4u a2;
#pragma unroll
    for (int e = 0; e < 4; ++e) {
      const unsigned u0 = __builtin_amdgcn_readlane(p0, 2 * e), u1 = __builtin_amdgcn_readlane(p1, 2 * e);
      a2[e] = ((c16 >> 1) == g4) ? ((lane & 1) ? u1 : u0) : 0u;
    }
#pragma unroll
    for (int T = 0; T < 10; ++T) {
      const v4f d = mfma(a2, bdn[T], v4f{0.f, 0.f, 0.f, 0.f});
      const int n = 64 * T + 32 * g4 + c16, off = WS_Z + ((n >> 2) * NFFN + f) * 64 + (n & 3) * 16;
      put(rs, lane < 32 ? off : OOB, fbits(d[0]), fbits(d[1]), tag);
      put(rs, lane < 32 ? off + 4 * NFFN * 64 : OOB, fbits(d[2]), fbits(d[3]), tag);
    }
    STAMP(10);
  }
}
}

extern "C" void kernel_launch(void* const* d_in, const int* in_sizes, int n_in, void* d_out, int out_size, void* d_ws, size_t ws_size, hipStream_t stream) {
  Args a;
  a.x = (const unsigned short*)d_in[0]; a.k = (const unsigned short*)d_in[1]; a.v = (const unsigned short*)d_in[2];
  a.wq = (const unsigned short*)d_in[3]; a.wo = (const unsigned short*)d_in[4];
  a.wg = (const unsigned short*)d_in[6]; a.wu = (const unsigned short*)d_in[7]; a.wd = (const unsigned short*)d_in[8];
  a.out = (unsigned short*)d_out; a.ws = (unsigned char*)d_ws;
  hipLaunchKernelGGL(decode_block, dim3(NWG), dim3(NTHR), 0, stream, a);
}
```

```cpp
#include <hip/hip_runtime.h>
#include <hip/hip_bf16.h>
#include <stdint.h>

#define PACE 3
#define NEX 5
#define POLLSLEEP 1
#define REWARM 1
#define STAT1 192
#define LEADN 8
#define WSLEEP 1
#define GPOS 0
#define DPOS 0
#define FPOS 0
#define QDELAY 10
#define EDELAY 10
#define ZDELAY 0
#define MIMIC_SCORE 1

namespace {
typedef unsigned v4u __attribute__((ext_vector_type(4)));
typedef unsigned v2u __attribute__((ext_vector_type(2)));
typedef float v4f __attribute__((ext_vector_type(4)));
typedef float v2f __attribute__((ext_vector_type(2)));
typedef short v4s __attribute__((ext_vector_type(4)));
typedef short v8s __attribute__((ext_vector_type(8)));
typedef __bf16 bf2 __attribute__((ext_vector_type(2)));
typedef __bf16 bf8 __attribute__((ext_vector_type(8)));
#define LAS __attribute__((address_space(3)))
#define CAS __attribute__((address_space(4)))

constexpr int DM = 640, NH = 32, SK = 16384, DH = 192, FF = 1536;
constexpr int NWG = 256, NW = 8, NS = NW - 1, NTHR = NW * 64;
constexpr unsigned ITEMB = 8 * DH * 2;
constexpr unsigned HEADB = (unsigned)SK * DH * 2, B1OFF = NH * HEADB;
constexpr int IPH = SK / 8;
constexpr int S0 = IPH / 8, S1 = STAT1, STAT = S0 + S1;
constexpr int POOL = IPH - 8 * S1, LEAD = LEADN, NEXTRA = NEX;
constexpr unsigned END = 0xF0000000u, EMPTY = 0xFFFFFFFFu;
constexpr int NRED = DM / 4, NFFN = FF / 8;
constexpr int SPIN_CAP = 1 << 17;
constexpr float M_INIT = -3.0e38f;

constexpr int WS_CTR = 4096, CTR_STRIDE = 4352;
constexpr int WS_F = 0x24000, WS_D = 0x28000, WS_G = 0x30000, WS_Q = 0x40000, WS_P = 0x100000, PBLK = 3200, WS_Y = 0x200000, WS_E = 0x280000, WS_Z = 0x400000, WS_BYTES = 0x600000;
constexpr int OOB = (int)0x80000000u;
static_assert(WS_CTR + NH * CTR_STRIDE <= WS_F && WS_F + 24 * 128 <= WS_D && WS_D + NWG * 128 <= WS_G && WS_G + NWG * 128 <= WS_Q && WS_Q + NWG * DH * 16 <= WS_P && WS_P + NWG * PBLK <= WS_Y, "ws map");
static_assert(WS_Y + NWG * 80 * 16 <= WS_E && WS_E + NFFN * 5120 <= WS_Z && WS_Z + NRED * NFFN * 64 <= WS_BYTES, "ws map");

constexpr int L_WO = 0, L_G = L_WO + DH * 160, L_WD = L_G + 24 * 1024, L_QP = L_WD + 8 * 1280;
constexpr int L_QS = L_QP + NW * 2 * DH * 4, RECF = DH + 4, L_REC = L_QS + 2 * DH * 2, L_O = L_REC + NS * 2 * RECF * 4, L_HS = L_O + 2 * DH * 2;
constexpr int L_CT = L_HS + 3 * 192, DYNMAX = POOL + 64, L_DYN = L_CT + 64, L_PF = L_DYN + DYNMAX * 4, L_BYTES = L_PF + NW * 1024;
static_assert(L_BYTES > 81920 && L_BYTES <= 160 * 1024 && L_REC % 16 == 0 && L_O % 16 == 0 && L_HS % 16 == 0 && L_PF % 16 == 0, "one workgroup per CU");

struct Args {
  const unsigned short *x, *k, *v, *wq, *wo, *wg, *wu, *wd;
  unsigned short* out;
  unsigned char* ws;
};

__device__ __forceinline__ float dot2(unsigned a, unsigned b, float c) {
  return __builtin_amdgcn_fdot2_f32_bf16(__builtin_bit_cast(bf2, a), __builtin_bit_cast(bf2, b), c, false);
}
template <int CTRL> __device__ __forceinline__ float dpp(float x) {
  return __builtin_bit_cast(float, __builtin_amdgcn_mov_dpp(__builtin_bit_cast(int, x), CTRL, 0xf, 0xf, true));
}
template <int CTRL> __device__ __forceinline__ unsigned dppu(unsigned x) { return (unsigned)__builtin_amdgcn_mov_dpp((int)x, CTRL, 0xf, 0xf, true); }
constexpr int BC = 0x150;
constexpr int XOR1 = 0xB1, XOR2 = 0x4E, HMIR = 0x141, ROR4 = 0x124, ROR8 = 0x128;
__device__ __forceinline__ float bflo(unsigned x) { return __builtin_bit_cast(float, x << 16); }
__device__ __forceinline__ float bfhi(unsigned x) { return __builtin_bit_cast(float, x & 0xffff0000u); }
__device__ __forceinline__ unsigned pk(float lo, float hi) {
  const bf2 r = {(__bf16)lo, (__bf16)hi};
  return __builtin_bit_cast(unsigned, r);
}
__device__ __forceinline__ float bfr(float x) { return (float)(__bf16)x; }
__device__ __forceinline__ unsigned fbits(float x) { return __builtin_bit_cast(unsigned, x); }
__device__ __forceinline__ float ubits(unsigned x) { return __builtin_bit_cast(float, x); }
__device__ __forceinline__ float sw16add(float a, float b) {
  const auto s = __builtin_amdgcn_permlane16_swap(fbits(a), fbits(b), false, false);
  return ubits(s[0]) + ubits(s[1]);
}
__device__ __forceinline__ float sw32add(float a, float b) {
  const auto s = __builtin_amdgcn_permlane32_swap(fbits(a), fbits(b), false, false);
  return ubits(s[0]) + ubits(s[1]);
}
__device__ __forceinline__ float sw16max(float a) {
  const auto s = __builtin_amdgcn_permlane16_swap(fbits(a), fbits(a), false, false);
  return fmaxf(ubits(s[0]), ubits(s[1]));
}
__device__ __forceinline__ float sw32max(float a) {
  const auto s = __builtin_amdgcn_permlane32_swap(fbits(a), fbits(a), false, false);
  return fmaxf(ubits(s[0]), ubits(s[1]));
}
__device__ __forceinline__ float rowsum16(float s) { s += dpp<XOR1>(s); s += dpp<XOR2>(s); s += dpp<HMIR>(s); s += dpp<ROR8>(s); return s; }
__device__ __forceinline__ float rdlane(float x, int l) { return ubits(__builtin_amdgcn_readlane(fbits(x), l)); }
__device__ __forceinline__ v4u mkrsrc(const void* p, unsigned bytes) {
  const unsigned long long a = (unsigned long long)p;
  return v4u{(unsigned)a, (unsigned)(a >> 32) & 0xffffu, bytes, 0x00020000u};
}
__device__ __forceinline__ v4f mfma(v4u a, v4u b, v4f c) {
  return __builtin_amdgcn_mfma_f32_16x16x32_bf16(__builtin_bit_cast(bf8, a), __builtin_bit_cast(bf8, b), c, 0, 0, 0);
}
__device__ __forceinline__ v2u trread(LAS unsigned char* p) {
  return __builtin_bit_cast(v2u, __builtin_amdgcn_ds_read_tr16_b64_v4i16((LAS v4s*)p));
}
#define BARRIER() asm volatile("s_waitcnt lgkmcnt(0)\n\ts_barrier" ::: "memory")

#define LD3 "s_nop 4\n\tbuffer_load_dwordx4 %0, %3, %4, %5 offen nt\n\tbuffer_load_dwordx4 %1, %3, %4, %5 offen offset:128 nt\n\tbuffer_load_dwordx4 %2, %3, %4, %5 offen offset:256 nt"
__device__ __forceinline__ void ld_first(v4u& d, int voff, v4u rs, unsigned soff) {
  asm volatile("s_nop 4\n\tbuffer_load_dwordx4 %0, %1, %2, %3 offen nt" : "=v"(d) : "v"(voff), "s"(rs), "s"(soff));
}
__device__ __forceinline__ void ld3_first(v4u (&r)[3], int voff, v4u rs, unsigned so) { asm volatile(LD3 : "=&v"(r[0]), "=&v"(r[1]), "=&v"(r[2]) : "v"(voff), "s"(rs), "s"(so)); }
__device__ __forceinline__ void ld3_again(v4u (&r)[3], int voff, v4u rs, unsigned so) { asm volatile(LD3 : "+v"(r[0]), "+v"(r[1]), "+v"(r[2]) : "v"(voff), "s"(rs), "s"(so)); }
template <int N> __device__ __forceinline__ void wait3(v4u (&r)[3]) { asm volatile("s_waitcnt vmcnt(%3)" : "+v"(r[0]), "+v"(r[1]), "+v"(r[2]) : "i"(N)); }
__device__ __forceinline__ void pin3(v4u (&r)[3]) { asm volatile("" : "+v"(r[0]), "+v"(r[1]), "+v"(r[2])); }
template <int N> __device__ __forceinline__ void waitvm() { asm volatile("s_waitcnt vmcnt(%0)" : : "i"(N) : "memory"); }

template <bool NT> __device__ __forceinline__ void dma16(const void* base, int voff, unsigned lds) {
  unsigned keep;
  if (NT) asm volatile("s_mov_b32 %0, m0\n\ts_mov_b32 m0, %3\n\ts_nop 2\n\tglobal_load_lds_dwordx4 %1, %2 nt\n\ts_mov_b32 m0, %0" : "=&s"(keep) : "v"(voff), "s"(base), "s"(lds) : "memory");
  else asm volatile("s_mov_b32 %0, m0\n\ts_mov_b32 m0, %3\n\ts_nop 2\n\tglobal_load_lds_dwordx4 %1, %2\n\ts_mov_b32 m0, %0" : "=&s"(keep) : "v"(voff), "s"(base), "s"(lds) : "memory");
}

__device__ __forceinline__ void put(__amdgpu_buffer_rsrc_t rs, int off, unsigned a, unsigned b, unsigned tag) {
  __builtin_amdgcn_raw_buffer_store_b128(v4u{a, tag, b, tag}, rs, off, 0, 16);
}
#define STAMP(n)
#define SSTAMP(n)
#define STAMP_INIT()
#define STAMP_DUMP()
#define GDBG
#define GSTAMP()
#define GARG(n)
template <int N> __device__ __forceinline__ void gather(__amdgpu_buffer_rsrc_t rs, int off, unsigned tag, v4u (&t)[N] GDBG) {
  bool done = false;
#pragma unroll
  for (int k = 0; k < N; ++k) t[k] = v4u{0u, 0u, 0u, 0u};
  for (int spin = 0; spin < SPIN_CAP; ++spin) {
    if (!done) {
#pragma unroll
      for (int k = 0; k < N; ++k) t[k] = __builtin_amdgcn_raw_buffer_load_b128(rs, off + (k & 3) * 1024, (k >> 2) * 4096, 16);
      unsigned mn = tag, mx = tag;
#pragma unroll
      for (int k = 0; k < N; ++k) { mn = min(mn, min(t[k].y, t[k].w)); mx = max(mx, max(t[k].y, t[k].w)); }
      done = mn == mx;
    }
    GSTAMP();
    if (__all(done)) break;
    asm volatile("" ::: "memory");
    __builtin_amdgcn_s_sleep(POLLSLEEP);
  }
}

template <int N> __device__ __forceinline__ void watch(__amdgpu_buffer_rsrc_t rs, int off, int stride, int lane, unsigned tag, volatile LAS unsigned* mask) {
  bool seen = lane >= N;
  for (int spin = 0; spin < SPIN_CAP; ++spin) {
    if (!seen) seen = __builtin_amdgcn_raw_buffer_load_b32(rs, off + lane * stride, 0, 16) == tag;
    const unsigned long long m = __ballot(seen);
    if (lane == 0) *mask = (unsigned)m;
    if (m == ~0ull) break;
    asm volatile("" ::: "memory");
    __builtin_amdgcn_s_sleep(WSLEEP);
  }
  if (lane == 0) *mask = ~0u;
}
#define hint(p, D) do { for (int sp_ = 0; sp_ < SPIN_CAP && *(p) == 0u; ++sp_) __builtin_amdgcn_s_sleep(1); __builtin_amdgcn_s_sleep(D); } while (0)

__device__ __forceinline__ float ln_rden(float var) { return __builtin_amdgcn_rcpf(bfr(__builtin_amdgcn_sqrtf(bfr(bfr(var) + 1e-5f)))); }
__device__ __forceinline__ float ln_val(float x, float mean_b, float rden) { return bfr(bfr(x - mean_b) * rden); }

struct Soft { float m, l, acc[24]; };
__device__ __forceinline__ float score(const v4u (&kk)[3], const v4u (&q)[3]) {
  float s0 = 0.f, s1 = 0.f;
#pragma unroll
  for (int j = 0; j < 3; ++j) {
    s0 = dot2(kk[j].x, q[j].x, s0); s1 = dot2(kk[j].y, q[j].y, s1);
    s0 = dot2(kk[j].z, q[j].z, s0); s1 = dot2(kk[j].w, q[j].w, s1);
  }
  return s0 + s1;
}
__device__ __forceinline__ float soft(Soft& st, float s) {
  s += dpp<XOR1>(s); s += dpp<XOR2>(s); s += dpp<HMIR>(s);
  s = bfr(bfr(s) * 0.07216878364870322f) * 1.4426950408889634f;
  if (__any(s > st.m)) {
    const float mn = fmaxf(st.m, s), al = __builtin_amdgcn_exp2f(st.m - mn);
    st.l *= al;
#pragma unroll
    for (int k = 0; k < 24; ++k) st.acc[k] *= al;
    st.m = mn;
  }
  const float p = __builtin_amdgcn_exp2f(s - st.m);
  st.l += p;
  return p;
}
__device__ __forceinline__ void pv(Soft& st, float p, const v4u (&vv)[3]) {
#pragma unroll
  for (int j = 0; j < 3; ++j)
#pragma unroll
    for (int e = 0; e < 4; ++e) {
      st.acc[j * 8 + 2 * e] = fmaf(p, bflo(vv[j][e]), st.acc[j * 8 + 2 * e]);
      st.acc[j * 8 + 2 * e + 1] = fmaf(p, bfhi(vv[j][e]), st.acc[j * 8 + 2 * e + 1]);
    }
}

__global__ void __launch_bounds__(NTHR, 2) decode_block(Args a) {
  __shared__ __attribute__((aligned(16))) unsigned char smem[L_BYTES];
  LAS unsigned char* L = (LAS unsigned char*)smem;
  volatile LAS unsigned* CT = (volatile LAS unsigned*)(L + L_CT);
  volatile LAS unsigned* DYN = (volatile LAS unsigned*)(L + L_DYN);
  const int tid = threadIdx.x, lane = tid & 63, w = __builtin_amdgcn_readfirstlane(tid >> 6);
  const int wg = blockIdx.x, h = wg >> 3, j = wg & 7;
  const int hf = lane >> 5, l5 = lane & 31;
  const bool ffn = ((h + j) & 3) != 3;
  const int cg = 3 * (h >> 2) + (h & 3) - ((h & 3) > ((3 - j) & 3) ? 1 : 0), f = 8 * cg + j;
  const __amdgpu_buffer_rsrc_t rs = __builtin_amdgcn_make_buffer_rsrc((void*)a.ws, 0, WS_BYTES, 0x00020000);
  unsigned* ctr = (unsigned*)(a.ws + WS_CTR + h * CTR_STRIDE);
  STAMP_INIT();
  STAMP(0);

  unsigned long long pc; unsigned len;
  asm volatile("s_getpc_b64 %0\n.Ldb_here:\n\ts_mov_b32 %1, .Ldb_end-.Ldb_here" : "=s"(pc), "=s"(len));
  const v4u rc = mkrsrc((const void*)pc, len);
  {
    unsigned keep;
    const int vo = w * 1024 + lane * 16;
    asm volatile("s_mov_b32 %0, m0\n\ts_mov_b32 m0, %3\n\ts_nop 2\n\tbuffer_load_dwordx4 %1, %2, 0 offen lds\n\tbuffer_load_dwordx4 %1, %2, %4 offen lds\n\t"
                 "buffer_load_dwordx4 %1, %2, %5 offen lds\n\tbuffer_load_dwordx4 %1, %2, %6 offen lds\n\ts_mov_b32 m0, %0"
                 : "=&s"(keep) : "v"(vo), "s"(rc), "s"((unsigned)(uintptr_t)L + L_PF + w * 1024), "s"(8192), "s"(16384), "s"(24576) : "memory");
  }
  unsigned epoch;
  {
    const int zero = 0;
    if (w == 0) {
      __builtin_amdgcn_s_setprio(3);
      if (j == 0) asm volatile("s_nop 4\n\tglobal_store_dword %0, %0, %1 sc1" : : "v"(zero), "s"(ctr) : "memory");
    }
    asm volatile("s_nop 4\n\tglobal_load_dword %0, %1, %2 sc1" : "=v"(epoch) : "v"(zero), "s"(a.ws));
  }
  v4u wqr[5], xa, xb; v2u xc;
  {
    const size_t rowoff = (size_t)(80 * j + 10 * w) * (NH * DH * 2) + h * (DH * 2);
    const v4u rw = mkrsrc((const unsigned char*)a.wq + rowoff, (unsigned)((size_t)DM * NH * DH * 2 - rowoff));
    const int vo = l5 < 24 ? hf * (NH * DH * 2) + l5 * 16 : OOB;
#pragma unroll
    for (int i = 0; i < 5; ++i) ld_first(wqr[i], vo, rw, i * 2 * NH * DH * 2);
    const v4u rx = mkrsrc(a.x, 2 * DM * 2);
    const int xo = hf * (DM * 2) + l5 * 40;
    asm volatile("s_nop 4\n\tbuffer_load_dwordx4 %0, %3, %4, 0 offen\n\tbuffer_load_dwordx4 %1, %3, %4, 0 offen offset:16\n\tbuffer_load_dwordx2 %2, %3, %4, 0 offen offset:32"
                 : "=&v"(xa), "=&v"(xb), "=&v"(xc) : "v"(xo), "s"(rx));
  }
  const v4u rk = mkrsrc((const unsigned char*)a.k + (size_t)h * HEADB, B1OFF + HEADB), rv = mkrsrc((const unsigned char*)a.v + (size_t)h * HEADB, B1OFF + HEADB);
  const int voff = (lane >> 3) * (DH * 2) + (lane & 7) * 16;
  const unsigned sb0 = (unsigned)(S0 * j) * ITEMB, sb1 = B1OFF + (unsigned)(S1 * j - S0) * ITEMB;
  v4u km[3], vm[3], ke[NEXTRA][3], ve[NEXTRA][3];
  const unsigned so0 = w ? sb0 + (unsigned)(w - 1) * ITEMB : END;
  asm volatile("s_barrier" ::: "memory");
  ld3_first(km, voff, rk, so0); ld3_first(vm, voff, rv, so0);
  if (w == 0) {
    for (int k = lane; k < DYNMAX; k += 64) DYN[k] = EMPTY;
    if (lane < 48) {
      ((LAS unsigned*)(L + L_HS))[lane] = 0u; ((LAS unsigned*)(L + L_HS))[48 + lane] = 0u; ((LAS unsigned*)(L + L_HS))[96 + lane] = lane < 40 ? 0x3f803f80u : 0u;
    }
    if (lane < 8) CT[lane] = lane ? 0u : (2 * NEXTRA + 1) * NS;
  }
  asm volatile("s_waitcnt vmcnt(6)" : "+v"(wqr[0]), "+v"(wqr[1]), "+v"(wqr[2]), "+v"(wqr[3]), "+v"(wqr[4]), "+v"(xa), "+v"(xb), "+v"(xc), "+v"(epoch));
  ld3_first(ke[0], voff, rk, so0 + NS * ITEMB); ld3_first(ve[0], voff, rv, so0 + NS * ITEMB);
  const unsigned tag = ((__builtin_amdgcn_readfirstlane(epoch) + 0x1E3779B9u) & 0x1FFFFFFFu) | 0x20000000u;
  SSTAMP(1);
  {
    const unsigned xw[10] = {xa.x, xa.y, xa.z, xa.w, xb.x, xb.y, xb.z, xb.w, xc.x, xc.y};
    float s = 0.f, sq = 0.f;
#pragma unroll
    for (int k = 0; k < 10; ++k) { s = dot2(xw[k], 0x3f803f80u, s); sq = dot2(xw[k], xw[k], sq); }
    s = rowsum16(sw16add(s, sq)) * (1.0f / DM);
    const auto sx = __builtin_amdgcn_permlane16_swap(fbits(s), fbits(s), false, false);
    const float mean = ubits(sx[0]), mb = bfr(mean), rd = ln_rden(fmaxf(ubits(sx[1]) - mean * mean, 0.f));
    const float mb0 = rdlane(mb, 0), mb1 = rdlane(mb, 32), rd0 = rdlane(rd, 0), rd1 = rdlane(rd, 32);
    const CAS unsigned* x32 = (const CAS unsigned*)a.x + 40 * j + 5 * w;
    float qa[8], qb[8];
#pragma unroll
    for (int e = 0; e < 8; ++e) { qa[e] = 0.f; qb[e] = 0.f; }
#pragma unroll
    for (int i = 0; i < 5; ++i) {
      const unsigned u0 = x32[i], u1 = x32[DM / 2 + i];
      const float n0 = ln_val(hf ? bfhi(u0) : bflo(u0), mb0, rd0), n1 = ln_val(hf ? bfhi(u1) : bflo(u1), mb1, rd1);
#pragma unroll
      for (int e = 0; e < 4; ++e) {
        const float w0 = bflo(wqr[i][e]), w1 = bfhi(wqr[i][e]);
        qa[2 * e] = fmaf(n0, w0, qa[2 * e]); qa[2 * e + 1] = fmaf(n0, w1, qa[2 * e + 1]);
        qb[2 * e] = fmaf(n1, w0, qb[2 * e]); qb[2 * e + 1] = fmaf(n1, w1, qb[2 * e + 1]);
      }
    }
    float r[8];
#pragma unroll
    for (int e = 0; e < 8; ++e) r[e] = sw32add(qa[e], qb[e]);
    if (l5 < 24) {
      LAS v4f* dst = (LAS v4f*)(L + L_QP + ((w * 2 + hf) * DH + l5 * 8) * 4);
      dst[0] = v4f{r[0], r[1], r[2], r[3]}; dst[1] = v4f{r[4], r[5], r[6], r[7]};
    }
  }
  if (w == 0) asm volatile("s_waitcnt vmcnt(0) ; HELPER_ONLY" ::: "memory");
  BARRIER();
  SSTAMP(2);

  if (w == 0) {
    asm volatile("; HELPER_ENTRY");
    {
      const LAS float* QP = (const LAS float*)(L + L_QP);
#pragma unroll
      for (int k = 0; k < 3; ++k) {
        float s0 = 0.f, s1 = 0.f;
#pragma unroll
        for (int ww = 0; ww < NW; ++ww) { s0 += QP[(ww * 2) * DH + lane + 64 * k]; s1 += QP[(ww * 2 + 1) * DH + lane + 64 * k]; }
        __builtin_amdgcn_raw_buffer_store_b128(v4u{fbits(s0), tag, fbits(s1), tag}, rs, WS_Q + ((h * 8 + j) * DH + lane + 64 * k) * 16, 0, 16);
      }
      if (lane == 0) CT[1] = 1u;
      SSTAMP(13);
    }
    const int g4 = lane >> 4, tq = (lane & 15) >> 2, tp = lane & 3;
    {
      const unsigned lb = (unsigned)(uintptr_t)L;
      const unsigned char* wo = (const unsigned char*)a.wo + (size_t)h * DH * (DM * 2) + j * 160;
#pragma unroll 1
      for (int k = 0; k < 30; ++k) { const int c = k * 64 + lane, r = c / 10; dma16<true>(wo, r * (DM * 2) + (c - r * 10) * 16, lb + L_WO + k * 1024); }
      if (ffn) {
        const size_t o0 = (size_t)80 * j * (FF * 2) + cg * 128;
        const int vo = (lane >> 3) * (FF * 2) + (lane & 7) * 16;
#pragma unroll 1
        for (int k = 0; k < 10; ++k) {
          dma16<true>((const unsigned char*)a.wd + (size_t)f * 8 * (DM * 2) + k * 1024, lane * 16, lb + L_WD + k * 1024);
          dma16<true>((const unsigned char*)a.wg + o0 + k * 8 * FF * 2, vo, lb + L_G + k * 1024);
          dma16<true>((const unsigned char*)a.wu + o0 + k * 8 * FF * 2, vo, lb + L_G + 10240 + k * 1024);
        }
      }
    }
    BARRIER();
    SSTAMP(3);
    waitvm<0>();
    {
      v4u fr[30];
#pragma unroll
      for (int k = 0; k < 30; ++k) {
        LAS unsigned char* p = L + L_WO + (32 * (k % 6) + 8 * g4 + tq) * 160 + (16 * (k / 6) + 4 * tp) * 2;
        const v2u lo = trread(p), hi = trread(p + 4 * 160);
        fr[k] = v4u{lo.x, lo.y, hi.x, hi.y};
      }
      asm volatile("s_waitcnt lgkmcnt(0)" ::: "memory");
#pragma unroll
      for (int k = 0; k < 30; ++k) *(LAS v4u*)(L + L_WO + k * 1024 + lane * 16) = fr[k];
#pragma unroll
      for (int k = 0; k < 24; ++k) {
        const int row = 32 * (k % 3) + 8 * g4 + tq;
        LAS unsigned char* p = L + L_G + (tp >> 1) * 10240 + (row < 80 ? row : tq) * 128 + (8 * (k / 3) + 4 * (tp & 1)) * 2;
        const v2u lo = trread(p), hi = trread(p + 4 * 128);
        fr[k] = v4u{lo.x, lo.y, hi.x, hi.y};
      }
      asm volatile("s_waitcnt lgkmcnt(0)" ::: "memory");
#pragma unroll
      for (int k = 0; k < 24; ++k) *(LAS v4u*)(L + L_G + k * 1024 + lane * 16) = fr[k];
#pragma unroll
      for (int T = 0; T < 10; ++T) {
        LAS unsigned char* p = L + L_WD + tq * 1280 + (64 * T + 16 * g4 + 4 * tp) * 2;
        const v2u lo = trread(p), hi = trread(p + 4 * 1280);
        fr[T] = v4u{lo.x, lo.y, hi.x, hi.y};
      }
      asm volatile("s_waitcnt lgkmcnt(0)" ::: "memory");
#pragma unroll
      for (int T = 0; T < 10; ++T) *(LAS v4u*)(L + L_WD + T * 1024 + lane * 16) = fr[T];
    }
    int filled = 0;
    for (int spin = 0; spin < 16 * SPIN_CAP; ++spin) {
      int need = (int)__builtin_amdgcn_readfirstlane(CT[0]) + LEAD - (STAT + filled);
      if (need > 0) {
        if (filled == 0) {
          unsigned keep;
#pragma unroll 1
          for (unsigned o = 0; o < len; o += 1024)
            asm volatile("s_mov_b32 %0, m0\n\ts_mov_b32 m0, %3\n\ts_nop 2\n\tbuffer_load_dwordx4 %1, %2, %4 offen lds\n\ts_mov_b32 m0, %0"
                         : "=&s"(keep) : "v"(lane * 16), "s"(rc), "s"((unsigned)(uintptr_t)L + L_PF), "s"(o) : "memory");
        }
        need = need > 32 ? 32 : need;
        unsigned old = 0;
        if (lane == 0) old = __hip_atomic_fetch_add(ctr, (unsigned)need, __ATOMIC_RELAXED, __HIP_MEMORY_SCOPE_AGENT);
        old = __builtin_amdgcn_readfirstlane(old);
        const bool done = old + need >= (unsigned)POOL;
        if (lane < (done ? need + NW : need)) DYN[filled + lane] = (lane < need && old + lane < (unsigned)POOL) ? B1OFF + (8 * S1 + old + lane) * ITEMB : END;
        filled += need;
        if (done) break;
      } else __builtin_amdgcn_s_sleep(2);
    }
    waitvm<0>();
    __builtin_amdgcn_s_setprio(0);
  } else {
    asm volatile("; STREAM_ENTRY");
    if (w == NW - 1) {
      hint(CT + 1, QDELAY);
      v4u g[24];
      gather<24>(rs, WS_Q + (h * 8 * DH + lane) * 16, tag, g);
      SSTAMP(13);
      LAS unsigned short* QS = (LAS unsigned short*)(L + L_QS);
#pragma unroll
      for (int k = 0; k < 3; ++k) {
        float s0 = 0.f, s1 = 0.f;
#pragma unroll
        for (int p = 0; p < 8; ++p) { s0 += ubits(g[p * 3 + k].x); s1 += ubits(g[p * 3 + k].z); }
        const unsigned u = pk(s0, s1);
        QS[lane + 64 * k] = (unsigned short)u; QS[DH + lane + 64 * k] = (unsigned short)(u >> 16);
      }
    }
#pragma unroll
    for (int e = 1; e < NEXTRA; ++e) {
      if (w != NW - 1) waitvm<PACE>();
      ld3_first(ke[e], voff, rk, so0 + (e + 1) * NS * ITEMB);
      if (w != NW - 1) waitvm<PACE>();
      ld3_first(ve[e], voff, rv, so0 + (e + 1) * NS * ITEMB);
    }
    BARRIER();
    SSTAMP(3);
    v4u q[3];
#pragma unroll
    for (int jj = 0; jj < 3; ++jj) q[jj] = *(const LAS v4u*)(L + L_QS + ((lane & 7) + 8 * jj) * 16);
    Soft st;
    st.m = M_INIT; st.l = 0.f;
#pragma unroll
    for (int k = 0; k < 24; ++k) st.acc[k] = 0.f;
    auto step = [&](unsigned nxt) {
      wait3<3>(km);
      float s = score(km, q);
      asm volatile("" : "+v"(s));
      ld3_again(km, voff, rk, nxt);
      const float p = soft(st, s);
      wait3<3>(vm);
      pv(st, p, vm);
      __builtin_amdgcn_sched_barrier(0);
      ld3_again(vm, voff, rv, nxt);
    };
#pragma unroll
    for (int e = 0; e < NEXTRA; ++e) {
      step(so0 + (NEXTRA + 1 + e) * NS * ITEMB);
      pin3(ke[e]); pin3(ve[e]);
      const float pe = soft(st, score(ke[e], q));
      pv(st, pe, ve[e]);
    }
    unsigned cur = so0;
#pragma unroll 1
    for (int b = 0; b < 2; ++b) {
      while (cur != END && (cur >= B1OFF) == (b == 1)) {
        unsigned t = 0;
        if (lane == 0) t = __hip_atomic_fetch_add((LAS unsigned*)(L + L_CT), 1u, __ATOMIC_RELAXED, __HIP_MEMORY_SCOPE_WORKGROUP);
        t = __builtin_amdgcn_readfirstlane(t);
        if (t < (unsigned)STAT) cur = (t < (unsigned)S0 ? sb0 : sb1) + t * ITEMB;
        else {
          int spin = 0;
          while ((cur = __builtin_amdgcn_readfirstlane(DYN[t - STAT])) == EMPTY) {
            __builtin_amdgcn_s_sleep(1);
            if (++spin > SPIN_CAP) { cur = END; break; }
          }
        }
        step(cur);
      }
      wait3<0>(km); wait3<0>(vm);
      if (GPOS == 0 && b == 1) {
        unsigned t = 0;
        if (lane == 0) t = __hip_atomic_fetch_add((LAS unsigned*)(L + L_CT) + 4, 1u, __ATOMIC_RELAXED, __HIP_MEMORY_SCOPE_WORKGROUP);
        if (__builtin_amdgcn_readfirstlane(t) == NS - 1) __builtin_amdgcn_raw_buffer_store_b32(tag, rs, lane == 0 ? WS_G + wg * 128 : OOB, 0, 16);
      }
      float M = fmaxf(st.m, dpp<ROR8>(st.m)); M = sw32max(sw16max(M));
      const float sc = __builtin_amdgcn_exp2f(st.m - M);
      float l = st.l * sc; l += dpp<ROR8>(l); l = sw16add(l, l); l = sw32add(l, l);
      float t1[12], t2[6];
#pragma unroll
      for (int k = 0; k < 12; ++k) t1[k] = sw16add(st.acc[k] * sc, st.acc[k + 12] * sc);
#pragma unroll
      for (int k = 0; k < 6; ++k) { t2[k] = sw32add(t1[k], t1[k + 6]); t2[k] += dpp<ROR8>(t2[k]); }
      LAS float* rec = (LAS float*)(L + L_REC) + ((w - 1) * 2 + b) * RECF;
      if (!(lane & 8)) {
        const int a0 = 6 * hf + 12 * ((lane >> 4) & 1);
#pragma unroll
        for (int k = 0; k < 6; k += 2) { const int ai = a0 + k; *(LAS v2f*)(rec + 4 + ((lane & 7) + 8 * (ai >> 3)) * 8 + (ai & 7)) = v2f{t2[k], t2[k + 1]}; }
      }
      if (lane == 0) *(LAS v2f*)rec = v2f{M, l};
#pragma unroll
      for (int jj = 0; jj < 3; ++jj) q[jj] = *(const LAS v4u*)(L + L_QS + DH * 2 + ((lane & 7) + 8 * jj) * 16);
      st.m = M_INIT; st.l = 0.f;
#pragma unroll
      for (int k = 0; k < 24; ++k) st.acc[k] = 0.f;
    }
  }
  STAMP(4);
  const int g4 = lane >> 4, c16 = lane & 15;
  const v4f ctag = {0.f, ubits(tag), 0.f, ubits(tag)};
  const v4u zero4 = {0u, 0u, 0u, 0u};
  BARRIER();
  STAMP(5);
  const bool tilew = w == 0 || w >= 4;
  const int tile = w ? w - 3 : 0;
  v4u bw[6]; unsigned xv0 = 0, xv1 = 0;
  if (!tilew) {
    const int dim = tid - 64;
    if (GPOS == 1) __builtin_amdgcn_raw_buffer_store_b32(tag, rs, dim == 0 ? WS_G + wg * 128 : OOB, 0, 16);
    {
      const LAS float* rec = (const LAS float*)(L + L_REC);
      v2f ml = *(const LAS v2f*)(rec + (c16 < 2 * NS ? c16 : 0) * RECF);
      float av[2 * NS];
#pragma unroll
      for (int r = 0; r < 2 * NS; ++r) av[r] = rec[r * RECF + 4 + dim];
      if (c16 >= 2 * NS) ml = v2f{M_INIT, 0.f};
      float M = fmaxf(ml.x, dpp<XOR2>(ml.x)); M = fmaxf(M, dpp<ROR4>(M)); M = fmaxf(M, dpp<ROR8>(M));
      const float e = __builtin_amdgcn_exp2f(ml.x - M);
      float ls = e * ml.y; ls += dpp<XOR2>(ls); ls += dpp<ROR4>(ls); ls += dpp<ROR8>(ls);
      float o0 = 0.f, o1 = 0.f;
#pragma unroll
      for (int r = 0; r < 2 * NS; r += 2) { o0 = fmaf(rdlane(e, r), av[r], o0); o1 = fmaf(rdlane(e, r + 1), av[r + 1], o1); }
      if (GPOS == 2) __builtin_amdgcn_raw_buffer_store_b32(tag, rs, dim == 0 ? WS_G + wg * 128 : OOB, 0, 16);
      put(rs, WS_P + wg * PBLK + dim * 16, fbits(o0), fbits(o1), tag);
      const float lsx = dpp<XOR1>(ls), Mx = dpp<XOR1>(M);
      put(rs, dim < 2 ? WS_P + wg * PBLK + (DH + dim) * 16 : OOB, fbits((lane & 1) ? lsx : M), fbits((lane & 1) ? ls : Mx), tag);
      STAMP(6);
    }
    v4u g[8], gm = zero4;
#pragma unroll
    for (int p = 0; p < 8; ++p) g[p] = zero4;
    const int po = WS_P + h * 8 * PBLK + dim * 16, pm = WS_P + (h * 8 + ((lane >> 1) & 7)) * PBLK + (DH + (lane & 1)) * 16;
    for (unsigned sent = 0, spin = 0; sent != 0xffu && spin < 4 * SPIN_CAP; ++spin) {
      const unsigned want = __builtin_amdgcn_readfirstlane(CT[5]) & ~sent & 0xffu;
#pragma unroll
      for (int p = 0; p < 8; ++p)
        if ((want >> p) & 1u) g[p] = __builtin_amdgcn_raw_buffer_load_b128(rs, po, p * PBLK, 16);
      sent |= want;
    }
    for (int spin = 0; spin < SPIN_CAP; ++spin) {
      gm = __builtin_amdgcn_raw_buffer_load_b128(rs, pm, 0, 16);
      bool ok = true;
#pragma unroll
      for (int p = 0; p < 8; ++p)
        if (!__all(g[p].y == tag && g[p].w == tag)) { g[p] = __builtin_amdgcn_raw_buffer_load_b128(rs, po, p * PBLK, 16); ok = false; }
      if (ok && __all(gm.y == tag && gm.w == tag)) break;
      asm volatile("" ::: "memory");
    }
    STAMP(7);
    float M0 = ubits(gm.x), M1 = ubits(gm.z);
    M0 = fmaxf(M0, dpp<XOR2>(M0)); M1 = fmaxf(M1, dpp<XOR2>(M1)); M0 = fmaxf(M0, dpp<ROR4>(M0)); M1 = fmaxf(M1, dpp<ROR4>(M1)); M0 = fmaxf(M0, dpp<ROR8>(M0)); M1 = fmaxf(M1, dpp<ROR8>(M1));
    const float e0 = __builtin_amdgcn_exp2f(ubits(gm.x) - M0), e1 = __builtin_amdgcn_exp2f(ubits(gm.z) - M1);
    float l0 = e0 * dpp<XOR1>(ubits(gm.x)), l1 = e1 * dpp<XOR1>(ubits(gm.z));
    l0 += dpp<XOR2>(l0); l1 += dpp<XOR2>(l1); l0 += dpp<ROR4>(l0); l1 += dpp<ROR4>(l1); l0 += dpp<ROR8>(l0); l1 += dpp<ROR8>(l1);
    float o0 = 0.f, o1 = 0.f;
#pragma unroll
    for (int p = 0; p < 8; ++p) { o0 = fmaf(rdlane(e0, 2 * p), ubits(g[p].x), o0); o1 = fmaf(rdlane(e1, 2 * p), ubits(g[p].z), o1); }
    const unsigned ob = pk(o0 * __builtin_amdgcn_rcpf(rdlane(l0, 0)), o1 * __builtin_amdgcn_rcpf(rdlane(l1, 0)));
    ((LAS unsigned short*)(L + L_O))[dim] = (unsigned short)ob; ((LAS unsigned short*)(L + L_O))[DH + dim] = (unsigned short)(ob >> 16);
  } else {
#pragma unroll
    for (int k = 0; k < 6; ++k) bw[k] = *(const LAS v4u*)(L + L_WO + (tile * 6 + k) * 1024 + lane * 16);
    xv0 = a.x[80 * j + 16 * tile + c16]; xv1 = a.x[DM + 80 * j + 16 * tile + c16];
    asm volatile("" : "+v"(xv0), "+v"(xv1));
    if (w == NW - 1) { watch<8>(rs, WS_G + h * 8 * 128, 128, lane, tag, CT + 5); STAMP(13); }
  }
  BARRIER();
  STAMP(1);
  if (tilew) {
    const int ybase = WS_Y + (j * 5 + tile) * (NH * 256);
    if (DPOS == 0) __builtin_amdgcn_raw_buffer_store_b32(tag, rs, tid == 0 ? WS_D + wg * 128 : OOB, 0, 16);
    v4f d = ctag;
#pragma unroll
    for (int k = 0; k < 6; ++k) {
      const v4u ao = *(const LAS v4u*)(L + L_O + (c16 & 2) * DH + (32 * k + 8 * g4) * 2);
      d = mfma((c16 & 1) ? zero4 : ao, bw[k], d);
    }
    if (DPOS == 1) { asm volatile("" : "+v"(d)); __builtin_amdgcn_raw_buffer_store_b32(tag, rs, tid == 0 ? WS_D + wg * 128 : OOB, 0, 16); }
    __builtin_amdgcn_raw_buffer_store_b128(__builtin_bit_cast(v4u, d), rs, lane < 16 ? ybase + h * 256 + c16 * 16 : OOB, 0, 16);
    STAMP(8);
    if (ffn) {
      v4u g[8];
#pragma unroll
      for (int k = 0; k < 8; ++k) g[k] = zero4;
      for (unsigned sent = 0, spin = 0; sent != 0x11111111u && spin < 4 * SPIN_CAP; ++spin) {
        unsigned m = __builtin_amdgcn_readfirstlane(CT[6]);
        m &= m >> 1; m &= m >> 2; m &= 0x11111111u & ~sent;
#pragma unroll
        for (int k = 0; k < 8; ++k)
          if ((m >> (4 * k)) & 1u) g[k] = __builtin_amdgcn_raw_buffer_load_b128(rs, ybase + lane * 16 + (k & 3) * 1024, (k >> 2) * 4096, 16);
        sent |= m;
      }
      for (int spin = 0; spin < SPIN_CAP; ++spin) {
        bool ok = true;
#pragma unroll
        for (int k = 0; k < 8; ++k)
          if (!__all(g[k].y == tag && g[k].w == tag)) { g[k] = __builtin_amdgcn_raw_buffer_load_b128(rs, ybase + lane * 16 + (k & 3) * 1024, (k >> 2) * 4096, 16); ok = false; }
        if (ok) break;
        asm volatile("" ::: "memory");
      }
      STAMP(2);
      const float s0 = ((ubits(g[0].x) + ubits(g[1].x)) + (ubits(g[2].x) + ubits(g[3].x))) + ((ubits(g[4].x) + ubits(g[5].x)) + (ubits(g[6].x) + ubits(g[7].x)));
      const float s1 = ((ubits(g[0].z) + ubits(g[1].z)) + (ubits(g[2].z) + ubits(g[3].z))) + ((ubits(g[4].z) + ubits(g[5].z)) + (ubits(g[6].z) + ubits(g[7].z)));
      float s = sw16add(s0, s1); s = sw32add(s, s);
      const float hv = bflo((g4 & 1) ? xv1 : xv0) + bfr(s);
      if (lane < 32) ((LAS unsigned short*)(L + L_HS))[g4 * 96 + 16 * tile + c16] = (unsigned short)pk(hv, 0.f);
    }
  } else if (ffn && w == 1) { watch<32>(rs, WS_D + j * 128, 8 * 128, lane, tag, CT + 6); STAMP(14); }
  if (ffn) {
    v4u bg[3], bdn[3]; int zo[3];
#pragma unroll
    for (int k = 0; k < 3; ++k) bg[k] = *(const LAS v4u*)(L + L_G + (w * 3 + k) * 1024 + lane * 16);
    const int u = (w - 1) & 3;
#pragma unroll
    for (int T = 0; T < 3; ++T) {
      const int dt = u + 4 * T, n = 64 * dt + lane;
      bdn[T] = *(const LAS v4u*)(L + L_WD + (dt < 10 ? dt : 0) * 1024 + lane * 16);
      zo[T] = dt < 10 ? WS_Z + ((n >> 2) * NFFN + f) * 64 + (n & 3) * 16 : OOB;
      asm volatile("" : "+v"(zo[T]));
    }
    int eo = WS_E + (cg * 8 + w) * 5120;
    const int eo0 = lane < 32 ? eo + j * 512 + lane * 16 : OOB, eo1 = lane < 8 ? eo + 4096 + j * 128 + lane * 16 : OOB;
    BARRIER();
    STAMP(3);
    {
      v4f d = ctag, ds = ctag;
#pragma unroll
      for (int k = 0; k < 3; ++k) {
        v4u ah = *(const LAS v4u*)(L + L_HS + (c16 < 5 ? c16 >> 1 : 0) * 192 + (32 * k + 8 * g4) * 2);
        if ((c16 & 1) || c16 > 4) ah = zero4;
        d = mfma(ah, bg[k], d); ds = mfma(ah, ah, ds);
      }
      __builtin_amdgcn_raw_buffer_store_b128(__builtin_bit_cast(v4u, d), rs, eo0, 0, 16);
      __builtin_amdgcn_raw_buffer_store_b128(__builtin_bit_cast(v4u, ds), rs, eo1, 0, 16);
    }
    STAMP(9);
    if (w >= 1 && w <= 4) {
      v4u g[5];
      __builtin_amdgcn_s_sleep(EDELAY);
      gather<5>(rs, WS_E + f * 5120 + lane * 16, tag, g);
      if (FPOS == 0) __builtin_amdgcn_raw_buffer_store_b32(tag, rs, (tid == 64 && j == 0) ? WS_F + cg * 128 : OOB, 0, 16);
      STAMP(10);
      const float s0 = (ubits(g[0].x) + ubits(g[1].x)) + (ubits(g[2].x) + ubits(g[3].x)), s1 = (ubits(g[0].z) + ubits(g[1].z)) + (ubits(g[2].z) + ubits(g[3].z));
      const float gs = sw32add(s0, s1);
      const float cs0 = sw32add(s0, s0);
      const auto cx = __builtin_amdgcn_permlane16_swap(fbits(cs0), fbits(cs0), false, false);
      const auto gx = __builtin_amdgcn_permlane16_swap(fbits(gs), fbits(gs), false, false);
      float t0 = ubits(g[4].x), t1 = ubits(g[4].z);
      t0 += dpp<ROR8>(t0); t1 += dpp<ROR8>(t1);
      t0 = sw16add(t0, t0); t1 = sw16add(t1, t1);
      const float tt = sw32add(t0, t1) * (1.0f / DM);
      const float mean = dpp<BC + 4>(tt), q0 = dpp<BC + 0>(tt), q1 = dpp<BC + 2>(tt);
      const float rdv = ln_rden(fmaxf((hf ? q1 : q0) - mean * mean, 0.f));
      const float gv = bfr((ubits(gx[0]) - bfr(mean) * ubits(cx[1])) * rdv), uv = dpp<ROR8>(gv);
      const float y2 = (-2.0f * 0.7978845608028654f * 1.4426950408889634f) * (gv + 0.044715f * gv * gv * gv);
      const float act = bfr(gv * __builtin_amdgcn_rcpf(1.0f + __builtin_amdgcn_exp2f(y2))) * uv;
      const unsigned ap = pk(act, dpp<XOR1>(act));
      const auto ax = __builtin_amdgcn_permlane32_swap(ap, ap, false, false);
      const unsigned keep = ((c16 >> 2) == g4 && !(c16 & 1)) ? 0xffffffffu : 0u, k1 = (c16 & 2) ? keep : 0u, k0 = keep ^ k1;
      const v4u b0 = {dppu<BC + 0>(ax[0]), dppu<BC + 2>(ax[0]), dppu<BC + 4>(ax[0]), dppu<BC + 6>(ax[0])};
      const v4u b1 = {dppu<BC + 0>(ax[1]), dppu<BC + 2>(ax[1]), dppu<BC + 4>(ax[1]), dppu<BC + 6>(ax[1])};
      v4u a2 = (b0 & k0) | (b1 & k1);
      if (FPOS == 1) { asm volatile("" : "+v"(a2)); __builtin_amdgcn_raw_buffer_store_b32(tag, rs, (tid == 64 && j == 0) ? WS_F + cg * 128 : OOB, 0, 16); }
#pragma unroll
      for (int T = 0; T < 3; ++T) __builtin_amdgcn_raw_buffer_store_b128(__builtin_bit_cast(v4u, mfma(a2, bdn[T], ctag)), rs, zo[T], 0, 16);
      STAMP(11);
    } else if (w == NW - 1 && cg < 20) {
      const __amdgpu_buffer_rsrc_t ro = __builtin_amdgcn_make_buffer_rsrc((void*)a.out, 0, 2 * DM * 2, 0x00020000);
      const float hv = bflo(((const LAS unsigned short*)(L + L_HS))[(g4 & 1) * 96 + 4 * cg + (lane & 3)]);
      v4u g[12];
      watch<24>(rs, WS_F, 128, lane, tag, CT + 7);
      STAMP(15);
      __builtin_amdgcn_s_sleep(ZDELAY);
      gather<12>(rs, WS_Z + (20 * j + cg) * (NFFN * 64) + lane * 16, tag, g);
      float s0[4], s1[4];
#pragma unroll
      for (int k = 0; k < 4; ++k) { s0[k] = (ubits(g[k].x) + ubits(g[k + 4].x)) + ubits(g[k + 8].x); s1[k] = (ubits(g[k].z) + ubits(g[k + 4].z)) + ubits(g[k + 8].z); }
      float s = sw16add((s0[0] + s0[1]) + (s0[2] + s0[3]), (s1[0] + s1[1]) + (s1[2] + s1[3]));
      s = sw32add(s, s); s += dpp<ROR4>(s); s += dpp<ROR8>(s);
      const unsigned o = pk(hv + bfr(s), 0.f);
      __builtin_amdgcn_raw_buffer_store_b16((unsigned short)o, ro, (lane < 32 && c16 < 4) ? (g4 * DM + 80 * j + 4 * cg + c16) * 2 : OOB, 0, 0);
      __builtin_amdgcn_raw_buffer_store_b32(tag, rs, (wg == 0 && lane == 0) ? 0 : OOB, 0, 0);
      STAMP(12);
    }
  }
  STAMP_DUMP();
  asm volatile(".Ldb_end:");
}
}

extern "C" void kernel_launch(void* const* d_in, const int* in_sizes, int n_in, void* d_out, int out_size, void* d_ws, size_t ws_size, hipStream_t stream) {
  Args a;
  a.x = (const unsigned short*)d_in[0]; a.k = (const unsigned short*)d_in[1]; a.v = (const unsigned short*)d_in[2];
  a.wq = (const unsigned short*)d_in[3]; a.wo = (const unsigned short*)d_in[4];
  a.wg = (const unsigned short*)d_in[6]; a.wu = (const unsigned short*)d_in[7]; a.wd = (const unsigned short*)d_in[8];
  a.out = (unsigned short*)d_out; a.ws = (unsigned char*)d_ws;
  hipLaunchKernelGGL(decode_block, dim3(NWG), dim3(NTHR), 0, stream, a);
}
```

```cpp
#define KSTAMP 1
#define STARTSTAMPS_OFF 1
#include <cstdio>
#include <vector>
#include <algorithm>
#include <hip/hip_runtime.h>
#include <hip/hip_bf16.h>
#include <stdint.h>

#define PACE 3
#define NEX 5
#define POLLSLEEP 1
#define REWARM 1
#define STAT1 192
#define LEADN 8
#define WSLEEP 1
#define GPOS 0
#define DPOS 0
#define FPOS 0
#define QDELAY 10
#define EDELAY 10
#define ZDELAY 0
#define MIMIC_SCORE 1

namespace {
typedef unsigned v4u __attribute__((ext_vector_type(4)));
typedef unsigned v2u __attribute__((ext_vector_type(2)));
typedef float v4f __attribute__((ext_vector_type(4)));
typedef float v2f __attribute__((ext_vector_type(2)));
typedef short v4s __attribute__((ext_vector_type(4)));
typedef short v8s __attribute__((ext_vector_type(8)));
typedef __bf16 bf2 __attribute__((ext_vector_type(2)));
typedef __bf16 bf8 __attribute__((ext_vector_type(8)));
#define LAS __attribute__((address_space(3)))
#define CAS __attribute__((address_space(4)))

constexpr int DM = 640, NH = 32, SK = 16384, DH = 192, FF = 1536;
constexpr int NWG = 256, NW = 8, NS = NW - 1, NTHR = NW * 64;
constexpr unsigned ITEMB = 8 * DH * 2;
constexpr unsigned HEADB = (unsigned)SK * DH * 2, B1OFF = NH * HEADB;
constexpr int IPH = SK / 8;
constexpr int S0 = IPH / 8, S1 = STAT1, STAT = S0 + S1;
constexpr int POOL = IPH - 8 * S1, LEAD = LEADN, NEXTRA = NEX;
constexpr unsigned END = 0xF0000000u, EMPTY = 0xFFFFFFFFu;
constexpr int NRED = DM / 4, NFFN = FF / 8;
constexpr int SPIN_CAP = 1 << 17;
constexpr float M_INIT = -3.0e38f;

constexpr int WS_CTR = 4096, CTR_STRIDE = 4352;
constexpr int WS_F = 0x24000, WS_D = 0x28000, WS_G = 0x30000, WS_Q = 0x40000, WS_P = 0x100000, PBLK = 3200, WS_Y = 0x200000, WS_E = 0x280000, WS_Z = 0x400000, WS_BYTES = 0x600000;
constexpr int OOB = (int)0x80000000u;
static_assert(WS_CTR + NH * CTR_STRIDE <= WS_F && WS_F + 24 * 128 <= WS_D && WS_D + NWG * 128 <= WS_G && WS_G + NWG * 128 <= WS_Q && WS_Q + NWG * DH * 16 <= WS_P && WS_P + NWG * PBLK <= WS_Y, "ws map");
static_assert(WS_Y + NWG * 80 * 16 <= WS_E && WS_E + NFFN * 5120 <= WS_Z && WS_Z + NRED * NFFN * 64 <= WS_BYTES, "ws map");

constexpr int L_WO = 0, L_G = L_WO + DH * 160, L_WD = L_G + 24 * 1024, L_QP = L_WD + 8 * 1280;
constexpr int L_QS = L_QP + NW * 2 * DH * 4, RECF = DH + 4, L_REC = L_QS + 2 * DH * 2, L_O = L_REC + NS * 2 * RECF * 4, L_HS = L_O + 2 * DH * 2;
constexpr int L_CT = L_HS + 3 * 192, DYNMAX = POOL + 64, L_DYN = L_CT + 64, L_PF = L_DYN + DYNMAX * 4, L_BYTES = L_PF + NW * 1024;
static_assert(L_BYTES > 81920 && L_BYTES <= 160 * 1024 && L_REC % 16 == 0 && L_O % 16 == 0 && L_HS % 16 == 0 && L_PF % 16 == 0, "one workgroup per CU");

struct Args {
  const unsigned short *x, *k, *v, *wq, *wo, *wg, *wu, *wd;
  unsigned short* out;
  unsigned char* ws;
};

__device__ __forceinline__ float dot2(unsigned a, unsigned b, float c) {
  return __builtin_amdgcn_fdot2_f32_bf16(__builtin_bit_cast(bf2, a), __builtin_bit_cast(bf2, b), c, false);
}
template <int CTRL> __device__ __forceinline__ float dpp(float x) {
  return __builtin_bit_cast(float, __builtin_amdgcn_mov_dpp(__builtin_bit_cast(int, x), CTRL, 0xf, 0xf, true));
}
template <int CTRL> __device__ __forceinline__ unsigned dppu(unsigned x) { return (unsigned)__builtin_amdgcn_mov_dpp((int)x, CTRL, 0xf, 0xf, true); }
constexpr int BC = 0x150;
constexpr int XOR1 = 0xB1, XOR2 = 0x4E, HMIR = 0x141, ROR4 = 0x124, ROR8 = 0x128;
__device__ __forceinline__ float bflo(unsigned x) { return __builtin_bit_cast(float, x << 16); }
__device__ __forceinline__ float bfhi(unsigned x) { return __builtin_bit_cast(float, x & 0xffff0000u); }
__device__ __forceinline__ unsigned pk(float lo, float hi) {
  const bf2 r = {(__bf16)lo, (__bf16)hi};
  return __builtin_bit_cast(unsigned, r);
}
__device__ __forceinline__ float bfr(float x) { return (float)(__bf16)x; }
__device__ __forceinline__ unsigned fbits(float x) { return __builtin_bit_cast(unsigned, x); }
__device__ __forceinline__ float ubits(unsigned x) { return __builtin_bit_cast(float, x); }
__device__ __forceinline__ float sw16add(float a, float b) {
  const auto s = __builtin_amdgcn_permlane16_swap(fbits(a), fbits(b), false, false);
  return ubits(s[0]) + ubits(s[1]);
}
__device__ __forceinline__ float sw32add(float a, float b) {
  const auto s = __builtin_amdgcn_permlane32_swap(fbits(a), fbits(b), false, false);
  return ubits(s[0]) + ubits(s[1]);
}
__device__ __forceinline__ float sw16max(float a) {
  const auto s = __builtin_amdgcn_permlane16_swap(fbits(a), fbits(a), false, false);
  return fmaxf(ubits(s[0]), ubits(s[1]));
}
__device__ __forceinline__ float sw32max(float a) {
  const auto s = __builtin_amdgcn_permlane32_swap(fbits(a), fbits(a), false, false);
  return fmaxf(ubits(s[0]), ubits(s[1]));
}
__device__ __forceinline__ float rowsum16(float s) { s += dpp<XOR1>(s); s += dpp<XOR2>(s); s += dpp<HMIR>(s); s += dpp<ROR8>(s); return s; }
__device__ __forceinline__ float rdlane(float x, int l) { return ubits(__builtin_amdgcn_readlane(fbits(x), l)); }
__device__ __forceinline__ v4u mkrsrc(const void* p, unsigned bytes) {
  const unsigned long long a = (unsigned long long)p;
  return v4u{(unsigned)a, (unsigned)(a >> 32) & 0xffffu, bytes, 0x00020000u};
}
__device__ __forceinline__ v4f mfma(v4u a, v4u b, v4f c) {
  return __builtin_amdgcn_mfma_f32_16x16x32_bf16(__builtin_bit_cast(bf8, a), __builtin_bit_cast(bf8, b), c, 0, 0, 0);
}
__device__ __forceinline__ v2u trread(LAS unsigned char* p) {
  return __builtin_bit_cast(v2u, __builtin_amdgcn_ds_read_tr16_b64_v4i16((LAS v4s*)p));
}
#define BARRIER() asm volatile("s_waitcnt lgkmcnt(0)\n\ts_barrier" ::: "memory")

#define LD3 "s_nop 4\n\tbuffer_load_dwordx4 %0, %3, %4, %5 offen nt\n\tbuffer_load_dwordx4 %1, %3, %4, %5 offen offset:128 nt\n\tbuffer_load_dwordx4 %2, %3, %4, %5 offen offset:256 nt"
__device__ __forceinline__ void ld_first(v4u& d, int voff, v4u rs, unsigned soff) {
  asm volatile("s_nop 4\n\tbuffer_load_dwordx4 %0, %1, %2, %3 offen nt" : "=v"(d) : "v"(voff), "s"(rs), "s"(soff));
}
__device__ __forceinline__ void ld3_first(v4u (&r)[3], int voff, v4u rs, unsigned so) { asm volatile(LD3 : "=&v"(r[0]), "=&v"(r[1]), "=&v"(r[2]) : "v"(voff), "s"(rs), "s"(so)); }
__device__ __forceinline__ void ld3_again(v4u (&r)[3], int voff, v4u rs, unsigned so) { asm volatile(LD3 : "+v"(r[0]), "+v"(r[1]), "+v"(r[2]) : "v"(voff), "s"(rs), "s"(so)); }
template <int N> __device__ __forceinline__ void wait3(v4u (&r)[3]) { asm volatile("s_waitcnt vmcnt(%3)" : "+v"(r[0]), "+v"(r[1]), "+v"(r[2]) : "i"(N)); }
__device__ __forceinline__ void pin3(v4u (&r)[3]) { asm volatile("" : "+v"(r[0]), "+v"(r[1]), "+v"(r[2])); }
template <int N> __device__ __forceinline__ void waitvm() { asm volatile("s_waitcnt vmcnt(%0)" : : "i"(N) : "memory"); }

template <bool NT> __device__ __forceinline__ void dma16(const void* base, int voff, unsigned lds) {
  unsigned keep;
  if (NT) asm volatile("s_mov_b32 %0, m0\n\ts_mov_b32 m0, %3\n\ts_nop 2\n\tglobal_load_lds_dwordx4 %1, %2 nt\n\ts_mov_b32 m0, %0" : "=&s"(keep) : "v"(voff), "s"(base), "s"(lds) : "memory");
  else asm volatile("s_mov_b32 %0, m0\n\ts_mov_b32 m0, %3\n\ts_nop 2\n\tglobal_load_lds_dwordx4 %1, %2\n\ts_mov_b32 m0, %0" : "=&s"(keep) : "v"(voff), "s"(base), "s"(lds) : "memory");
}

__device__ __forceinline__ void put(__amdgpu_buffer_rsrc_t rs, int off, unsigned a, unsigned b, unsigned tag) {
  __builtin_amdgcn_raw_buffer_store_b128(v4u{a, tag, b, tag}, rs, off, 0, 16);
}
__shared__ unsigned long long g_stamp[NW * 16];
#define STAMPMASK 0xffff
#define STAMP(n) do { if (((STAMPMASK >> (n)) & 1) && (threadIdx.x & 63) == 0) g_stamp[(threadIdx.x >> 6) * 16 + (n)] = __builtin_amdgcn_s_memrealtime(); } while (0)
#define SSTAMP(n)
#define STAMP_INIT() do { if ((threadIdx.x & 63) < 16) g_stamp[(threadIdx.x >> 6) * 16 + (threadIdx.x & 63)] = 0; } while (0)
#define STAMP_DUMP() do { if ((threadIdx.x & 63) < 16) ((unsigned long long*)(a.ws + 0xC00000))[(blockIdx.x * NW + (threadIdx.x >> 6)) * 16 + (threadIdx.x & 63)] = g_stamp[(threadIdx.x >> 6) * 16 + (threadIdx.x & 63)]; } while (0)
#define GDBG , int dbg = -1
#define GSTAMP() do { if (dbg >= 0 && (threadIdx.x & 63) == 0) g_stamp[(threadIdx.x >> 6) * 16 + dbg] = spin + 1; } while (0)
#define GARG(n) , n
template <int N> __device__ __forceinline__ void gather(__amdgpu_buffer_rsrc_t rs, int off, unsigned tag, v4u (&t)[N] GDBG) {
  bool done = false;
#pragma unroll
  for (int k = 0; k < N; ++k) t[k] = v4u{0u, 0u, 0u, 0u};
  for (int spin = 0; spin < SPIN_CAP; ++spin) {
    if (!done) {
#pragma unroll
      for (int k = 0; k < N; ++k) t[k] = __builtin_amdgcn_raw_buffer_load_b128(rs, off + (k & 3) * 1024, (k >> 2) * 4096, 16);
      unsigned mn = tag, mx = tag;
#pragma unroll
      for (int k = 0; k < N; ++k) { mn = min(mn, min(t[k].y, t[k].w)); mx = max(mx, max(t[k].y, t[k].w)); }
      done = mn == mx;
    }
    GSTAMP();
    if (__all(done)) break;
    asm volatile("" ::: "memory");
    __builtin_amdgcn_s_sleep(POLLSLEEP);
  }
}

template <int N> __device__ __forceinline__ void watch(__amdgpu_buffer_rsrc_t rs, int off, int stride, int lane, unsigned tag, volatile LAS unsigned* mask) {
  bool seen = lane >= N;
  for (int spin = 0; spin < SPIN_CAP; ++spin) {
    if (!seen) seen = __builtin_amdgcn_raw_buffer_load_b32(rs, off + lane * stride, 0, 16) == tag;
    const unsigned long long m = __ballot(seen);
    if (lane == 0) *mask = (unsigned)m;
    if (m == ~0ull) break;
    asm volatile("" ::: "memory");
    __builtin_amdgcn_s_sleep(WSLEEP);
  }
  if (lane == 0) *mask = ~0u;
}
#define hint(p, D) do { for (int sp_ = 0; sp_ < SPIN_CAP && *(p) == 0u; ++sp_) __builtin_amdgcn_s_sleep(1); __builtin_amdgcn_s_sleep(D); } while (0)

__device__ __forceinline__ float ln_rden(float var) { return __builtin_amdgcn_rcpf(bfr(__builtin_amdgcn_sqrtf(bfr(bfr(var) + 1e-5f)))); }
__device__ __forceinline__ float ln_val(float x, float mean_b, float rden) { return bfr(bfr(x - mean_b) * rden); }

struct Soft { float m, l, acc[24]; };
__device__ __forceinline__ float score(const v4u (&kk)[3], const v4u (&q)[3]) {
  float s0 = 0.f, s1 = 0.f;
#pragma unroll
  for (int j = 0; j < 3; ++j) {
    s0 = dot2(kk[j].x, q[j].x, s0); s1 = dot2(kk[j].y, q[j].y, s1);
    s0 = dot2(kk[j].z, q[j].z, s0); s1 = dot2(kk[j].w, q[j].w, s1);
  }
  return s0 + s1;
}
__device__ __forceinline__ float soft(Soft& st, float s) {
  s += dpp<XOR1>(s); s += dpp<XOR2>(s); s += dpp<HMIR>(s);
  s = bfr(bfr(s) * 0.07216878364870322f) * 1.4426950408889634f;
  if (__any(s > st.m)) {
    const float mn = fmaxf(st.m, s), al = __builtin_amdgcn_exp2f(st.m - mn);
    st.l *= al;
#pragma unroll
    for (int k = 0; k < 24; ++k) st.acc[k] *= al;
    st.m = mn;
  }
  const float p = __builtin_amdgcn_exp2f(s - st.m);
  st.l += p;
  return p;
}
__device__ __forceinline__ void pv(Soft& st, float p, const v4u (&vv)[3]) {
#pragma unroll
  for (int j = 0; j < 3; ++j)
#pragma unroll
    for (int e = 0; e < 4; ++e) {
      st.acc[j * 8 + 2 * e] = fmaf(p, bflo(vv[j][e]), st.acc[j * 8 + 2 * e]);
      st.acc[j * 8 + 2 * e + 1] = fmaf(p, bfhi(vv[j][e]), st.acc[j * 8 + 2 * e + 1]);
    }
}

__global__ void __launch_bounds__(NTHR, 2) decode_block(Args a) {
  __shared__ __attribute__((aligned(16))) unsigned char smem[L_BYTES];
  LAS unsigned char* L = (LAS unsigned char*)smem;
  volatile LAS unsigned* CT = (volatile LAS unsigned*)(L + L_CT);
  volatile LAS unsigned* DYN = (volatile LAS unsigned*)(L + L_DYN);
  const int tid = threadIdx.x, lane = tid & 63, w = __builtin_amdgcn_readfirstlane(tid >> 6);
  const int wg = blockIdx.x, h = wg >> 3, j = wg & 7;
  const int hf = lane >> 5, l5 = lane & 31;
  const bool ffn = ((h + j) & 3) != 3;
  const int cg = 3 * (h >> 2) + (h & 3) - ((h & 3) > ((3 - j) & 3) ? 1 : 0), f = 8 * cg + j;
  const __amdgpu_buffer_rsrc_t rs = __builtin_amdgcn_make_buffer_rsrc((void*)a.ws, 0, WS_BYTES, 0x00020000);
  unsigned* ctr = (unsigned*)(a.ws + WS_CTR + h * CTR_STRIDE);
  STAMP_INIT();
  STAMP(0);

  unsigned long long pc; unsigned len;
  asm volatile("s_getpc_b64 %0\n.Ldb_here:\n\ts_mov_b32 %1, .Ldb_end-.Ldb_here" : "=s"(pc), "=s"(len));
  const v4u rc = mkrsrc((const void*)pc, len);
  {
    unsigned keep;
    const int vo = w * 1024 + lane * 16;
    asm volatile("s_mov_b32 %0, m0\n\ts_mov_b32 m0, %3\n\ts_nop 2\n\tbuffer_load_dwordx4 %1, %2, 0 offen lds\n\tbuffer_load_dwordx4 %1, %2, %4 offen lds\n\t"
                 "buffer_load_dwordx4 %1, %2, %5 offen lds\n\tbuffer_load_dwordx4 %1, %2, %6 offen lds\n\ts_mov_b32 m0, %0"
                 : "=&s"(keep) : "v"(vo), "s"(rc), "s"((unsigned)(uintptr_t)L + L_PF + w * 1024), "s"(8192), "s"(16384), "s"(24576) : "memory");
  }
  unsigned epoch;
  {
    const int zero = 0;
    if (w == 0) {
      __builtin_amdgcn_s_setprio(3);
      if (j == 0) asm volatile("s_nop 4\n\tglobal_store_dword %0, %0, %1 sc1" : : "v"(zero), "s"(ctr) : "memory");
    }
    asm volatile("s_nop 4\n\tglobal_load_dword %0, %1, %2 sc1" : "=v"(epoch) : "v"(zero), "s"(a.ws));
  }
  v4u wqr[5], xa, xb; v2u xc;
  {
    const size_t rowoff = (size_t)(80 * j + 10 * w) * (NH * DH * 2) + h * (DH * 2);
    const v4u rw = mkrsrc((const unsigned char*)a.wq + rowoff, (unsigned)((size_t)DM * NH * DH * 2 - rowoff));
    const int vo = l5 < 24 ? hf * (NH * DH * 2) + l5 * 16 : OOB;
#pragma unroll
    for (int i = 0; i < 5; ++i) ld_first(wqr[i], vo, rw, i * 2 * NH * DH * 2);
    const v4u rx = mkrsrc(a.x, 2 * DM * 2);
    const int xo = hf * (DM * 2) + l5 * 40;
    asm volatile("s_nop 4\n\tbuffer_load_dwordx4 %0, %3, %4, 0 offen\n\tbuffer_load_dwordx4 %1, %3, %4, 0 offen offset:16\n\tbuffer_load_dwordx2 %2, %3, %4, 0 offen offset:32"
                 : "=&v"(xa), "=&v"(xb), "=&v"(xc) : "v"(xo), "s"(rx));
  }
  const v4u rk = mkrsrc((const unsigned char*)a.k + (size_t)h * HEADB, B1OFF + HEADB), rv = mkrsrc((const unsigned char*)a.v + (size_t)h * HEADB, B1OFF + HEADB);
  const int voff = (lane >> 3) * (DH * 2) + (lane & 7) * 16;
  const unsigned sb0 = (unsigned)(S0 * j) * ITEMB, sb1 = B1OFF + (unsigned)(S1 * j - S0) * ITEMB;
  v4u km[3], vm[3], ke[NEXTRA][3], ve[NEXTRA][3];
  const unsigned so0 = w ? sb0 + (unsigned)(w - 1) * ITEMB : END;
  asm volatile("s_barrier" ::: "memory");
  ld3_first(km, voff, rk, so0); ld3_first(vm, voff, rv, so0);
  if (w == 0) {
    for (int k = lane; k < DYNMAX; k += 64) DYN[k] = EMPTY;
    if (lane < 48) {
      ((LAS unsigned*)(L + L_HS))[lane] = 0u; ((LAS unsigned*)(L + L_HS))[48 + lane] = 0u; ((LAS unsigned*)(L + L_HS))[96 + lane] = lane < 40 ? 0x3f803f80u : 0u;
    }
    if (lane < 8) CT[lane] = lane ? 0u : (2 * NEXTRA + 1) * NS;
  }
  asm volatile("s_waitcnt vmcnt(6)" : "+v"(wqr[0]), "+v"(wqr[1]), "+v"(wqr[2]), "+v"(wqr[3]), "+v"(wqr[4]), "+v"(xa), "+v"(xb), "+v"(xc), "+v"(epoch));
  ld3_first(ke[0], voff, rk, so0 + NS * ITEMB); ld3_first(ve[0], voff, rv, so0 + NS * ITEMB);
  const unsigned tag = ((__builtin_amdgcn_readfirstlane(epoch) + 0x1E3779B9u) & 0x1FFFFFFFu) | 0x20000000u;
  SSTAMP(1);
  {
    const unsigned xw[10] = {xa.x, xa.y, xa.z, xa.w, xb.x, xb.y, xb.z, xb.w, xc.x, xc.y};
    float s = 0.f, sq = 0.f;
#pragma unroll
    for (int k = 0; k < 10; ++k) { s = dot2(xw[k], 0x3f803f80u, s); sq = dot2(xw[k], xw[k], sq); }
    s = rowsum16(sw16add(s, sq)) * (1.0f / DM);
    const auto sx = __builtin_amdgcn_permlane16_swap(fbits(s), fbits(s), false, false);
    const float mean = ubits(sx[0]), mb = bfr(mean), rd = ln_rden(fmaxf(ubits(sx[1]) - mean * mean, 0.f));
    const float mb0 = rdlane(mb, 0), mb1 = rdlane(mb, 32), rd0 = rdlane(rd, 0), rd1 = rdlane(rd, 32);
    const CAS unsigned* x32 = (const CAS unsigned*)a.x + 40 * j + 5 * w;
    float qa[8], qb[8];
#pragma unroll
    for (int e = 0; e < 8; ++e) { qa[e] = 0.f; qb[e] = 0.f; }
#pragma unroll
    for (int i = 0; i < 5; ++i) {
      const unsigned u0 = x32[i], u1 = x32[DM / 2 + i];
      const float n0 = ln_val(hf ? bfhi(u0) : bflo(u0), mb0, rd0), n1 = ln_val(hf ? bfhi(u1) : bflo(u1), mb1, rd1);
#pragma unroll
      for (int e = 0; e < 4; ++e) {
        const float w0 = bflo(wqr[i][e]), w1 = bfhi(wqr[i][e]);
        qa[2 * e] = fmaf(n0, w0, qa[2 * e]); qa[2 * e + 1] = fmaf(n0, w1, qa[2 * e + 1]);
        qb[2 * e] = fmaf(n1, w0, qb[2 * e]); qb[2 * e + 1] = fmaf(n1, w1, qb[2 * e + 1]);
      }
    }
    float r[8];
#pragma unroll
    for (int e = 0; e < 8; ++e) r[e] = sw32add(qa[e], qb[e]);
    if (l5 < 24) {
      LAS v4f* dst = (LAS v4f*)(L + L_QP + ((w * 2 + hf) * DH + l5 * 8) * 4);
      dst[0] = v4f{r[0], r[1], r[2], r[3]}; dst[1] = v4f{r[4], r[5], r[6], r[7]};
    }
  }
  if (w == 0) asm volatile("s_waitcnt vmcnt(0) ; HELPER_ONLY" ::: "memory");
  BARRIER();
  SSTAMP(2);

  if (w == 0) {
    asm volatile("; HELPER_ENTRY");
    {
      const LAS float* QP = (const LAS float*)(L + L_QP);
#pragma unroll
      for (int k = 0; k < 3; ++k) {
        float s0 = 0.f, s1 = 0.f;
#pragma unroll
        for (int ww = 0; ww < NW; ++ww) { s0 += QP[(ww * 2) * DH + lane + 64 * k]; s1 += QP[(ww * 2 + 1) * DH + lane + 64 * k]; }
        __builtin_amdgcn_raw_buffer_store_b128(v4u{fbits(s0), tag, fbits(s1), tag}, rs, WS_Q + ((h * 8 + j) * DH + lane + 64 * k) * 16, 0, 16);
      }
      if (lane == 0) CT[1] = 1u;
      SSTAMP(13);
    }
    const int g4 = lane >> 4, tq = (lane & 15) >> 2, tp = lane & 3;
    {
      const unsigned lb = (unsigned)(uintptr_t)L;
      const unsigned char* wo = (const unsigned char*)a.wo + (size_t)h * DH * (DM * 2) + j * 160;
#pragma unroll 1
      for (int k = 0; k < 30; ++k) { const int c = k * 64 + lane, r = c / 10; dma16<true>(wo, r * (DM * 2) + (c - r * 10) * 16, lb + L_WO + k * 1024); }
      if (ffn) {
        const size_t o0 = (size_t)80 * j * (FF * 2) + cg * 128;
        const int vo = (lane >> 3) * (FF * 2) + (lane & 7) * 16;
#pragma unroll 1
        for (int k = 0; k < 10; ++k) {
          dma16<true>((const unsigned char*)a.wd + (size_t)f * 8 * (DM * 2) + k * 1024, lane * 16, lb + L_WD + k * 1024);
          dma16<true>((const unsigned char*)a.wg + o0 + k * 8 * FF * 2, vo, lb + L_G + k * 1024);
          dma16<true>((const unsigned char*)a.wu + o0 + k * 8 * FF * 2, vo, lb + L_G + 10240 + k * 1024);
        }
      }
    }
    BARRIER();
    SSTAMP(3);
    waitvm<0>();
    {
      v4u fr[30];
#pragma unroll
      for (int k = 0; k < 30; ++k) {
        LAS unsigned char* p = L + L_WO + (32 * (k % 6) + 8 * g4 + tq) * 160 + (16 * (k / 6) + 4 * tp) * 2;
        const v2u lo = trread(p), hi = trread(p + 4 * 160);
        fr[k] = v4u{lo.x, lo.y, hi.x, hi.y};
      }
      asm volatile("s_waitcnt lgkmcnt(0)" ::: "memory");
#pragma unroll
      for (int k = 0; k < 30; ++k) *(LAS v4u*)(L + L_WO + k * 1024 + lane * 16) = fr[k];
#pragma unroll
      for (int k = 0; k < 24; ++k) {
        const int row = 32 * (k % 3) + 8 * g4 + tq;
        LAS unsigned char* p = L + L_G + (tp >> 1) * 10240 + (row < 80 ? row : tq) * 128 + (8 * (k / 3) + 4 * (tp & 1)) * 2;
        const v2u lo = trread(p), hi = trread(p + 4 * 128);
        fr[k] = v4u{lo.x, lo.y, hi.x, hi.y};
      }
      asm volatile("s_waitcnt lgkmcnt(0)" ::: "memory");
#pragma unroll
      for (int k = 0; k < 24; ++k) *(LAS v4u*)(L + L_G + k * 1024 + lane * 16) = fr[k];
#pragma unroll
      for (int T = 0; T < 10; ++T) {
        LAS unsigned char* p = L + L_WD + tq * 1280 + (64 * T + 16 * g4 + 4 * tp) * 2;
        const v2u lo = trread(p), hi = trread(p + 4 * 1280);
        fr[T] = v4u{lo.x, lo.y, hi.x, hi.y};
      }
      asm volatile("s_waitcnt lgkmcnt(0)" ::: "memory");
#pragma unroll
      for (int T = 0; T < 10; ++T) *(LAS v4u*)(L + L_WD + T * 1024 + lane * 16) = fr[T];
    }
    int filled = 0;
    for (int spin = 0; spin < 16 * SPIN_CAP; ++spin) {
      int need = (int)__builtin_amdgcn_readfirstlane(CT[0]) + LEAD - (STAT + filled);
      if (need > 0) {
        if (filled == 0) {
          unsigned keep;
#pragma unroll 1
          for (unsigned o = 0; o < len; o += 1024)
            asm volatile("s_mov_b32 %0, m0\n\ts_mov_b32 m0, %3\n\ts_nop 2\n\tbuffer_load_dwordx4 %1, %2, %4 offen lds\n\ts_mov_b32 m0, %0"
                         : "=&s"(keep) : "v"(lane * 16), "s"(rc), "s"((unsigned)(uintptr_t)L + L_PF), "s"(o) : "memory");
        }
        need = need > 32 ? 32 : need;
        unsigned old = 0;
        if (lane == 0) old = __hip_atomic_fetch_add(ctr, (unsigned)need, __ATOMIC_RELAXED, __HIP_MEMORY_SCOPE_AGENT);
        old = __builtin_amdgcn_readfirstlane(old);
        const bool done = old + need >= (unsigned)POOL;
        if (lane < (done ? need + NW : need)) DYN[filled + lane] = (lane < need && old + lane < (unsigned)POOL) ? B1OFF + (8 * S1 + old + lane) * ITEMB : END;
        filled += need;
        if (done) break;
      } else __builtin_amdgcn_s_sleep(2);
    }
    waitvm<0>();
    __builtin_amdgcn_s_setprio(0);
  } else {
    asm volatile("; STREAM_ENTRY");
    if (w == NW - 1) {
      hint(CT + 1, QDELAY);
      v4u g[24];
      gather<24>(rs, WS_Q + (h * 8 * DH + lane) * 16, tag, g);
      SSTAMP(13);
      LAS unsigned short* QS = (LAS unsigned short*)(L + L_QS);
#pragma unroll
      for (int k = 0; k < 3; ++k) {
        float s0 = 0.f, s1 = 0.f;
#pragma unroll
        for (int p = 0; p < 8; ++p) { s0 += ubits(g[p * 3 + k].x); s1 += ubits(g[p * 3 + k].z); }
        const unsigned u = pk(s0, s1);
        QS[lane + 64 * k] = (unsigned short)u; QS[DH + lane + 64 * k] = (unsigned short)(u >> 16);
      }
    }
#pragma unroll
    for (int e = 1; e < NEXTRA; ++e) {
      if (w != NW - 1) waitvm<PACE>();
      ld3_first(ke[e], voff, rk, so0 + (e + 1) * NS * ITEMB);
      if (w != NW - 1) waitvm<PACE>();
      ld3_first(ve[e], voff, rv, so0 + (e + 1) * NS * ITEMB);
    }
    BARRIER();
    SSTAMP(3);
    v4u q[3];
#pragma unroll
    for (int jj = 0; jj < 3; ++jj) q[jj] = *(const LAS v4u*)(L + L_QS + ((lane & 7) + 8 * jj) * 16);
    Soft st;
    st.m = M_INIT; st.l = 0.f;
#pragma unroll
    for (int k = 0; k < 24; ++k) st.acc[k] = 0.f;
    auto step = [&](unsigned nxt) {
      wait3<3>(km);
      float s = score(km, q);
      asm volatile("" : "+v"(s));
      ld3_again(km, voff, rk, nxt);
      const float p = soft(st, s);
      wait3<3>(vm);
      pv(st, p, vm);
      __builtin_amdgcn_sched_barrier(0);
      ld3_again(vm, voff, rv, nxt);
    };
#pragma unroll
    for (int e = 0; e < NEXTRA; ++e) {
      step(so0 + (NEXTRA + 1 + e) * NS * ITEMB);
      pin3(ke[e]); pin3(ve[e]);
      const float pe = soft(st, score(ke[e], q));
      pv(st, pe, ve[e]);
    }
    unsigned cur = so0;
#pragma unroll 1
    for (int b = 0; b < 2; ++b) {
      while (cur != END && (cur >= B1OFF) == (b == 1)) {
        unsigned t = 0;
        if (lane == 0) t = __hip_atomic_fetch_add((LAS unsigned*)(L + L_CT), 1u, __ATOMIC_RELAXED, __HIP_MEMORY_SCOPE_WORKGROUP);
        t = __builtin_amdgcn_readfirstlane(t);
        if (t < (unsigned)STAT) cur = (t < (unsigned)S0 ? sb0 : sb1) + t * ITEMB;
        else {
          int spin = 0;
          while ((cur = __builtin_amdgcn_readfirstlane(DYN[t - STAT])) == EMPTY) {
            __builtin_amdgcn_s_sleep(1);
            if (++spin > SPIN_CAP) { cur = END; break; }
          }
        }
        step(cur);
      }
      wait3<0>(km); wait3<0>(vm);
      if (GPOS == 0 && b == 1) {
        unsigned t = 0;
        if (lane == 0) t = __hip_atomic_fetch_add((LAS unsigned*)(L + L_CT) + 4, 1u, __ATOMIC_RELAXED, __HIP_MEMORY_SCOPE_WORKGROUP);
        if (__builtin_amdgcn_readfirstlane(t) == NS - 1) __builtin_amdgcn_raw_buffer_store_b32(tag, rs, lane == 0 ? WS_G + wg * 128 : OOB, 0, 16);
      }
      float M = fmaxf(st.m, dpp<ROR8>(st.m)); M = sw32max(sw16max(M));
      const float sc = __builtin_amdgcn_exp2f(st.m - M);
      float l = st.l * sc; l += dpp<ROR8>(l); l = sw16add(l, l); l = sw32add(l, l);
      float t1[12], t2[6];
#pragma unroll
      for (int k = 0; k < 12; ++k) t1[k] = sw16add(st.acc[k] * sc, st.acc[k + 12] * sc);
#pragma unroll
      for (int k = 0; k < 6; ++k) { t2[k] = sw32add(t1[k], t1[k + 6]); t2[k] += dpp<ROR8>(t2[k]); }
      LAS float* rec = (LAS float*)(L + L_REC) + ((w - 1) * 2 + b) * RECF;
      if (!(lane & 8)) {
        const int a0 = 6 * hf + 12 * ((lane >> 4) & 1);
#pragma unroll
        for (int k = 0; k < 6; k += 2) { const int ai = a0 + k; *(LAS v2f*)(rec + 4 + ((lane & 7) + 8 * (ai >> 3)) * 8 + (ai & 7)) = v2f{t2[k], t2[k + 1]}; }
      }
      if (lane == 0) *(LAS v2f*)rec = v2f{M, l};
#pragma unroll
      for (int jj = 0; jj < 3; ++jj) q[jj] = *(const LAS v4u*)(L + L_QS + DH * 2 + ((lane & 7) + 8 * jj) * 16);
      st.m = M_INIT; st.l = 0.f;
#pragma unroll
      for (int k = 0; k < 24; ++k) st.acc[k] = 0.f;
    }
  }
  STAMP(4);
  const int g4 = lane >> 4, c16 = lane & 15;
  const v4f ctag = {0.f, ubits(tag), 0.f, ubits(tag)};
  const v4u zero4 = {0u, 0u, 0u, 0u};
  BARRIER();
  STAMP(5);
  const bool tilew = w == 0 || w >= 4;
  const int tile = w ? w - 3 : 0;
  v4u bw[6]; unsigned xv0 = 0, xv1 = 0;
  if (!tilew) {
    const int dim = tid - 64;
    if (GPOS == 1) __builtin_amdgcn_raw_buffer_store_b32(tag, rs, dim == 0 ? WS_G + wg * 128 : OOB, 0, 16);
    {
      const LAS float* rec = (const LAS float*)(L + L_REC);
      v2f ml = *(const LAS v2f*)(rec + (c16 < 2 * NS ? c16 : 0) * RECF);
      float av[2 * NS];
#pragma unroll
      for (int r = 0; r < 2 * NS; ++r) av[r] = rec[r * RECF + 4 + dim];
      if (c16 >= 2 * NS) ml = v2f{M_INIT, 0.f};
      float M = fmaxf(ml.x, dpp<XOR2>(ml.x)); M = fmaxf(M, dpp<ROR4>(M)); M = fmaxf(M, dpp<ROR8>(M));
      const float e = __builtin_amdgcn_exp2f(ml.x - M);
      float ls = e * ml.y; ls += dpp<XOR2>(ls); ls += dpp<ROR4>(ls); ls += dpp<ROR8>(ls);
      float o0 = 0.f, o1 = 0.f;
#pragma unroll
      for (int r = 0; r < 2 * NS; r += 2) { o0 = fmaf(rdlane(e, r), av[r], o0); o1 = fmaf(rdlane(e, r + 1), av[r + 1], o1); }
      if (GPOS == 2) __builtin_amdgcn_raw_buffer_store_b32(tag, rs, dim == 0 ? WS_G + wg * 128 : OOB, 0, 16);
      put(rs, WS_P + wg * PBLK + dim * 16, fbits(o0), fbits(o1), tag);
      const float lsx = dpp<XOR1>(ls), Mx = dpp<XOR1>(M);
      put(rs, dim < 2 ? WS_P + wg * PBLK + (DH + dim) * 16 : OOB, fbits((lane & 1) ? lsx : M), fbits((lane & 1) ? ls : Mx), tag);
      STAMP(6);
    }
    v4u g[8], gm = zero4;
#pragma unroll
    for (int p = 0; p < 8; ++p) g[p] = zero4;
    const int po = WS_P + h * 8 * PBLK + dim * 16, pm = WS_P + (h * 8 + ((lane >> 1) & 7)) * PBLK + (DH + (lane & 1)) * 16;
    for (unsigned sent = 0, spin = 0; sent != 0xffu && spin < 4 * SPIN_CAP; ++spin) {
      const unsigned want = __builtin_amdgcn_readfirstlane(CT[5]) & ~sent & 0xffu;
#pragma unroll
      for (int p = 0; p < 8; ++p)
        if ((want >> p) & 1u) g[p] = __builtin_amdgcn_raw_buffer_load_b128(rs, po, p * PBLK, 16);
      sent |= want;
    }
    for (int spin = 0; spin < SPIN_CAP; ++spin) {
      gm = __builtin_amdgcn_raw_buffer_load_b128(rs, pm, 0, 16);
      bool ok = true;
#pragma unroll
      for (int p = 0; p < 8; ++p)
        if (!__all(g[p].y == tag && g[p].w == tag)) { g[p] = __builtin_amdgcn_raw_buffer_load_b128(rs, po, p * PBLK, 16); ok = false; }
      if (ok && __all(gm.y == tag && gm.w == tag)) break;
      asm volatile("" ::: "memory");
    }
    STAMP(7);
    float M0 = ubits(gm.x), M1 = ubits(gm.z);
    M0 = fmaxf(M0, dpp<XOR2>(M0)); M1 = fmaxf(M1, dpp<XOR2>(M1)); M0 = fmaxf(M0, dpp<ROR4>(M0)); M1 = fmaxf(M1, dpp<ROR4>(M1)); M0 = fmaxf(M0, dpp<ROR8>(M0)); M1 = fmaxf(M1, dpp<ROR8>(M1));
    const float e0 = __builtin_amdgcn_exp2f(ubits(gm.x) - M0), e1 = __builtin_amdgcn_exp2f(ubits(gm.z) - M1);
    float l0 = e0 * dpp<XOR1>(ubits(gm.x)), l1 = e1 * dpp<XOR1>(ubits(gm.z));
    l0 += dpp<XOR2>(l0); l1 += dpp<XOR2>(l1); l0 += dpp<ROR4>(l0); l1 += dpp<ROR4>(l1); l0 += dpp<ROR8>(l0); l1 += dpp<ROR8>(l1);
    float o0 = 0.f, o1 = 0.f;
#pragma unroll
    for (int p = 0; p < 8; ++p) { o0 = fmaf(rdlane(e0, 2 * p), ubits(g[p].x), o0); o1 = fmaf(rdlane(e1, 2 * p), ubits(g[p].z), o1); }
    const unsigned ob = pk(o0 * __builtin_amdgcn_rcpf(rdlane(l0, 0)), o1 * __builtin_amdgcn_rcpf(rdlane(l1, 0)));
    ((LAS unsigned short*)(L + L_O))[dim] = (unsigned short)ob; ((LAS unsigned short*)(L + L_O))[DH + dim] = (unsigned short)(ob >> 16);
  } else {
#pragma unroll
    for (int k = 0; k < 6; ++k) bw[k] = *(const LAS v4u*)(L + L_WO + (tile * 6 + k) * 1024 + lane * 16);
    xv0 = a.x[80 * j + 16 * tile + c16]; xv1 = a.x[DM + 80 * j + 16 * tile + c16];
    asm volatile("" : "+v"(xv0), "+v"(xv1));
    if (w == NW - 1) { watch<8>(rs, WS_G + h * 8 * 128, 128, lane, tag, CT + 5); STAMP(13); }
  }
  BARRIER();
  STAMP(1);
  if (tilew) {
    const int ybase = WS_Y + (j * 5 + tile) * (NH * 256);
    if (DPOS == 0) __builtin_amdgcn_raw_buffer_store_b32(tag, rs, tid == 0 ? WS_D + wg * 128 : OOB, 0, 16);
    v4f d = ctag;
#pragma unroll
    for (int k = 0; k < 6; ++k) {
      const v4u ao = *(const LAS v4u*)(L + L_O + (c16 & 2) * DH + (32 * k + 8 * g4) * 2);
      d = mfma((c16 & 1) ? zero4 : ao, bw[k], d);
    }
    if (DPOS == 1) { asm volatile("" : "+v"(d)); __builtin_amdgcn_raw_buffer_store_b32(tag, rs, tid == 0 ? WS_D + wg * 128 : OOB, 0, 16); }
    __builtin_amdgcn_raw_buffer_store_b128(__builtin_bit_cast(v4u, d), rs, lane < 16 ? ybase + h * 256 + c16 * 16 : OOB, 0, 16);
    STAMP(8);
    if (ffn) {
      v4u g[8];
#pragma unroll
      for (int k = 0; k < 8; ++k) g[k] = zero4;
      for (unsigned sent = 0, spin = 0; sent != 0x11111111u && spin < 4 * SPIN_CAP; ++spin) {
        unsigned m = __builtin_amdgcn_readfirstlane(CT[6]);
        m &= m >> 1; m &= m >> 2; m &= 0x11111111u & ~sent;
#pragma unroll
        for (int k = 0; k < 8; ++k)
          if ((m >> (4 * k)) & 1u) g[k] = __builtin_amdgcn_raw_buffer_load_b128(rs, ybase + lane * 16 + (k & 3) * 1024, (k >> 2) * 4096, 16);
        sent |= m;
      }
      for (int spin = 0; spin < SPIN_CAP; ++spin) {
        bool ok = true;
#pragma unroll
        for (int k = 0; k < 8; ++k)
          if (!__all(g[k].y == tag && g[k].w == tag)) { g[k] = __builtin_amdgcn_raw_buffer_load_b128(rs, ybase + lane * 16 + (k & 3) * 1024, (k >> 2) * 4096, 16); ok = false; }
        if (ok) break;
        asm volatile("" ::: "memory");
      }
      STAMP(2);
      const float s0 = ((ubits(g[0].x) + ubits(g[1].x)) + (ubits(g[2].x) + ubits(g[3].x))) + ((ubits(g[4].x) + ubits(g[5].x)) + (ubits(g[6].x) + ubits(g[7].x)));
      const float s1 = ((ubits(g[0].z) + ubits(g[1].z)) + (ubits(g[2].z) + ubits(g[3].z))) + ((ubits(g[4].z) + ubits(g[5].z)) + (ubits(g[6].z) + ubits(g[7].z)));
      float s = sw16add(s0, s1); s = sw32add(s, s);
      const float hv = bflo((g4 & 1) ? xv1 : xv0) + bfr(s);
      if (lane < 32) ((LAS unsigned short*)(L + L_HS))[g4 * 96 + 16 * tile + c16] = (unsigned short)pk(hv, 0.f);
    }
  } else if (ffn && w == 1) { watch<32>(rs, WS_D + j * 128, 8 * 128, lane, tag, CT + 6); STAMP(14); }
  if (ffn) {
    v4u bg[3], bdn[3]; int zo[3];
#pragma unroll
    for (int k = 0; k < 3; ++k) bg[k] = *(const LAS v4u*)(L + L_G + (w * 3 + k) * 1024 + lane * 16);
    const int u = (w - 1) & 3;
#pragma unroll
    for (int T = 0; T < 3; ++T) {
      const int dt = u + 4 * T, n = 64 * dt + lane;
      bdn[T] = *(const LAS v4u*)(L + L_WD + (dt < 10 ? dt : 0) * 1024 + lane * 16);
      zo[T] = dt < 10 ? WS_Z + ((n >> 2) * NFFN + f) * 64 + (n & 3) * 16 : OOB;
      asm volatile("" : "+v"(zo[T]));
    }
    int eo = WS_E + (cg * 8 + w) * 5120;
    const int eo0 = lane < 32 ? eo + j * 512 + lane * 16 : OOB, eo1 = lane < 8 ? eo + 4096 + j * 128 + lane * 16 : OOB;
    BARRIER();
    STAMP(3);
    {
      v4f d = ctag, ds = ctag;
#pragma unroll
      for (int k = 0; k < 3; ++k) {
        v4u ah = *(const LAS v4u*)(L + L_HS + (c16 < 5 ? c16 >> 1 : 0) * 192 + (32 * k + 8 * g4) * 2);
        if ((c16 & 1) || c16 > 4) ah = zero4;
        d = mfma(ah, bg[k], d); ds = mfma(ah, ah, ds);
      }
      __builtin_amdgcn_raw_buffer_store_b128(__builtin_bit_cast(v4u, d), rs, eo0, 0, 16);
      __builtin_amdgcn_raw_buffer_store_b128(__builtin_bit_cast(v4u, ds), rs, eo1, 0, 16);
    }
    STAMP(9);
    if (w >= 1 && w <= 4) {
      v4u g[5];
      __builtin_amdgcn_s_sleep(EDELAY);
      gather<5>(rs, WS_E + f * 5120 + lane * 16, tag, g);
      if (FPOS == 0) __builtin_amdgcn_raw_buffer_store_b32(tag, rs, (tid == 64 && j == 0) ? WS_F + cg * 128 : OOB, 0, 16);
      STAMP(10);
      const float s0 = (ubits(g[0].x) + ubits(g[1].x)) + (ubits(g[2].x) + ubits(g[3].x)), s1 = (ubits(g[0].z) + ubits(g[1].z)) + (ubits(g[2].z) + ubits(g[3].z));
      const float gs = sw32add(s0, s1);
      const float cs0 = sw32add(s0, s0);
      const auto cx = __builtin_amdgcn_permlane16_swap(fbits(cs0), fbits(cs0), false, false);
      const auto gx = __builtin_amdgcn_permlane16_swap(fbits(gs), fbits(gs), false, false);
      float t0 = ubits(g[4].x), t1 = ubits(g[4].z);
      t0 += dpp<ROR8>(t0); t1 += dpp<ROR8>(t1);
      t0 = sw16add(t0, t0); t1 = sw16add(t1, t1);
      const float tt = sw32add(t0, t1) * (1.0f / DM);
      const float mean = dpp<BC + 4>(tt), q0 = dpp<BC + 0>(tt), q1 = dpp<BC + 2>(tt);
      const float rdv = ln_rden(fmaxf((hf ? q1 : q0) - mean * mean, 0.f));
      const float gv = bfr((ubits(gx[0]) - bfr(mean) * ubits(cx[1])) * rdv), uv = dpp<ROR8>(gv);
      const float y2 = (-2.0f * 0.7978845608028654f * 1.4426950408889634f) * (gv + 0.044715f * gv * gv * gv);
      const float act = bfr(gv * __builtin_amdgcn_rcpf(1.0f + __builtin_amdgcn_exp2f(y2))) * uv;
      const unsigned ap = pk(act, dpp<XOR1>(act));
      const auto ax = __builtin_amdgcn_permlane32_swap(ap, ap, false, false);
      const unsigned keep = ((c16 >> 2) == g4 && !(c16 & 1)) ? 0xffffffffu : 0u, k1 = (c16 & 2) ? keep : 0u, k0 = keep ^ k1;
      const v4u b0 = {dppu<BC + 0>(ax[0]), dppu<BC + 2>(ax[0]), dppu<BC + 4>(ax[0]), dppu<BC + 6>(ax[0])};
      const v4u b1 = {dppu<BC + 0>(ax[1]), dppu<BC + 2>(ax[1]), dppu<BC + 4>(ax[1]), dppu<BC + 6>(ax[1])};
      v4u a2 = (b0 & k0) | (b1 & k1);
      if (FPOS == 1) { asm volatile("" : "+v"(a2)); __builtin_amdgcn_raw_buffer_store_b32(tag, rs, (tid == 64 && j == 0) ? WS_F + cg * 128 : OOB, 0, 16); }
#pragma unroll
      for (int T = 0; T < 3; ++T) __builtin_amdgcn_raw_buffer_store_b128(__builtin_bit_cast(v4u, mfma(a2, bdn[T], ctag)), rs, zo[T], 0, 16);
      STAMP(11);
    } else if (w == NW - 1 && cg < 20) {
      const __amdgpu_buffer_rsrc_t ro = __builtin_amdgcn_make_buffer_rsrc((void*)a.out, 0, 2 * DM * 2, 0x00020000);
      const float hv = bflo(((const LAS unsigned short*)(L + L_HS))[(g4 & 1) * 96 + 4 * cg + (lane & 3)]);
      v4u g[12];
      watch<24>(rs, WS_F, 128, lane, tag, CT + 7);
      STAMP(15);
      __builtin_amdgcn_s_sleep(ZDELAY);
      gather<12>(rs, WS_Z + (20 * j + cg) * (NFFN * 64) + lane * 16, tag, g);
      float s0[4], s1[4];
#pragma unroll
      for (int k = 0; k < 4; ++k) { s0[k] = (ubits(g[k].x) + ubits(g[k + 4].x)) + ubits(g[k + 8].x); s1[k] = (ubits(g[k].z) + ubits(g[k + 4].z)) + ubits(g[k + 8].z); }
      float s = sw16add((s0[0] + s0[1]) + (s0[2] + s0[3]), (s1[0] + s1[1]) + (s1[2] + s1[3]));
      s = sw32add(s, s); s += dpp<ROR4>(s); s += dpp<ROR8>(s);
      const unsigned o = pk(hv + bfr(s), 0.f);
      __builtin_amdgcn_raw_buffer_store_b16((unsigned short)o, ro, (lane < 32 && c16 < 4) ? (g4 * DM + 80 * j + 4 * cg + c16) * 2 : OOB, 0, 0);
      __builtin_amdgcn_raw_buffer_store_b32(tag, rs, (wg == 0 && lane == 0) ? 0 : OOB, 0, 0);
      STAMP(12);
    }
  }
  STAMP_DUMP();
  asm volatile(".Ldb_end:");
}
}

extern "C" void kernel_launch(void* const* d_in, const int* in_sizes, int n_in, void* d_out, int out_size, void* d_ws, size_t ws_size, hipStream_t stream) {
  Args a;
  a.x = (const unsigned short*)d_in[0]; a.k = (const unsigned short*)d_in[1]; a.v = (const unsigned short*)d_in[2];
  a.wq = (const unsigned short*)d_in[3]; a.wo = (const unsigned short*)d_in[4];
  a.wg = (const unsigned short*)d_in[6]; a.wu = (const unsigned short*)d_in[7]; a.wd = (const unsigned short*)d_in[8];
  a.out = (unsigned short*)d_out; a.ws = (unsigned char*)d_ws;
  hipStreamCaptureStatus cs = hipStreamCaptureStatusNone;
  (void)hipStreamIsCapturing(stream, &cs);
  if (cs == hipStreamCaptureStatusNone) {
    printf("DIAG ptrs:"); for (int i = 0; i < 9; ++i) printf(" %p", d_in[i]); printf(" out %p ws %p\n", d_out, d_ws);
    hipPointerAttribute_t at; if (hipPointerGetAttributes(&at, d_ws) == hipSuccess) printf("DIAG ws type %d flags %u\n", (int)at.type, at.allocationFlags);
    if (hipPointerGetAttributes(&at, d_in[1]) == hipSuccess) printf("DIAG k type %d flags %u\n", (int)at.type, at.allocationFlags);
    std::vector<unsigned long long> hs(256 * 8 * 16);
    hipEvent_t e0, e1; (void)hipEventCreate(&e0); (void)hipEventCreate(&e1);
    for (int it = 0; it < 8; ++it) {
      (void)hipMemsetAsync(d_ws, 0x5a + it, ws_size, stream);
      (void)hipMemsetAsync((char*)d_ws + 0xC00000, 0, hs.size() * 8, stream);
      (void)hipEventRecord(e0, stream);
      hipLaunchKernelGGL(decode_block, dim3(NWG), dim3(NTHR), 0, stream, a);
      (void)hipEventRecord(e1, stream);
      (void)hipStreamSynchronize(stream);
      float ms = 0; (void)hipEventElapsedTime(&ms, e0, e1);
      (void)hipMemcpy(hs.data(), (char*)d_ws + 0xC00000, hs.size() * 8, hipMemcpyDeviceToHost);
      unsigned long long t0 = ~0ull; for (int k = 0; k < 2048; ++k) if (hs[k * 16]) t0 = std::min(t0, hs[k * 16]);
      printf("DIAG ev %.1f |", ms * 1000.f);
      for (int n = 1; n < 16; ++n) { unsigned long long mx = 0, mn = ~0ull; for (int k = 0; k < 2048; ++k) if (hs[k * 16 + n]) { mx = std::max(mx, hs[k * 16 + n]); mn = std::min(mn, hs[k * 16 + n]); }
        if (mx) printf(" %d:%.1f-%.1f", n, (mn - t0) * 0.01, (mx - t0) * 0.01); }
      printf("\n");
    }
    fflush(stdout);
  }
  hipLaunchKernelGGL(decode_block, dim3(NWG), dim3(NTHR), 0, stream, a);
}
```

```cpp
#define VH1 22
#define VH0 99
#define KH1 99
#include <hip/hip_runtime.h>
#include <hip/hip_bf16.h>
#include <stdint.h>

#define PACE 3
#define NEX 5
#define POLLSLEEP 1
#define REWARM 1
#define STAT1 192
#define LEADN 8
#define WSLEEP 1
#define GPOS 0
#define DPOS 0
#define FPOS 0
#define NACT 5
#define GRAB 1
#define GRABEND 128
#define WPOS 1
#define QDELAY 10
#define EDELAY 10
#define ZDELAY 0
#define MIMIC_SCORE 1

namespace {
typedef unsigned v4u __attribute__((ext_vector_type(4)));
typedef unsigned v2u __attribute__((ext_vector_type(2)));
typedef float v4f __attribute__((ext_vector_type(4)));
typedef float v2f __attribute__((ext_vector_type(2)));
typedef short v4s __attribute__((ext_vector_type(4)));
typedef short v8s __attribute__((ext_vector_type(8)));
typedef __bf16 bf2 __attribute__((ext_vector_type(2)));
typedef __bf16 bf8 __attribute__((ext_vector_type(8)));
#define LAS __attribute__((address_space(3)))
#define CAS __attribute__((address_space(4)))

constexpr int DM = 640, NH = 32, SK = 16384, DH = 192, FF = 1536;
constexpr int NWG = 256, NW = 8, NS = NW - 1, NTHR = NW * 64;
constexpr unsigned ITEMB = 8 * DH * 2;
constexpr unsigned HEADB = (unsigned)SK * DH * 2, B1OFF = NH * HEADB;
constexpr int IPH = SK / 8;
constexpr int S0 = IPH / 8, S1 = STAT1, STAT = S0 + S1;
constexpr int POOL = IPH - 8 * S1, LEAD = LEADN, NEXTRA = NEX;
constexpr unsigned END = 0xF0000000u, EMPTY = 0xFFFFFFFFu;
constexpr int NRED = DM / 4, NFFN = FF / 8;
constexpr int SPIN_CAP = 1 << 17;
constexpr float M_INIT = -3.0e38f;

constexpr int WS_CTR = 4096, CTR_STRIDE = 4352;
constexpr int WS_F = 0x24000, WS_D = 0x28000, WS_G = 0x30000, WS_Q = 0x40000, WS_P = 0x100000, PBLK = 3200, WS_Y = 0x200000, WS_E = 0x280000, WS_Z = 0x400000, WS_BYTES = 0x600000;
constexpr int OOB = (int)0x80000000u;
static_assert(WS_CTR + NH * CTR_STRIDE <= WS_F && WS_F + 24 * 128 <= WS_D && WS_D + NWG * 128 <= WS_G && WS_G + NWG * 128 <= WS_Q && WS_Q + NWG * DH * 16 <= WS_P && WS_P + NWG * PBLK <= WS_Y, "ws map");
static_assert(WS_Y + NWG * 80 * 16 <= WS_E && WS_E + NFFN * 5120 <= WS_Z && WS_Z + NRED * NFFN * 64 <= WS_BYTES, "ws map");

constexpr int L_WO = 0, L_G = L_WO + DH * 160, L_WD = L_G + 2 * 80 * 128, L_QP = L_WD + 8 * 1280;
constexpr int L_QS = L_QP + NW * 2 * DH * 4, RECF = DH + 4, L_REC = L_QS + 2 * DH * 2, L_O = L_REC + NS * 2 * RECF * 4, L_HS = L_O + 2 * DH * 2;
constexpr int L_CT = L_HS + 3 * 192, DYNMAX = POOL + 96, L_DYN = L_CT + 64, L_PF = L_DYN + DYNMAX * 4, L_BYTES = L_PF + NW * 1024;
static_assert(L_BYTES > 81920 && L_BYTES <= 160 * 1024 && L_REC % 16 == 0 && L_O % 16 == 0 && L_HS % 16 == 0 && L_PF % 16 == 0, "one workgroup per CU");

struct Args {
  const unsigned short *x, *k, *v, *wq, *wo, *wg, *wu, *wd;
  unsigned short* out;
  unsigned char* ws;
};

__device__ __forceinline__ float dot2(unsigned a, unsigned b, float c) {
  return __builtin_amdgcn_fdot2_f32_bf16(__builtin_bit_cast(bf2, a), __builtin_bit_cast(bf2, b), c, false);
}
template <int CTRL> __device__ __forceinline__ float dpp(float x) {
  return __builtin_bit_cast(float, __builtin_amdgcn_mov_dpp(__builtin_bit_cast(int, x), CTRL, 0xf, 0xf, true));
}
template <int CTRL> __device__ __forceinline__ unsigned dppu(unsigned x) { return (unsigned)__builtin_amdgcn_mov_dpp((int)x, CTRL, 0xf, 0xf, true); }
constexpr int BC = 0x150;
constexpr int XOR1 = 0xB1, XOR2 = 0x4E, HMIR = 0x141, ROR4 = 0x124, ROR8 = 0x128;
__device__ __forceinline__ float bflo(unsigned x) { return __builtin_bit_cast(float, x << 16); }
__device__ __forceinline__ float bfhi(unsigned x) { return __builtin_bit_cast(float, x & 0xffff0000u); }
__device__ __forceinline__ unsigned pk(float lo, float hi) {
  const bf2 r = {(__bf16)lo, (__bf16)hi};
  return __builtin_bit_cast(unsigned, r);
}
__device__ __forceinline__ float bfr(float x) { return (float)(__bf16)x; }
__device__ __forceinline__ unsigned fbits(float x) { return __builtin_bit_cast(unsigned, x); }
__device__ __forceinline__ float ubits(unsigned x) { return __builtin_bit_cast(float, x); }
__device__ __forceinline__ float sw16add(float a, float b) {
  const auto s = __builtin_amdgcn_permlane16_swap(fbits(a), fbits(b), false, false);
  return ubits(s[0]) + ubits(s[1]);
}
__device__ __forceinline__ float sw32add(float a, float b) {
  const auto s = __builtin_amdgcn_permlane32_swap(fbits(a), fbits(b), false, false);
  return ubits(s[0]) + ubits(s[1]);
}
__device__ __forceinline__ float sw16max(float a) {
  const auto s = __builtin_amdgcn_permlane16_swap(fbits(a), fbits(a), false, false);
  return fmaxf(ubits(s[0]), ubits(s[1]));
}
__device__ __forceinline__ float sw32max(float a) {
  const auto s = __builtin_amdgcn_permlane32_swap(fbits(a), fbits(a), false, false);
  return fmaxf(ubits(s[0]), ubits(s[1]));
}
__device__ __forceinline__ float rowsum16(float s) { s += dpp<XOR1>(s); s += dpp<XOR2>(s); s += dpp<HMIR>(s); s += dpp<ROR8>(s); return s; }
__device__ __forceinline__ float rdlane(float x, int l) { return ubits(__builtin_amdgcn_readlane(fbits(x), l)); }
__device__ __forceinline__ v4u mkrsrc(const void* p, unsigned bytes) {
  const unsigned long long a = (unsigned long long)p;
  return v4u{(unsigned)a, (unsigned)(a >> 32) & 0xffffu, bytes, 0x00020000u};
}
__device__ __forceinline__ v4f mfma(v4u a, v4u b, v4f c) {
  return __builtin_amdgcn_mfma_f32_16x16x32_bf16(__builtin_bit_cast(bf8, a), __builtin_bit_cast(bf8, b), c, 0, 0, 0);
}
template <int STRIDE> __device__ __forceinline__ v4u bfrag(LAS unsigned char* p) {
  const v2u lo = __builtin_bit_cast(v2u, __builtin_amdgcn_ds_read_tr16_b64_v4i16((LAS v4s*)p));
  const v2u hi = __builtin_bit_cast(v2u, __builtin_amdgcn_ds_read_tr16_b64_v4i16((LAS v4s*)(p + 4 * STRIDE)));
  return v4u{lo.x, lo.y, hi.x, hi.y};
}
#define BARRIER() asm volatile("s_waitcnt lgkmcnt(0)\n\ts_barrier" ::: "memory")

#define LD3 "s_nop 4\n\tbuffer_load_dwordx4 %0, %3, %4, %5 offen nt\n\tbuffer_load_dwordx4 %1, %3, %4, %5 offen offset:128 nt\n\tbuffer_load_dwordx4 %2, %3, %4, %5 offen offset:256 nt"
__device__ __forceinline__ void ld_first(v4u& d, int voff, v4u rs, unsigned soff) {
  asm volatile("s_nop 4\n\tbuffer_load_dwordx4 %0, %1, %2, %3 offen nt" : "=v"(d) : "v"(voff), "s"(rs), "s"(soff));
}
__device__ __forceinline__ void ld3_first(v4u (&r)[3], int voff, v4u rs, unsigned so) { asm volatile(LD3 : "=&v"(r[0]), "=&v"(r[1]), "=&v"(r[2]) : "v"(voff), "s"(rs), "s"(so)); }
#define LD3D "s_nop 4\n\tbuffer_load_dwordx4 %0, %3, %4, %5 offen\n\tbuffer_load_dwordx4 %1, %3, %4, %5 offen offset:128\n\tbuffer_load_dwordx4 %2, %3, %4, %5 offen offset:256"
__device__ __forceinline__ void ld3_sel(v4u (&r)[3], int voff, v4u rs, unsigned so, unsigned thr) {
  asm volatile("s_cmp_ge_u32 %5, %6\n\ts_cbranch_scc1 1f\n\t" LD3 "\n\ts_branch 2f\n1:\n\t" LD3D "\n2:" : "+v"(r[0]), "+v"(r[1]), "+v"(r[2]) : "v"(voff), "s"(rs), "s"(so), "s"(thr) : "scc");
}
__device__ __forceinline__ void ld3_again(v4u (&r)[3], int voff, v4u rs, unsigned so) { asm volatile(LD3 : "+v"(r[0]), "+v"(r[1]), "+v"(r[2]) : "v"(voff), "s"(rs), "s"(so)); }
template <int N> __device__ __forceinline__ void wait3(v4u (&r)[3]) { asm volatile("s_waitcnt vmcnt(%3)" : "+v"(r[0]), "+v"(r[1]), "+v"(r[2]) : "i"(N)); }
__device__ __forceinline__ void pin3(v4u (&r)[3]) { asm volatile("" : "+v"(r[0]), "+v"(r[1]), "+v"(r[2])); }
template <int N> __device__ __forceinline__ void waitvm() { asm volatile("s_waitcnt vmcnt(%0)" : : "i"(N) : "memory"); }

template <bool NT> __device__ __forceinline__ void dma16(const void* base, int voff, unsigned lds) {
  unsigned keep;
  if (NT) asm volatile("s_mov_b32 %0, m0\n\ts_mov_b32 m0, %3\n\ts_nop 2\n\tglobal_load_lds_dwordx4 %1, %2 nt\n\ts_mov_b32 m0, %0" : "=&s"(keep) : "v"(voff), "s"(base), "s"(lds) : "memory");
  else asm volatile("s_mov_b32 %0, m0\n\ts_mov_b32 m0, %3\n\ts_nop 2\n\tglobal_load_lds_dwordx4 %1, %2\n\ts_mov_b32 m0, %0" : "=&s"(keep) : "v"(voff), "s"(base), "s"(lds) : "memory");
}

__device__ __forceinline__ void put(__amdgpu_buffer_rsrc_t rs, int off, unsigned a, unsigned b, unsigned tag) {
  __builtin_amdgcn_raw_buffer_store_b128(v4u{a, tag, b, tag}, rs, off, 0, 16);
}
#define STAMP(n)
#define SSTAMP(n)
#define STAMP_INIT()
#define STAMP_DUMP()
#define GDBG
#define GSTAMP()
#define GARG(n)
template <int N> __device__ __forceinline__ void gather(__amdgpu_buffer_rsrc_t rs, int off, unsigned tag, v4u (&t)[N] GDBG) {
  bool done = false;
#pragma unroll
  for (int k = 0; k < N; ++k) t[k] = v4u{0u, 0u, 0u, 0u};
  for (int spin = 0; spin < SPIN_CAP; ++spin) {
    if (!done) {
#pragma unroll
      for (int k = 0; k < N; ++k) t[k] = __builtin_amdgcn_raw_buffer_load_b128(rs, off + (k & 3) * 1024, (k >> 2) * 4096, 16);
      unsigned mn = tag, mx = tag;
#pragma unroll
      for (int k = 0; k < N; ++k) { mn = min(mn, min(t[k].y, t[k].w)); mx = max(mx, max(t[k].y, t[k].w)); }
      done = mn == mx;
    }
    GSTAMP();
    if (__all(done)) break;
    asm volatile("" ::: "memory");
    __builtin_amdgcn_s_sleep(POLLSLEEP);
  }
}

template <int N> __device__ __forceinline__ void watch(__amdgpu_buffer_rsrc_t rs, int off, int stride, int lane, unsigned tag, volatile LAS unsigned* mask) {
  bool seen = lane >= N;
  for (int spin = 0; spin < SPIN_CAP; ++spin) {
    if (!seen) seen = __builtin_amdgcn_raw_buffer_load_b32(rs, off + lane * stride, 0, 16) == tag;
    const unsigned long long m = __ballot(seen);
    if (lane == 0) *mask = (unsigned)m;
    if (m == ~0ull) break;
    asm volatile("" ::: "memory");
    __builtin_amdgcn_s_sleep(WSLEEP);
  }
  if (lane == 0) *mask = ~0u;
}
#define hint(p, D) do { for (int sp_ = 0; sp_ < SPIN_CAP && *(p) == 0u; ++sp_) __builtin_amdgcn_s_sleep(1); __builtin_amdgcn_s_sleep(D); } while (0)

__device__ __forceinline__ float ln_rden(float var) { return __builtin_amdgcn_rcpf(bfr(__builtin_amdgcn_sqrtf(bfr(bfr(var) + 1e-5f)))); }
__device__ __forceinline__ float ln_val(float x, float mean_b, float rden) { return bfr(bfr(x - mean_b) * rden); }

struct Soft { float m, l, acc[24]; };
__device__ __forceinline__ float score(const v4u (&kk)[3], const v4u (&q)[3]) {
  float s0 = 0.f, s1 = 0.f;
#pragma unroll
  for (int j = 0; j < 3; ++j) {
    s0 = dot2(kk[j].x, q[j].x, s0); s1 = dot2(kk[j].y, q[j].y, s1);
    s0 = dot2(kk[j].z, q[j].z, s0); s1 = dot2(kk[j].w, q[j].w, s1);
  }
  return s0 + s1;
}
__device__ __forceinline__ float soft(Soft& st, float s) {
  s += dpp<XOR1>(s); s += dpp<XOR2>(s); s += dpp<HMIR>(s);
  s = bfr(bfr(s) * 0.07216878364870322f) * 1.4426950408889634f;
  if (__any(s > st.m)) {
    const float mn = fmaxf(st.m, s), al = __builtin_amdgcn_exp2f(st.m - mn);
    st.l *= al;
#pragma unroll
    for (int k = 0; k < 24; ++k) st.acc[k] *= al;
    st.m = mn;
  }
  const float p = __builtin_amdgcn_exp2f(s - st.m);
  st.l += p;
  return p;
}
__device__ __forceinline__ void pv(Soft& st, float p, const v4u (&vv)[3]) {
#pragma unroll
  for (int j = 0; j < 3; ++j)
#pragma unroll
    for (int e = 0; e < 4; ++e) {
      st.acc[j * 8 + 2 * e] = fmaf(p, bflo(vv[j][e]), st.acc[j * 8 + 2 * e]);
      st.acc[j * 8 + 2 * e + 1] = fmaf(p, bfhi(vv[j][e]), st.acc[j * 8 + 2 * e + 1]);
    }
}

__global__ void __launch_bounds__(NTHR, 2) decode_block(Args a) {
  __shared__ __attribute__((aligned(16))) unsigned char smem[L_BYTES];
  LAS unsigned char* L = (LAS unsigned char*)smem;
  volatile LAS unsigned* CT = (volatile LAS unsigned*)(L + L_CT);
  volatile LAS unsigned* DYN = (volatile LAS unsigned*)(L + L_DYN);
  const int tid = threadIdx.x, lane = tid & 63, w = __builtin_amdgcn_readfirstlane(tid >> 6);
  const int wg = blockIdx.x, h = wg >> 3, j = wg & 7;
  const int hf = lane >> 5, l5 = lane & 31;
  const bool ffn = ((h + j) & 3) != 3;
  const int cg = 3 * (h >> 2) + (h & 3) - ((h & 3) > ((3 - j) & 3) ? 1 : 0), f = 8 * cg + j;
  const __amdgpu_buffer_rsrc_t rs = __builtin_amdgcn_make_buffer_rsrc((void*)a.ws, 0, WS_BYTES, 0x00020000);
  unsigned* ctr = (unsigned*)(a.ws + WS_CTR + h * CTR_STRIDE);
  STAMP_INIT();
  STAMP(0);

  auto load_weights = [&]() {
    const unsigned lb = (unsigned)(uintptr_t)L;
    const unsigned char* wo = (const unsigned char*)a.wo + (size_t)h * DH * (DM * 2) + j * 160;
    int vo = (lane / 10) * (DM * 2) + (lane % 10) * 16, c10 = lane % 10;
#pragma unroll 2
    for (int k = 0; k < 30; ++k) {
      dma16<true>(wo, vo, lb + L_WO + k * 1024);
      c10 += 4; vo += 6 * (DM * 2) + 64;
      if (c10 >= 10) { c10 -= 10; vo += DM * 2 - 160; }
    }
    if (ffn) {
      const size_t o0 = (size_t)80 * j * (FF * 2) + cg * 128;
      const int vg = (lane >> 3) * (FF * 2) + (lane & 7) * 16;
#pragma unroll 1
      for (int k = 0; k < 10; ++k) {
        dma16<true>((const unsigned char*)a.wg + o0 + k * 8 * FF * 2, vg, lb + L_G + k * 1024);
        dma16<true>((const unsigned char*)a.wu + o0 + k * 8 * FF * 2, vg, lb + L_G + 10240 + k * 1024);
        dma16<true>((const unsigned char*)a.wd + (size_t)f * 8 * (DM * 2) + k * 1024, lane * 16, lb + L_WD + k * 1024);
      }
    }
  };
  unsigned long long pc; unsigned len;
  asm volatile("s_getpc_b64 %0\n.Ldb_here:\n\ts_mov_b32 %1, .Ldb_end-.Ldb_here" : "=s"(pc), "=s"(len));
  const v4u rc = mkrsrc((const void*)pc, len);
  {
    unsigned keep;
    const int vo = w * 1024 + lane * 16;
    asm volatile("s_mov_b32 %0, m0\n\ts_mov_b32 m0, %3\n\ts_nop 2\n\tbuffer_load_dwordx4 %1, %2, 0 offen lds\n\tbuffer_load_dwordx4 %1, %2, %4 offen lds\n\t"
                 "buffer_load_dwordx4 %1, %2, %5 offen lds\n\tbuffer_load_dwordx4 %1, %2, %6 offen lds\n\ts_mov_b32 m0, %0"
                 : "=&s"(keep) : "v"(vo), "s"(rc), "s"((unsigned)(uintptr_t)L + L_PF + w * 1024), "s"(8192), "s"(16384), "s"(24576) : "memory");
  }
  unsigned epoch;
  {
    const int zero = 0;
    if (w == 0) {
      __builtin_amdgcn_s_setprio(3);
      if (j == 0) asm volatile("s_nop 4\n\tglobal_store_dword %0, %0, %1 sc1" : : "v"(zero), "s"(ctr) : "memory");
    }
    asm volatile("s_nop 4\n\tglobal_load_dword %0, %1, %2 sc1" : "=v"(epoch) : "v"(zero), "s"(a.ws));
  }
  v4u wqr[5], xa, xb; v2u xc;
  {
    const size_t rowoff = (size_t)(80 * j + 10 * w) * (NH * DH * 2) + h * (DH * 2);
    const v4u rw = mkrsrc((const unsigned char*)a.wq + rowoff, (unsigned)((size_t)DM * NH * DH * 2 - rowoff));
    const int vo = l5 < 24 ? hf * (NH * DH * 2) + l5 * 16 : OOB;
#pragma unroll
    for (int i = 0; i < 5; ++i) ld_first(wqr[i], vo, rw, i * 2 * NH * DH * 2);
    const v4u rx = mkrsrc(a.x, 2 * DM * 2);
    const int xo = hf * (DM * 2) + l5 * 40;
    asm volatile("s_nop 4\n\tbuffer_load_dwordx4 %0, %3, %4, 0 offen\n\tbuffer_load_dwordx4 %1, %3, %4, 0 offen offset:16\n\tbuffer_load_dwordx2 %2, %3, %4, 0 offen offset:32"
                 : "=&v"(xa), "=&v"(xb), "=&v"(xc) : "v"(xo), "s"(rx));
  }
  const v4u rk = mkrsrc((const unsigned char*)a.k + (size_t)h * HEADB, B1OFF + HEADB), rv = mkrsrc((const unsigned char*)a.v + (size_t)h * HEADB, B1OFF + HEADB);
  const int voff = (lane >> 3) * (DH * 2) + (lane & 7) * 16;
  const unsigned sb0 = (unsigned)(S0 * j) * ITEMB, sb1 = B1OFF + (unsigned)(S1 * j - S0) * ITEMB;
  v4u km[3], vm[3], ke[NEXTRA][3], ve[NEXTRA][3];
  const unsigned so0 = w ? sb0 + (unsigned)(w - 1) * ITEMB : END;
  asm volatile("s_barrier" ::: "memory");
  ld3_first(km, voff, rk, so0); ld3_first(vm, voff, rv, so0);
  if (w == 0) {
    for (int k = lane; k < DYNMAX; k += 64) DYN[k] = EMPTY;
    if (lane < 48) {
      ((LAS unsigned*)(L + L_HS))[lane] = 0u; ((LAS unsigned*)(L + L_HS))[48 + lane] = 0u; ((LAS unsigned*)(L + L_HS))[96 + lane] = lane < 40 ? 0x3f803f80u : 0u;
    }
    if (lane < 8) CT[lane] = lane ? 0u : (2 * NEXTRA + 1) * NS;
  }
  asm volatile("s_waitcnt vmcnt(6)" : "+v"(wqr[0]), "+v"(wqr[1]), "+v"(wqr[2]), "+v"(wqr[3]), "+v"(wqr[4]), "+v"(xa), "+v"(xb), "+v"(xc), "+v"(epoch));
  ld3_first(ke[0], voff, rk, so0 + NS * ITEMB); ld3_first(ve[0], voff, rv, so0 + NS * ITEMB);
  const unsigned tag = ((__builtin_amdgcn_readfirstlane(epoch) + 0x1E3779B9u) & 0x1FFFFFFFu) | 0x20000000u;
  SSTAMP(1);
  {
    const unsigned xw[10] = {xa.x, xa.y, xa.z, xa.w, xb.x, xb.y, xb.z, xb.w, xc.x, xc.y};
    float s = 0.f, sq = 0.f;
#pragma unroll
    for (int k = 0; k < 10; ++k) { s = dot2(xw[k], 0x3f803f80u, s); sq = dot2(xw[k], xw[k], sq); }
    s = rowsum16(sw16add(s, sq)) * (1.0f / DM);
    const auto sx = __builtin_amdgcn_permlane16_swap(fbits(s), fbits(s), false, false);
    const float mean = ubits(sx[0]), mb = bfr(mean), rd = ln_rden(fmaxf(ubits(sx[1]) - mean * mean, 0.f));
    const float mb0 = rdlane(mb, 0), mb1 = rdlane(mb, 32), rd0 = rdlane(rd, 0), rd1 = rdlane(rd, 32);
    const CAS unsigned* x32 = (const CAS unsigned*)a.x + 40 * j + 5 * w;
    float qa[8], qb[8];
#pragma unroll
    for (int e = 0; e < 8; ++e) { qa[e] = 0.f; qb[e] = 0.f; }
#pragma unroll
    for (int i = 0; i < 5; ++i) {
      const unsigned u0 = x32[i], u1 = x32[DM / 2 + i];
      const float n0 = ln_val(hf ? bfhi(u0) : bflo(u0), mb0, rd0), n1 = ln_val(hf ? bfhi(u1) : bflo(u1), mb1, rd1);
#pragma unroll
      for (int e = 0; e < 4; ++e) {
        const float w0 = bflo(wqr[i][e]), w1 = bfhi(wqr[i][e]);
        qa[2 * e] = fmaf(n0, w0, qa[2 * e]); qa[2 * e + 1] = fmaf(n0, w1, qa[2 * e + 1]);
        qb[2 * e] = fmaf(n1, w0, qb[2 * e]); qb[2 * e + 1] = fmaf(n1, w1, qb[2 * e + 1]);
      }
    }
    float r[8];
#pragma unroll
    for (int e = 0; e < 8; ++e) r[e] = sw32add(qa[e], qb[e]);
    if (l5 < 24) {
      LAS v4f* dst = (LAS v4f*)(L + L_QP + ((w * 2 + hf) * DH + l5 * 8) * 4);
      dst[0] = v4f{r[0], r[1], r[2], r[3]}; dst[1] = v4f{r[4], r[5], r[6], r[7]};
    }
  }
  if (w == 0) asm volatile("s_waitcnt vmcnt(0) ; HELPER_ONLY" ::: "memory");
  BARRIER();
  SSTAMP(2);

  if (w == 0) {
    asm volatile("; HELPER_ENTRY");
    {
      const LAS float* QP = (const LAS float*)(L + L_QP);
#pragma unroll
      for (int k = 0; k < 3; ++k) {
        float s0 = 0.f, s1 = 0.f;
#pragma unroll
        for (int ww = 0; ww < NW; ++ww) { s0 += QP[(ww * 2) * DH + lane + 64 * k]; s1 += QP[(ww * 2 + 1) * DH + lane + 64 * k]; }
        __builtin_amdgcn_raw_buffer_store_b128(v4u{fbits(s0), tag, fbits(s1), tag}, rs, WS_Q + ((h * 8 + j) * DH + lane + 64 * k) * 16, 0, 16);
      }
      if (lane == 0) CT[1] = 1u;
      SSTAMP(13);
    }
    BARRIER();
    SSTAMP(3);
    int filled = 0, grab = GRAB;
    for (int spin = 0; spin < 16 * SPIN_CAP; ++spin) {
      int need = (int)__builtin_amdgcn_readfirstlane(CT[0]) + LEAD - (STAT + filled);
      if (need > 0) {
        if (filled == 0) {
          unsigned keep;
#pragma unroll 1
          for (unsigned o = 0; o < len; o += 1024)
            asm volatile("s_mov_b32 %0, m0\n\ts_mov_b32 m0, %3\n\ts_nop 2\n\tbuffer_load_dwordx4 %1, %2, %4 offen lds\n\ts_mov_b32 m0, %0"
                         : "=&s"(keep) : "v"(lane * 16), "s"(rc), "s"((unsigned)(uintptr_t)L + L_PF), "s"(o) : "memory");
        }
        need = need > 32 ? 32 : need < grab ? grab : need;
        unsigned old = 0;
        if (lane == 0) old = __hip_atomic_fetch_add(ctr, (unsigned)need, __ATOMIC_RELAXED, __HIP_MEMORY_SCOPE_AGENT);
        old = __builtin_amdgcn_readfirstlane(old);
        const bool done = old + need >= (unsigned)POOL;
        if (lane < (done ? need + NW : need)) DYN[filled + lane] = (lane < need && old + lane < (unsigned)POOL) ? B1OFF + (8 * S1 + old + lane) * ITEMB : END;
        filled += need;
        if (old + GRABEND >= (unsigned)POOL) grab = 1;
        if (done) break;
      } else __builtin_amdgcn_s_sleep(2);
    }
    if (WPOS == 0) load_weights();
  } else {
    asm volatile("; STREAM_ENTRY");
    if (w == NW - 1) {
      hint(CT + 1, QDELAY);
      v4u g[24];
      gather<24>(rs, WS_Q + (h * 8 * DH + lane) * 16, tag, g);
      SSTAMP(13);
      LAS unsigned short* QS = (LAS unsigned short*)(L + L_QS);
#pragma unroll
      for (int k = 0; k < 3; ++k) {
        float s0 = 0.f, s1 = 0.f;
#pragma unroll
        for (int p = 0; p < 8; ++p) { s0 += ubits(g[p * 3 + k].x); s1 += ubits(g[p * 3 + k].z); }
        const unsigned u = pk(s0, s1);
        QS[lane + 64 * k] = (unsigned short)u; QS[DH + lane + 64 * k] = (unsigned short)(u >> 16);
      }
    }
#pragma unroll
    for (int e = 1; e < NEXTRA; ++e) {
      if (w != NW - 1) waitvm<PACE>();
      ld3_first(ke[e], voff, rk, so0 + (e + 1) * NS * ITEMB);
      if (w != NW - 1) waitvm<PACE>();
      ld3_first(ve[e], voff, rv, so0 + (e + 1) * NS * ITEMB);
    }
    BARRIER();
    SSTAMP(3);
    v4u q[3];
#pragma unroll
    for (int jj = 0; jj < 3; ++jj) q[jj] = *(const LAS v4u*)(L + L_QS + ((lane & 7) + 8 * jj) * 16);
    Soft st;
    st.m = M_INIT; st.l = 0.f;
#pragma unroll
    for (int k = 0; k < 24; ++k) st.acc[k] = 0.f;
    auto step = [&](unsigned nxt) {
      wait3<3>(km);
      float s = score(km, q);
      asm volatile("" : "+v"(s));
      ld3_sel(km, voff, rk, nxt, h >= KH1 ? B1OFF : END);
      const float p = soft(st, s);
      wait3<3>(vm);
      pv(st, p, vm);
      __builtin_amdgcn_sched_barrier(0);
      ld3_sel(vm, voff, rv, nxt, h >= VH1 ? (h >= VH0 ? 0u : B1OFF) : END);
    };
#pragma unroll
    for (int e = 0; e < NEXTRA; ++e) {
      step(so0 + (NEXTRA + 1 + e) * NS * ITEMB);
      pin3(ke[e]); pin3(ve[e]);
      const float pe = soft(st, score(ke[e], q));
      pv(st, pe, ve[e]);
    }
    unsigned cur = so0;
#pragma unroll 1
    for (int b = 0; b < 2; ++b) {
      while (cur != END && (cur >= B1OFF) == (b == 1)) {
        unsigned t = 0;
        if (lane == 0 && w <= NACT) t = __hip_atomic_fetch_add((LAS unsigned*)(L + L_CT), 1u, __ATOMIC_RELAXED, __HIP_MEMORY_SCOPE_WORKGROUP);
        t = __builtin_amdgcn_readfirstlane(t);
        if (w > NACT) cur = END;
        else if (t < (unsigned)STAT) cur = (t < (unsigned)S0 ? sb0 : sb1) + t * ITEMB;
        else {
          int spin = 0;
          while ((cur = __builtin_amdgcn_readfirstlane(DYN[t - STAT])) == EMPTY) {
            __builtin_amdgcn_s_sleep(1);
            if (++spin > SPIN_CAP) { cur = END; break; }
          }
        }
        step(cur);
      }
      wait3<0>(km); wait3<0>(vm);
      if (GPOS == 0 && b == 1) {
        unsigned t = 0;
        if (lane == 0) t = __hip_atomic_fetch_add((LAS unsigned*)(L + L_CT) + 4, 1u, __ATOMIC_RELAXED, __HIP_MEMORY_SCOPE_WORKGROUP);
        if (__builtin_amdgcn_readfirstlane(t) == NS - 1) __builtin_amdgcn_raw_buffer_store_b32(tag, rs, lane == 0 ? WS_G + wg * 128 : OOB, 0, 16);
      }
      float M = fmaxf(st.m, dpp<ROR8>(st.m)); M = sw32max(sw16max(M));
      const float sc = __builtin_amdgcn_exp2f(st.m - M);
      float l = st.l * sc; l += dpp<ROR8>(l); l = sw16add(l, l); l = sw32add(l, l);
      float t1[12], t2[6];
#pragma unroll
      for (int k = 0; k < 12; ++k) t1[k] = sw16add(st.acc[k] * sc, st.acc[k + 12] * sc);
#pragma unroll
      for (int k = 0; k < 6; ++k) { t2[k] = sw32add(t1[k], t1[k + 6]); t2[k] += dpp<ROR8>(t2[k]); }
      LAS float* rec = (LAS float*)(L + L_REC) + ((w - 1) * 2 + b) * RECF;
      if (!(lane & 8)) {
        const int a0 = 6 * hf + 12 * ((lane >> 4) & 1);
#pragma unroll
        for (int k = 0; k < 6; k += 2) { const int ai = a0 + k; *(LAS v2f*)(rec + 4 + ((lane & 7) + 8 * (ai >> 3)) * 8 + (ai & 7)) = v2f{t2[k], t2[k + 1]}; }
      }
      if (lane == 0) *(LAS v2f*)rec = v2f{M, l};
#pragma unroll
      for (int jj = 0; jj < 3; ++jj) q[jj] = *(const LAS v4u*)(L + L_QS + DH * 2 + ((lane & 7) + 8 * jj) * 16);
      st.m = M_INIT; st.l = 0.f;
#pragma unroll
      for (int k = 0; k < 24; ++k) st.acc[k] = 0.f;
    }
  }
  STAMP(4);
  const int g4 = lane >> 4, c16 = lane & 15, tq = c16 >> 2, tp = lane & 3;
  const v4f ctag = {0.f, ubits(tag), 0.f, ubits(tag)};
  const v4u zero4 = {0u, 0u, 0u, 0u};
  BARRIER();
  STAMP(5);
  const bool tilew = w == 0 || w >= 4;
  const int tile = w ? w - 3 : 0;
  unsigned xv0 = 0, xv1 = 0;
  if (!tilew) {
    const int dim = tid - 64;
    if (GPOS == 1) __builtin_amdgcn_raw_buffer_store_b32(tag, rs, dim == 0 ? WS_G + wg * 128 : OOB, 0, 16);
    {
      const LAS float* rec = (const LAS float*)(L + L_REC);
      v2f ml = *(const LAS v2f*)(rec + (c16 < 2 * NS ? c16 : 0) * RECF);
      float av[2 * NS];
#pragma unroll
      for (int r = 0; r < 2 * NS; ++r) av[r] = rec[r * RECF + 4 + dim];
      if (c16 >= 2 * NS) ml = v2f{M_INIT, 0.f};
      float M = fmaxf(ml.x, dpp<XOR2>(ml.x)); M = fmaxf(M, dpp<ROR4>(M)); M = fmaxf(M, dpp<ROR8>(M));
      const float e = __builtin_amdgcn_exp2f(ml.x - M);
      float ls = e * ml.y; ls += dpp<XOR2>(ls); ls += dpp<ROR4>(ls); ls += dpp<ROR8>(ls);
      float o0 = 0.f, o1 = 0.f;
#pragma unroll
      for (int r = 0; r < 2 * NS; r += 2) { o0 = fmaf(rdlane(e, r), av[r], o0); o1 = fmaf(rdlane(e, r + 1), av[r + 1], o1); }
      if (GPOS == 2) __builtin_amdgcn_raw_buffer_store_b32(tag, rs, dim == 0 ? WS_G + wg * 128 : OOB, 0, 16);
      put(rs, WS_P + wg * PBLK + dim * 16, fbits(o0), fbits(o1), tag);
      const float lsx = dpp<XOR1>(ls), Mx = dpp<XOR1>(M);
      put(rs, dim < 2 ? WS_P + wg * PBLK + (DH + dim) * 16 : OOB, fbits((lane & 1) ? lsx : M), fbits((lane & 1) ? ls : Mx), tag);
      STAMP(6);
    }
    v4u g[8], gm = zero4;
#pragma unroll
    for (int p = 0; p < 8; ++p) g[p] = zero4;
    const int po = WS_P + h * 8 * PBLK + dim * 16, pm = WS_P + (h * 8 + ((lane >> 1) & 7)) * PBLK + (DH + (lane & 1)) * 16;
    for (unsigned sent = 0, spin = 0; sent != 0xffu && spin < 4 * SPIN_CAP; ++spin) {
      const unsigned want = __builtin_amdgcn_readfirstlane(CT[5]) & ~sent & 0xffu;
#pragma unroll
      for (int p = 0; p < 8; ++p)
        if ((want >> p) & 1u) g[p] = __builtin_amdgcn_raw_buffer_load_b128(rs, po, p * PBLK, 16);
      sent |= want;
    }
    for (int spin = 0; spin < SPIN_CAP; ++spin) {
      gm = __builtin_amdgcn_raw_buffer_load_b128(rs, pm, 0, 16);
      bool ok = true;
#pragma unroll
      for (int p = 0; p < 8; ++p)
        if (!__all(g[p].y == tag && g[p].w == tag)) { g[p] = __builtin_amdgcn_raw_buffer_load_b128(rs, po, p * PBLK, 16); ok = false; }
      if (ok && __all(gm.y == tag && gm.w == tag)) break;
      asm volatile("" ::: "memory");
    }
    STAMP(7);
    float M0 = ubits(gm.x), M1 = ubits(gm.z);
    M0 = fmaxf(M0, dpp<XOR2>(M0)); M1 = fmaxf(M1, dpp<XOR2>(M1)); M0 = fmaxf(M0, dpp<ROR4>(M0)); M1 = fmaxf(M1, dpp<ROR4>(M1)); M0 = fmaxf(M0, dpp<ROR8>(M0)); M1 = fmaxf(M1, dpp<ROR8>(M1));
    const float e0 = __builtin_amdgcn_exp2f(ubits(gm.x) - M0), e1 = __builtin_amdgcn_exp2f(ubits(gm.z) - M1);
    float l0 = e0 * dpp<XOR1>(ubits(gm.x)), l1 = e1 * dpp<XOR1>(ubits(gm.z));
    l0 += dpp<XOR2>(l0); l1 += dpp<XOR2>(l1); l0 += dpp<ROR4>(l0); l1 += dpp<ROR4>(l1); l0 += dpp<ROR8>(l0); l1 += dpp<ROR8>(l1);
    float o0 = 0.f, o1 = 0.f;
#pragma unroll
    for (int p = 0; p < 8; ++p) { o0 = fmaf(rdlane(e0, 2 * p), ubits(g[p].x), o0); o1 = fmaf(rdlane(e1, 2 * p), ubits(g[p].z), o1); }
    const unsigned ob = pk(o0 * __builtin_amdgcn_rcpf(rdlane(l0, 0)), o1 * __builtin_amdgcn_rcpf(rdlane(l1, 0)));
    ((LAS unsigned short*)(L + L_O))[dim] = (unsigned short)ob; ((LAS unsigned short*)(L + L_O))[DH + dim] = (unsigned short)(ob >> 16);
  } else {
    if (WPOS == 1 && w == 0) load_weights();
    xv0 = a.x[80 * j + 16 * tile + c16]; xv1 = a.x[DM + 80 * j + 16 * tile + c16];
    if (w == 0) { if (ffn) waitvm<30>(); else waitvm<0>(); }
    if (w == NW - 1) { watch<8>(rs, WS_G + h * 8 * 128, 128, lane, tag, CT + 5); STAMP(13); }
  }
  BARRIER();
  STAMP(1);
  if (tilew) {
    const int ybase = WS_Y + (j * 5 + tile) * (NH * 256);
    if (DPOS == 0) __builtin_amdgcn_raw_buffer_store_b32(tag, rs, tid == 0 ? WS_D + wg * 128 : OOB, 0, 16);
    v4f d = ctag;
#pragma unroll
    for (int k = 0; k < 6; ++k) {
      const v4u ao = *(const LAS v4u*)(L + L_O + (c16 & 2) * DH + (32 * k + 8 * g4) * 2);
      d = mfma((c16 & 1) ? zero4 : ao, bfrag<160>(L + L_WO + (32 * k + 8 * g4 + tq) * 160 + (16 * tile + 4 * tp) * 2), d);
    }
    if (DPOS == 1) { asm volatile("" : "+v"(d)); __builtin_amdgcn_raw_buffer_store_b32(tag, rs, tid == 0 ? WS_D + wg * 128 : OOB, 0, 16); }
    __builtin_amdgcn_raw_buffer_store_b128(__builtin_bit_cast(v4u, d), rs, lane < 16 ? ybase + h * 256 + c16 * 16 : OOB, 0, 16);
    STAMP(8);
    if (ffn) {
      v4u g[8];
#pragma unroll
      for (int k = 0; k < 8; ++k) g[k] = zero4;
      for (unsigned sent = 0, spin = 0; sent != 0x11111111u && spin < 4 * SPIN_CAP; ++spin) {
        unsigned m = __builtin_amdgcn_readfirstlane(CT[6]);
        m &= m >> 1; m &= m >> 2; m &= 0x11111111u & ~sent;
#pragma unroll
        for (int k = 0; k < 8; ++k)
          if ((m >> (4 * k)) & 1u) g[k] = __builtin_amdgcn_raw_buffer_load_b128(rs, ybase + lane * 16 + (k & 3) * 1024, (k >> 2) * 4096, 16);
        sent |= m;
      }
      for (int spin = 0; spin < SPIN_CAP; ++spin) {
        bool ok = true;
#pragma unroll
        for (int k = 0; k < 8; ++k)
          if (!__all(g[k].y == tag && g[k].w == tag)) { g[k] = __builtin_amdgcn_raw_buffer_load_b128(rs, ybase + lane * 16 + (k & 3) * 1024, (k >> 2) * 4096, 16); ok = false; }
        if (ok) break;
        asm volatile("" ::: "memory");
      }
      STAMP(2);
      const float s0 = ((ubits(g[0].x) + ubits(g[1].x)) + (ubits(g[2].x) + ubits(g[3].x))) + ((ubits(g[4].x) + ubits(g[5].x)) + (ubits(g[6].x) + ubits(g[7].x)));
      const float s1 = ((ubits(g[0].z) + ubits(g[1].z)) + (ubits(g[2].z) + ubits(g[3].z))) + ((ubits(g[4].z) + ubits(g[5].z)) + (ubits(g[6].z) + ubits(g[7].z)));
      float s = sw16add(s0, s1); s = sw32add(s, s);
      const float hv = bflo((g4 & 1) ? xv1 : xv0) + bfr(s);
      if (lane < 32) ((LAS unsigned short*)(L + L_HS))[g4 * 96 + 16 * tile + c16] = (unsigned short)pk(hv, 0.f);
    }
  } else if (ffn && w == 1) { watch<32>(rs, WS_D + j * 128, 8 * 128, lane, tag, CT + 6); STAMP(14); }
  if (ffn) {
    int eo = WS_E + (cg * 8 + w) * 5120;
    const int eo0 = lane < 32 ? eo + j * 512 + lane * 16 : OOB, eo1 = lane < 8 ? eo + 4096 + j * 128 + lane * 16 : OOB;
    if (w == 0) waitvm<0>();
    BARRIER();
    STAMP(3);
    {
      v4f d = ctag, ds = ctag;
#pragma unroll
      for (int k = 0; k < 3; ++k) {
        v4u ah = *(const LAS v4u*)(L + L_HS + (c16 < 5 ? c16 >> 1 : 0) * 192 + (32 * k + 8 * g4) * 2);
        if ((c16 & 1) || c16 > 4) ah = zero4;
        const int row = 32 * k + 8 * g4 + tq;
        d = mfma(ah, bfrag<128>(L + L_G + (tp >> 1) * 10240 + (row < 80 ? row : tq) * 128 + (8 * w + 4 * (tp & 1)) * 2), d);
        ds = mfma(ah, ah, ds);
      }
      __builtin_amdgcn_raw_buffer_store_b128(__builtin_bit_cast(v4u, d), rs, eo0, 0, 16);
      __builtin_amdgcn_raw_buffer_store_b128(__builtin_bit_cast(v4u, ds), rs, eo1, 0, 16);
    }
    STAMP(9);
    if (w >= 1 && w <= 4) {
      v4u g[5], bdn[3]; int zo[3];
#pragma unroll
      for (int T = 0; T < 3; ++T) {
        const int dt = w - 1 + 4 * T, n = 64 * dt + lane;
        bdn[T] = bfrag<1280>(L + L_WD + tq * 1280 + (64 * (dt < 10 ? dt : 0) + 16 * g4 + 4 * tp) * 2);
        zo[T] = dt < 10 ? WS_Z + ((n >> 2) * NFFN + f) * 64 + (n & 3) * 16 : OOB;
        asm volatile("" : "+v"(zo[T]));
      }
      __builtin_amdgcn_s_sleep(EDELAY);
      gather<5>(rs, WS_E + f * 5120 + lane * 16, tag, g);
      if (FPOS == 0) __builtin_amdgcn_raw_buffer_store_b32(tag, rs, (tid == 64 && j == 0) ? WS_F + cg * 128 : OOB, 0, 16);
      STAMP(10);
      const float s0 = (ubits(g[0].x) + ubits(g[1].x)) + (ubits(g[2].x) + ubits(g[3].x)), s1 = (ubits(g[0].z) + ubits(g[1].z)) + (ubits(g[2].z) + ubits(g[3].z));
      const float gs = sw32add(s0, s1);
      const float cs0 = sw32add(s0, s0);
      const auto cx = __builtin_amdgcn_permlane16_swap(fbits(cs0), fbits(cs0), false, false);
      const auto gx = __builtin_amdgcn_permlane16_swap(fbits(gs), fbits(gs), false, false);
      float t0 = ubits(g[4].x), t1 = ubits(g[4].z);
      t0 += dpp<ROR8>(t0); t1 += dpp<ROR8>(t1);
      t0 = sw16add(t0, t0); t1 = sw16add(t1, t1);
      const float tt = sw32add(t0, t1) * (1.0f / DM);
      const float mean = dpp<BC + 4>(tt), q0 = dpp<BC + 0>(tt), q1 = dpp<BC + 2>(tt);
      const float rdv = ln_rden(fmaxf((hf ? q1 : q0) - mean * mean, 0.f));
      const float gv = bfr((ubits(gx[0]) - bfr(mean) * ubits(cx[1])) * rdv), uv = dpp<ROR8>(gv);
      const float y2 = (-2.0f * 0.7978845608028654f * 1.4426950408889634f) * (gv + 0.044715f * gv * gv * gv);
      const float act = bfr(gv * __builtin_amdgcn_rcpf(1.0f + __builtin_amdgcn_exp2f(y2))) * uv;
      const unsigned ap = pk(act, dpp<XOR1>(act));
      const auto ax = __builtin_amdgcn_permlane32_swap(ap, ap, false, false);
      const unsigned keep = ((c16 >> 2) == g4 && !(c16 & 1)) ? 0xffffffffu : 0u, k1 = (c16 & 2) ? keep : 0u, k0 = keep ^ k1;
      const v4u b0 = {dppu<BC + 0>(ax[0]), dppu<BC + 2>(ax[0]), dppu<BC + 4>(ax[0]), dppu<BC + 6>(ax[0])};
      const v4u b1 = {dppu<BC + 0>(ax[1]), dppu<BC + 2>(ax[1]), dppu<BC + 4>(ax[1]), dppu<BC + 6>(ax[1])};
      v4u a2 = (b0 & k0) | (b1 & k1);
      if (FPOS == 1) { asm volatile("" : "+v"(a2)); __builtin_amdgcn_raw_buffer_store_b32(tag, rs, (tid == 64 && j == 0) ? WS_F + cg * 128 : OOB, 0, 16); }
#pragma unroll
      for (int T = 0; T < 3; ++T) __builtin_amdgcn_raw_buffer_store_b128(__builtin_bit_cast(v4u, mfma(a2, bdn[T], ctag)), rs, zo[T], 0, 16);
      STAMP(11);
    } else if (w == NW - 1 && cg < 20) {
      const __amdgpu_buffer_rsrc_t ro = __builtin_amdgcn_make_buffer_rsrc((void*)a.out, 0, 2 * DM * 2, 0x00020000);
      const float hv = bflo(((const LAS unsigned short*)(L + L_HS))[(g4 & 1) * 96 + 4 * cg + (lane & 3)]);
      v4u g[12];
      watch<24>(rs, WS_F, 128, lane, tag, CT + 7);
      STAMP(15);
      __builtin_amdgcn_s_sleep(ZDELAY);
      gather<12>(rs, WS_Z + (20 * j + cg) * (NFFN * 64) + lane * 16, tag, g);
      float s0[4], s1[4];
#pragma unroll
      for (int k = 0; k < 4; ++k) { s0[k] = (ubits(g[k].x) + ubits(g[k + 4].x)) + ubits(g[k + 8].x); s1[k] = (ubits(g[k].z) + ubits(g[k + 4].z)) + ubits(g[k + 8].z); }
      float s = sw16add((s0[0] + s0[1]) + (s0[2] + s0[3]), (s1[0] + s1[1]) + (s1[2] + s1[3]));
      s = sw32add(s, s); s += dpp<ROR4>(s); s += dpp<ROR8>(s);
      const unsigned o = pk(hv + bfr(s), 0.f);
      __builtin_amdgcn_raw_buffer_store_b16((unsigned short)o, ro, (lane < 32 && c16 < 4) ? (g4 * DM + 80 * j + 4 * cg + c16) * 2 : OOB, 0, 0);
      __builtin_amdgcn_raw_buffer_store_b32(tag, rs, (wg == 0 && lane == 0) ? 0 : OOB, 0, 0);
      STAMP(12);
    }
  }
  STAMP_DUMP();
  asm volatile(".Ldb_end:");
}
}

extern "C" void kernel_launch(void* const* d_in, const int* in_sizes, int n_in, void* d_out, int out_size, void* d_ws, size_t ws_size, hipStream_t stream) {
  Args a;
  a.x = (const unsigned short*)d_in[0]; a.k = (const unsigned short*)d_in[1]; a.v = (const unsigned short*)d_in[2];
  a.wq = (const unsigned short*)d_in[3]; a.wo = (const unsigned short*)d_in[4];
  a.wg = (const unsigned short*)d_in[6]; a.wu = (const unsigned short*)d_in[7]; a.wd = (const unsigned short*)d_in[8];
  a.out = (unsigned short*)d_out; a.ws = (unsigned char*)d_ws;
  hipLaunchKernelGGL(decode_block, dim3(NWG), dim3(NTHR), 0, stream, a);
}
```

```cpp
#include <hip/hip_runtime.h>
#include <hip/hip_bf16.h>
#include <stdint.h>

namespace {
typedef unsigned v4u __attribute__((ext_vector_type(4)));
typedef unsigned v2u __attribute__((ext_vector_type(2)));
typedef float v4f __attribute__((ext_vector_type(4)));
typedef float v2f __attribute__((ext_vector_type(2)));
typedef short v4s __attribute__((ext_vector_type(4)));
typedef __bf16 bf2 __attribute__((ext_vector_type(2)));
typedef __bf16 bf8 __attribute__((ext_vector_type(8)));
#define LAS __attribute__((address_space(3)))
#define CAS __attribute__((address_space(4)))

constexpr int DM = 640, NH = 32, SK = 16384, DH = 192, FF = 1536;
constexpr int NWG = 256, NW = 8, NS = NW - 1, NTHR = NW * 64;
constexpr unsigned ITEMB = 8 * DH * 2;
constexpr unsigned HEADB = (unsigned)SK * DH * 2, B1OFF = NH * HEADB;
constexpr int IPH = SK / 8;
constexpr int S0 = IPH / 8, S1 = 192, STAT = S0 + S1;
constexpr int POOL = IPH - 8 * S1, LEAD = 8;
constexpr int NEXTRA = 5, NACT = 5;
constexpr unsigned END = 0xF0000000u, EMPTY = 0xFFFFFFFFu;
constexpr int NRED = DM / 4, NFFN = FF / 8;
constexpr int SPIN_CAP = 1 << 21;
constexpr float M_INIT = -3.0e38f;

constexpr int WS_CTR = 4096, CTR_STRIDE = 4352;
constexpr int WS_F = 0x24000, WS_D = 0x28000, WS_G = 0x30000, WS_Q = 0x40000, WS_P = 0x100000, PBLK = 3200, WS_Y = 0x200000, WS_E = 0x280000, WS_Z = 0x400000, WS_BYTES = 0x600000;
constexpr int OOB = (int)0x80000000u;
static_assert(WS_CTR + NH * CTR_STRIDE <= WS_F && WS_F + 24 * 128 <= WS_D && WS_D + NWG * 128 <= WS_G && WS_G + NWG * 128 <= WS_Q && WS_Q + NWG * DH * 16 <= WS_P && WS_P + NWG * PBLK <= WS_Y, "ws map");
static_assert(WS_Y + NWG * 80 * 16 <= WS_E && WS_E + NFFN * 5120 <= WS_Z && WS_Z + NRED * NFFN * 64 <= WS_BYTES, "ws map");

constexpr int L_WO = 0, L_G = L_WO + DH * 160, L_WD = L_G + 2 * 80 * 128, L_QP = L_WD + 8 * 1280;
constexpr int L_QS = L_QP + NW * 2 * DH * 4, RECF = DH + 4, L_REC = L_QS + 2 * DH * 2, L_O = L_REC + NS * 2 * RECF * 4, L_HS = L_O + 2 * DH * 2;
constexpr int L_CT = L_HS + 3 * 192, DYNMAX = POOL + 96, L_DYN = L_CT + 64, L_BYTES = L_DYN + DYNMAX * 4;
static_assert(L_BYTES > 81920 && L_BYTES <= 160 * 1024 && L_REC % 16 == 0 && L_O % 16 == 0 && L_HS % 16 == 0, "one workgroup per CU");

struct Args {
  const unsigned short *x, *k, *v, *wq, *wo, *wg, *wu, *wd;
  unsigned short* out;
  unsigned char* ws;
};

__device__ __forceinline__ float dot2(unsigned a, unsigned b, float c) {
  return __builtin_amdgcn_fdot2_f32_bf16(__builtin_bit_cast(bf2, a), __builtin_bit_cast(bf2, b), c, false);
}
template <int CTRL> __device__ __forceinline__ float dpp(float x) {
  return __builtin_bit_cast(float, __builtin_amdgcn_mov_dpp(__builtin_bit_cast(int, x), CTRL, 0xf, 0xf, true));
}
template <int CTRL> __device__ __forceinline__ unsigned dppu(unsigned x) { return (unsigned)__builtin_amdgcn_mov_dpp((int)x, CTRL, 0xf, 0xf, true); }
constexpr int BC = 0x150;
constexpr int XOR1 = 0xB1, XOR2 = 0x4E, HMIR = 0x141, ROR4 = 0x124, ROR8 = 0x128;
__device__ __forceinline__ float bflo(unsigned x) { return __builtin_bit_cast(float, x << 16); }
__device__ __forceinline__ float bfhi(unsigned x) { return __builtin_bit_cast(float, x & 0xffff0000u); }
__device__ __forceinline__ unsigned pk(float lo, float hi) {
  const bf2 r = {(__bf16)lo, (__bf16)hi};
  return __builtin_bit_cast(unsigned, r);
}
__device__ __forceinline__ float bfr(float x) { return (float)(__bf16)x; }
__device__ __forceinline__ unsigned fbits(float x) { return __builtin_bit_cast(unsigned, x); }
__device__ __forceinline__ float ubits(unsigned x) { return __builtin_bit_cast(float, x); }
__device__ __forceinline__ float sw16add(float a, float b) {
  const auto s = __builtin_amdgcn_permlane16_swap(fbits(a), fbits(b), false, false);
  return ubits(s[0]) + ubits(s[1]);
}
__device__ __forceinline__ float sw32add(float a, float b) {
  const auto s = __builtin_amdgcn_permlane32_swap(fbits(a), fbits(b), false, false);
  return ubits(s[0]) + ubits(s[1]);
}
__device__ __forceinline__ float sw16max(float a) {
  const auto s = __builtin_amdgcn_permlane16_swap(fbits(a), fbits(a), false, false);
  return fmaxf(ubits(s[0]), ubits(s[1]));
}
__device__ __forceinline__ float sw32max(float a) {
  const auto s = __builtin_amdgcn_permlane32_swap(fbits(a), fbits(a), false, false);
  return fmaxf(ubits(s[0]), ubits(s[1]));
}
__device__ __forceinline__ float rowsum16(float s) { s += dpp<XOR1>(s); s += dpp<XOR2>(s); s += dpp<HMIR>(s); s += dpp<ROR8>(s); return s; }
__device__ __forceinline__ float rdlane(float x, int l) { return ubits(__builtin_amdgcn_readlane(fbits(x), l)); }
__device__ __forceinline__ v4u mkrsrc(const void* p, unsigned bytes) {
  const unsigned long long a = (unsigned long long)p;
  return v4u{(unsigned)a, (unsigned)(a >> 32) & 0xffffu, bytes, 0x00020000u};
}
__device__ __forceinline__ v4f mfma(v4u a, v4u b, v4f c) {
  return __builtin_amdgcn_mfma_f32_16x16x32_bf16(__builtin_bit_cast(bf8, a), __builtin_bit_cast(bf8, b), c, 0, 0, 0);
}
template <int STRIDE> __device__ __forceinline__ v4u bfrag(LAS unsigned char* p) {
  const v2u lo = __builtin_bit_cast(v2u, __builtin_amdgcn_ds_read_tr16_b64_v4i16((LAS v4s*)p));
  const v2u hi = __builtin_bit_cast(v2u, __builtin_amdgcn_ds_read_tr16_b64_v4i16((LAS v4s*)(p + 4 * STRIDE)));
  return v4u{lo.x, lo.y, hi.x, hi.y};
}
#define BARRIER() asm volatile("s_waitcnt lgkmcnt(0)\n\ts_barrier" ::: "memory")

#define LD3 "s_nop 4\n\tbuffer_load_dwordx4 %0, %3, %4, %5 offen nt\n\tbuffer_load_dwordx4 %1, %3, %4, %5 offen offset:128 nt\n\tbuffer_load_dwordx4 %2, %3, %4, %5 offen offset:256 nt"
__device__ __forceinline__ void ld_first(v4u& d, int voff, v4u rs, unsigned soff) {
  asm volatile("s_nop 4\n\tbuffer_load_dwordx4 %0, %1, %2, %3 offen nt" : "=v"(d) : "v"(voff), "s"(rs), "s"(soff));
}
__device__ __forceinline__ void ld3_first(v4u (&r)[3], int voff, v4u rs, unsigned so) { asm volatile(LD3 : "=&v"(r[0]), "=&v"(r[1]), "=&v"(r[2]) : "v"(voff), "s"(rs), "s"(so)); }
__device__ __forceinline__ void ld3_again(v4u (&r)[3], int voff, v4u rs, unsigned so) { asm volatile(LD3 : "+v"(r[0]), "+v"(r[1]), "+v"(r[2]) : "v"(voff), "s"(rs), "s"(so)); }
template <int N> __device__ __forceinline__ void wait3(v4u (&r)[3]) { asm volatile("s_waitcnt vmcnt(%3)" : "+v"(r[0]), "+v"(r[1]), "+v"(r[2]) : "i"(N)); }
__device__ __forceinline__ void pin3(v4u (&r)[3]) { asm volatile("" : "+v"(r[0]), "+v"(r[1]), "+v"(r[2])); }
template <int N> __device__ __forceinline__ void waitvm() { asm volatile("s_waitcnt vmcnt(%0)" : : "i"(N) : "memory"); }

__device__ __forceinline__ void dma16(const void* base, int voff, unsigned lds) {
  unsigned keep;
  asm volatile("s_mov_b32 %0, m0\n\ts_mov_b32 m0, %3\n\ts_nop 2\n\tglobal_load_lds_dwordx4 %1, %2 nt\n\ts_mov_b32 m0, %0" : "=&s"(keep) : "v"(voff), "s"(base), "s"(lds) : "memory");
}

__device__ __forceinline__ void put(__amdgpu_buffer_rsrc_t rs, int off, unsigned a, unsigned b, unsigned tag) {
  __builtin_amdgcn_raw_buffer_store_b128(v4u{a, tag, b, tag}, rs, off, 0, 16);
}
#define STAMP(n)
#define SSTAMP(n)
#define STAMP_INIT()
#define STAMP_DUMP()
template <int N> __device__ __forceinline__ void gather(__amdgpu_buffer_rsrc_t rs, int off, unsigned tag, v4u (&t)[N]) {
  bool done = false;
#pragma unroll
  for (int k = 0; k < N; ++k) t[k] = v4u{0u, 0u, 0u, 0u};
  for (int spin = 0; spin < SPIN_CAP; ++spin) {
    if (!done) {
#pragma unroll
      for (int k = 0; k < N; ++k) t[k] = __builtin_amdgcn_raw_buffer_load_b128(rs, off + (k & 3) * 1024, (k >> 2) * 4096, 16);
      unsigned mn = tag, mx = tag;
#pragma unroll
      for (int k = 0; k < N; ++k) { mn = min(mn, min(t[k].y, t[k].w)); mx = max(mx, max(t[k].y, t[k].w)); }
      done = mn == mx;
    }
    if (__all(done)) break;
    asm volatile("" ::: "memory");
    __builtin_amdgcn_s_sleep(1);
  }
}

template <int N> __device__ __forceinline__ void watch(__amdgpu_buffer_rsrc_t rs, int off, int stride, int lane, unsigned tag, volatile LAS unsigned* mask) {
  bool seen = lane >= N;
  for (int spin = 0; spin < SPIN_CAP; ++spin) {
    if (!seen) seen = __builtin_amdgcn_raw_buffer_load_b32(rs, off + lane * stride, 0, 16) == tag;
    const unsigned long long m = __ballot(seen);
    if (lane == 0) *mask = (unsigned)m;
    if (m == ~0ull) break;
    asm volatile("" ::: "memory");
    __builtin_amdgcn_s_sleep(1);
  }
  if (lane == 0) *mask = ~0u;
}
#define hint(p, D) do { for (int sp_ = 0; sp_ < SPIN_CAP && *(p) == 0u; ++sp_) __builtin_amdgcn_s_sleep(1); __builtin_amdgcn_s_sleep(D); } while (0)

__device__ __forceinline__ float ln_rden(float var) { return __builtin_amdgcn_rcpf(bfr(__builtin_amdgcn_sqrtf(bfr(bfr(var) + 1e-5f)))); }
__device__ __forceinline__ float ln_val(float x, float mean_b, float rden) { return bfr(bfr(x - mean_b) * rden); }

struct Soft { float m, l, acc[24]; };
__device__ __forceinline__ float score(const v4u (&kk)[3], const v4u (&q)[3]) {
  float s0 = 0.f, s1 = 0.f;
#pragma unroll
  for (int j = 0; j < 3; ++j) {
    s0 = dot2(kk[j].x, q[j].x, s0); s1 = dot2(kk[j].y, q[j].y, s1);
    s0 = dot2(kk[j].z, q[j].z, s0); s1 = dot2(kk[j].w, q[j].w, s1);
  }
  return s0 + s1;
}
__device__ __forceinline__ float soft(Soft& st, float s) {
  s += dpp<XOR1>(s); s += dpp<XOR2>(s); s += dpp<HMIR>(s);
  s = bfr(bfr(s) * 0.07216878364870322f) * 1.4426950408889634f;
  if (__any(s > st.m)) {
    const float mn = fmaxf(st.m, s), al = __builtin_amdgcn_exp2f(st.m - mn);
    st.l *= al;
#pragma unroll
    for (int k = 0; k < 24; ++k) st.acc[k] *= al;
    st.m = mn;
  }
  const float p = __builtin_amdgcn_exp2f(s - st.m);
  st.l += p;
  return p;
}
__device__ __forceinline__ void pv(Soft& st, float p, const v4u (&vv)[3]) {
#pragma unroll
  for (int j = 0; j < 3; ++j)
#pragma unroll
    for (int e = 0; e < 4; ++e) {
      st.acc[j * 8 + 2 * e] = fmaf(p, bflo(vv[j][e]), st.acc[j * 8 + 2 * e]);
      st.acc[j * 8 + 2 * e + 1] = fmaf(p, bfhi(vv[j][e]), st.acc[j * 8 + 2 * e + 1]);
    }
}

__global__ void __launch_bounds__(NTHR, 2) decode_block(Args a) {
  __shared__ __attribute__((aligned(16))) unsigned char smem[L_BYTES];
  LAS unsigned char* L = (LAS unsigned char*)smem;
  volatile LAS unsigned* CT = (volatile LAS unsigned*)(L + L_CT);
  volatile LAS unsigned* DYN = (volatile LAS unsigned*)(L + L_DYN);
  const int tid = threadIdx.x, lane = tid & 63, w = __builtin_amdgcn_readfirstlane(tid >> 6);
  const int wg = blockIdx.x, h = wg >> 3, j = wg & 7;
  const int hf = lane >> 5, l5 = lane & 31;
  const bool ffn = ((h + j) & 3) != 3;
  const int cg = 3 * (h >> 2) + (h & 3) - ((h & 3) > ((3 - j) & 3) ? 1 : 0), f = 8 * cg + j;
  const __amdgpu_buffer_rsrc_t rs = __builtin_amdgcn_make_buffer_rsrc((void*)a.ws, 0, WS_BYTES, 0x00020000);
  unsigned* ctr = (unsigned*)(a.ws + WS_CTR + h * CTR_STRIDE);
  STAMP_INIT();
  STAMP(0);

  auto load_weights = [&]() {
    const unsigned lb = (unsigned)(uintptr_t)L;
    const unsigned char* wo = (const unsigned char*)a.wo + (size_t)h * DH * (DM * 2) + j * 160;
    int vo = (lane / 10) * (DM * 2) + (lane % 10) * 16, c10 = lane % 10;
#pragma unroll 2
    for (int k = 0; k < 30; ++k) {
      dma16(wo, vo, lb + L_WO + k * 1024);
      c10 += 4; vo += 6 * (DM * 2) + 64;
      if (c10 >= 10) { c10 -= 10; vo += DM * 2 - 160; }
    }
    if (ffn) {
      const size_t o0 = (size_t)80 * j * (FF * 2) + cg * 128;
      const int vg = (lane >> 3) * (FF * 2) + (lane & 7) * 16;
#pragma unroll 1
      for (int k = 0; k < 10; ++k) {
        dma16((const unsigned char*)a.wg + o0 + k * 8 * FF * 2, vg, lb + L_G + k * 1024);
        dma16((const unsigned char*)a.wu + o0 + k * 8 * FF * 2, vg, lb + L_G + 10240 + k * 1024);
        dma16((const unsigned char*)a.wd + (size_t)f * 8 * (DM * 2) + k * 1024, lane * 16, lb + L_WD + k * 1024);
      }
    }
  };
  unsigned epoch;
  {
    const int zero = 0;
    if (w == 0) {
      __builtin_amdgcn_s_setprio(3);
      if (j == 0) asm volatile("s_nop 4\n\tglobal_store_dword %0, %0, %1 sc1" : : "v"(zero), "s"(ctr) : "memory");
    }
    asm volatile("s_nop 4\n\tglobal_load_dword %0, %1, %2 sc1" : "=v"(epoch) : "v"(zero), "s"(a.ws));
  }
  v4u wqr[5], xa, xb; v2u xc;
  {
    const size_t rowoff = (size_t)(80 * j + 10 * w) * (NH * DH * 2) + h * (DH * 2);
    const v4u rw = mkrsrc((const unsigned char*)a.wq + rowoff, (unsigned)((size_t)DM * NH * DH * 2 - rowoff));
    const int vo = l5 < 24 ? hf * (NH * DH * 2) + l5 * 16 : OOB;
#pragma unroll
    for (int i = 0; i < 5; ++i) ld_first(wqr[i], vo, rw, i * 2 * NH * DH * 2);
    const v4u rx = mkrsrc(a.x, 2 * DM * 2);
    const int xo = hf * (DM * 2) + l5 * 40;
    asm volatile("s_nop 4\n\tbuffer_load_dwordx4 %0, %3, %4, 0 offen\n\tbuffer_load_dwordx4 %1, %3, %4, 0 offen offset:16\n\tbuffer_load_dwordx2 %2, %3, %4, 0 offen offset:32"
                 : "=&v"(xa), "=&v"(xb), "=&v"(xc) : "v"(xo), "s"(rx));
  }
  const v4u rk = mkrsrc((const unsigned char*)a.k + (size_t)h * HEADB, B1OFF + HEADB), rv = mkrsrc((const unsigned char*)a.v + (size_t)h * HEADB, B1OFF + HEADB);
  const int voff = (lane >> 3) * (DH * 2) + (lane & 7) * 16;
  const unsigned sb0 = (unsigned)(S0 * j) * ITEMB, sb1 = B1OFF + (unsigned)(S1 * j - S0) * ITEMB;
  v4u km[3], vm[3], ke[NEXTRA][3], ve[NEXTRA][3];
  const unsigned so0 = w ? sb0 + (unsigned)(w - 1) * ITEMB : END;
  asm volatile("s_barrier" ::: "memory");
  ld3_first(km, voff, rk, so0); ld3_first(vm, voff, rv, so0);
  if (w == 0) {
    for (int k = lane; k < DYNMAX; k += 64) DYN[k] = EMPTY;
    if (lane < 48) {
      ((LAS unsigned*)(L + L_HS))[lane] = 0u; ((LAS unsigned*)(L + L_HS))[48 + lane] = 0u; ((LAS unsigned*)(L + L_HS))[96 + lane] = lane < 40 ? 0x3f803f80u : 0u;
    }
    if (lane < 8) CT[lane] = lane ? 0u : (2 * NEXTRA + 1) * NS;
  }
  asm volatile("s_waitcnt vmcnt(6)" : "+v"(wqr[0]), "+v"(wqr[1]), "+v"(wqr[2]), "+v"(wqr[3]), "+v"(wqr[4]), "+v"(xa), "+v"(xb), "+v"(xc), "+v"(epoch));
  ld3_first(ke[0], voff, rk, so0 + NS * ITEMB); ld3_first(ve[0], voff, rv, so0 + NS * ITEMB);
  const unsigned tag = ((__builtin_amdgcn_readfirstlane(epoch) + 0x1E3779B9u) & 0x1FFFFFFFu) | 0x20000000u;
  SSTAMP(1);
  {
    const unsigned xw[10] = {xa.x, xa.y, xa.z, xa.w, xb.x, xb.y, xb.z, xb.w, xc.x, xc.y};
    float s = 0.f, sq = 0.f;
#pragma unroll
    for (int k = 0; k < 10; ++k) { s = dot2(xw[k], 0x3f803f80u, s); sq = dot2(xw[k], xw[k], sq); }
    s = rowsum16(sw16add(s, sq)) * (1.0f / DM);
    const auto sx = __builtin_amdgcn_permlane16_swap(fbits(s), fbits(s), false, false);
    const float mean = ubits(sx[0]), mb = bfr(mean), rd = ln_rden(fmaxf(ubits(sx[1]) - mean * mean, 0.f));
    const float mb0 = rdlane(mb, 0), mb1 = rdlane(mb, 32), rd0 = rdlane(rd, 0), rd1 = rdlane(rd, 32);
    const CAS unsigned* x32 = (const CAS unsigned*)a.x + 40 * j + 5 * w;
    float qa[8], qb[8];
#pragma unroll
    for (int e = 0; e < 8; ++e) { qa[e] = 0.f; qb[e] = 0.f; }
#pragma unroll
    for (int i = 0; i < 5; ++i) {
      const unsigned u0 = x32[i], u1 = x32[DM / 2 + i];
      const float n0 = ln_val(hf ? bfhi(u0) : bflo(u0), mb0, rd0), n1 = ln_val(hf ? bfhi(u1) : bflo(u1), mb1, rd1);
#pragma unroll
      for (int e = 0; e < 4; ++e) {
        const float w0 = bflo(wqr[i][e]), w1 = bfhi(wqr[i][e]);
        qa[2 * e] = fmaf(n0, w0, qa[2 * e]); qa[2 * e + 1] = fmaf(n0, w1, qa[2 * e + 1]);
        qb[2 * e] = fmaf(n1, w0, qb[2 * e]); qb[2 * e + 1] = fmaf(n1, w1, qb[2 * e + 1]);
      }
    }
    float r[8];
#pragma unroll
    for (int e = 0; e < 8; ++e) r[e] = sw32add(qa[e], qb[e]);
    if (l5 < 24) {
      LAS v4f* dst = (LAS v4f*)(L + L_QP + ((w * 2 + hf) * DH + l5 * 8) * 4);
      dst[0] = v4f{r[0], r[1], r[2], r[3]}; dst[1] = v4f{r[4], r[5], r[6], r[7]};
    }
  }
  if (w == 0) asm volatile("s_waitcnt vmcnt(0) ; HELPER_ONLY" ::: "memory");
  BARRIER();
  SSTAMP(2);

  if (w == 0) {
    asm volatile("; HELPER_ENTRY");
    {
      const LAS float* QP = (const LAS float*)(L + L_QP);
#pragma unroll
      for (int k = 0; k < 3; ++k) {
        float s0 = 0.f, s1 = 0.f;
#pragma unroll
        for (int ww = 0; ww < NW; ++ww) { s0 += QP[(ww * 2) * DH + lane + 64 * k]; s1 += QP[(ww * 2 + 1) * DH + lane + 64 * k]; }
        __builtin_amdgcn_raw_buffer_store_b128(v4u{fbits(s0), tag, fbits(s1), tag}, rs, WS_Q + ((h * 8 + j) * DH + lane + 64 * k) * 16, 0, 16);
      }
      if (lane == 0) CT[1] = 1u;
      SSTAMP(13);
    }
    BARRIER();
    SSTAMP(3);
    int filled = 0;
    for (int spin = 0; spin < 16 * SPIN_CAP; ++spin) {
      int need = (int)__builtin_amdgcn_readfirstlane(CT[0]) + LEAD - (STAT + filled);
      if (need > 0) {
        need = need > 32 ? 32 : need;
        unsigned old = 0;
        if (lane == 0) old = __hip_atomic_fetch_add(ctr, (unsigned)need, __ATOMIC_RELAXED, __HIP_MEMORY_SCOPE_AGENT);
        old = __builtin_amdgcn_readfirstlane(old);
        const bool done = old + need >= (unsigned)POOL;
        if (lane < (done ? need + NW : need)) DYN[filled + lane] = (lane < need && old + lane < (unsigned)POOL) ? B1OFF + (8 * S1 + old + lane) * ITEMB : END;
        filled += need;
        if (done) break;
      } else __builtin_amdgcn_s_sleep(2);
    }
  } else {
    asm volatile("; STREAM_ENTRY");
    if (w == NW - 1) {
      hint(CT + 1, 10);
      v4u g[24];
      gather<24>(rs, WS_Q + (h * 8 * DH + lane) * 16, tag, g);
      SSTAMP(13);
      LAS unsigned short* QS = (LAS unsigned short*)(L + L_QS);
#pragma unroll
      for (int k = 0; k < 3; ++k) {
        float s0 = 0.f, s1 = 0.f;
#pragma unroll
        for (int p = 0; p < 8; ++p) { s0 += ubits(g[p * 3 + k].x); s1 += ubits(g[p * 3 + k].z); }
        const unsigned u = pk(s0, s1);
        QS[lane + 64 * k] = (unsigned short)u; QS[DH + lane + 64 * k] = (unsigned short)(u >> 16);
      }
    }
#pragma unroll
    for (int e = 1; e < NEXTRA; ++e) {
      if (w != NW - 1) waitvm<3>();
      ld3_first(ke[e], voff, rk, so0 + (e + 1) * NS * ITEMB);
      if (w != NW - 1) waitvm<3>();
      ld3_first(ve[e], voff, rv, so0 + (e + 1) * NS * ITEMB);
    }
    BARRIER();
    SSTAMP(3);
    v4u q[3];
#pragma unroll
    for (int jj = 0; jj < 3; ++jj) q[jj] = *(const LAS v4u*)(L + L_QS + ((lane & 7) + 8 * jj) * 16);
    Soft st;
    st.m = M_INIT; st.l = 0.f;
#pragma unroll
    for (int k = 0; k < 24; ++k) st.acc[k] = 0.f;
    auto step = [&](unsigned nxt) {
      wait3<3>(km);
      float s = score(km, q);
      asm volatile("" : "+v"(s));
      ld3_again(km, voff, rk, nxt);
      const float p = soft(st, s);
      wait3<3>(vm);
      pv(st, p, vm);
      __builtin_amdgcn_sched_barrier(0);
      ld3_again(vm, voff, rv, nxt);
    };
#pragma unroll
    for (int e = 0; e < NEXTRA; ++e) {
      step(so0 + (NEXTRA + 1 + e) * NS * ITEMB);
      pin3(ke[e]); pin3(ve[e]);
      const float pe = soft(st, score(ke[e], q));
      pv(st, pe, ve[e]);
    }
    unsigned cur = so0;
#pragma unroll 1
    for (int b = 0; b < 2; ++b) {
      while (cur != END && (cur >= B1OFF) == (b == 1)) {
        unsigned t = 0;
        if (lane == 0 && w <= NACT) t = __hip_atomic_fetch_add((LAS unsigned*)(L + L_CT), 1u, __ATOMIC_RELAXED, __HIP_MEMORY_SCOPE_WORKGROUP);
        t = __builtin_amdgcn_readfirstlane(t);
        if (w > NACT) cur = END;
        else if (t < (unsigned)STAT) cur = (t < (unsigned)S0 ? sb0 : sb1) + t * ITEMB;
        else {
          int spin = 0;
          while ((cur = __builtin_amdgcn_readfirstlane(DYN[t - STAT])) == EMPTY) {
            __builtin_amdgcn_s_sleep(1);
            if (++spin > SPIN_CAP) { cur = END; break; }
          }
        }
        step(cur);
      }
      wait3<0>(km); wait3<0>(vm);
      if (b == 1) {
        unsigned t = 0;
        if (lane == 0) t = __hip_atomic_fetch_add((LAS unsigned*)(L + L_CT) + 4, 1u, __ATOMIC_RELAXED, __HIP_MEMORY_SCOPE_WORKGROUP);
        if (__builtin_amdgcn_readfirstlane(t) == NS - 1) __builtin_amdgcn_raw_buffer_store_b32(tag, rs, lane == 0 ? WS_G + wg * 128 : OOB, 0, 16);
      }
      float M = fmaxf(st.m, dpp<ROR8>(st.m)); M = sw32max(sw16max(M));
      const float sc = __builtin_amdgcn_exp2f(st.m - M);
      float l = st.l * sc; l += dpp<ROR8>(l); l = sw16add(l, l); l = sw32add(l, l);
      float t1[12], t2[6];
#pragma unroll
      for (int k = 0; k < 12; ++k) t1[k] = sw16add(st.acc[k] * sc, st.acc[k + 12] * sc);
#pragma unroll
      for (int k = 0; k < 6; ++k) { t2[k] = sw32add(t1[k], t1[k + 6]); t2[k] += dpp<ROR8>(t2[k]); }
      LAS float* rec = (LAS float*)(L + L_REC) + ((w - 1) * 2 + b) * RECF;
      if (!(lane & 8)) {
        const int a0 = 6 * hf + 12 * ((lane >> 4) & 1);
#pragma unroll
        for (int k = 0; k < 6; k += 2) { const int ai = a0 + k; *(LAS v2f*)(rec + 4 + ((lane & 7) + 8 * (ai >> 3)) * 8 + (ai & 7)) = v2f{t2[k], t2[k + 1]}; }
      }
      if (lane == 0) *(LAS v2f*)rec = v2f{M, l};
#pragma unroll
      for (int jj = 0; jj < 3; ++jj) q[jj] = *(const LAS v4u*)(L + L_QS + DH * 2 + ((lane & 7) + 8 * jj) * 16);
      st.m = M_INIT; st.l = 0.f;
#pragma unroll
      for (int k = 0; k < 24; ++k) st.acc[k] = 0.f;
    }
  }
  STAMP(4);
  const int g4 = lane >> 4, c16 = lane & 15, tq = c16 >> 2, tp = lane & 3;
  const v4f ctag = {0.f, ubits(tag), 0.f, ubits(tag)};
  const v4u zero4 = {0u, 0u, 0u, 0u};
  BARRIER();
  STAMP(5);
  const bool tilew = w == 0 || w >= 4;
  const int tile = w ? w - 3 : 0;
  unsigned xv0 = 0, xv1 = 0;
  if (!tilew) {
    const int dim = tid - 64;
    {
      const LAS float* rec = (const LAS float*)(L + L_REC);
      v2f ml = *(const LAS v2f*)(rec + (c16 < 2 * NS ? c16 : 0) * RECF);
      float av[2 * NS];
#pragma unroll
      for (int r = 0; r < 2 * NS; ++r) av[r] = rec[r * RECF + 4 + dim];
      if (c16 >= 2 * NS) ml = v2f{M_INIT, 0.f};
      float M = fmaxf(ml.x, dpp<XOR2>(ml.x)); M = fmaxf(M, dpp<ROR4>(M)); M = fmaxf(M, dpp<ROR8>(M));
      const float e = __builtin_amdgcn_exp2f(ml.x - M);
      float ls = e * ml.y; ls += dpp<XOR2>(ls); ls += dpp<ROR4>(ls); ls += dpp<ROR8>(ls);
      float o0 = 0.f, o1 = 0.f;
#pragma unroll
      for (int r = 0; r < 2 * NS; r += 2) { o0 = fmaf(rdlane(e, r), av[r], o0); o1 = fmaf(rdlane(e, r + 1), av[r + 1], o1); }
      put(rs, WS_P + wg * PBLK + dim * 16, fbits(o0), fbits(o1), tag);
      const float lsx = dpp<XOR1>(ls), Mx = dpp<XOR1>(M);
      put(rs, dim < 2 ? WS_P + wg * PBLK + (DH + dim) * 16 : OOB, fbits((lane & 1) ? lsx : M), fbits((lane & 1) ? ls : Mx), tag);
      STAMP(6);
    }
    v4u g[8], gm = zero4;
#pragma unroll
    for (int p = 0; p < 8; ++p) g[p] = zero4;
    const int po = WS_P + h * 8 * PBLK + dim * 16, pm = WS_P + (h * 8 + ((lane >> 1) & 7)) * PBLK + (DH + (lane & 1)) * 16;
    for (unsigned sent = 0, spin = 0; sent != 0xffu && spin < 4 * SPIN_CAP; ++spin) {
      const unsigned want = __builtin_amdgcn_readfirstlane(CT[5]) & ~sent & 0xffu;
#pragma unroll
      for (int p = 0; p < 8; ++p)
        if ((want >> p) & 1u) g[p] = __builtin_amdgcn_raw_buffer_load_b128(rs, po, p * PBLK, 16);
      sent |= want;
    }
    for (int spin = 0; spin < SPIN_CAP; ++spin) {
      gm = __builtin_amdgcn_raw_buffer_load_b128(rs, pm, 0, 16);
      bool ok = true;
#pragma unroll
      for (int p = 0; p < 8; ++p)
        if (!__all(g[p].y == tag && g[p].w == tag)) { g[p] = __builtin_amdgcn_raw_buffer_load_b128(rs, po, p * PBLK, 16); ok = false; }
      if (ok && __all(gm.y == tag && gm.w == tag)) break;
      asm volatile("" ::: "memory");
    }
    STAMP(7);
    float M0 = ubits(gm.x), M1 = ubits(gm.z);
    M0 = fmaxf(M0, dpp<XOR2>(M0)); M1 = fmaxf(M1, dpp<XOR2>(M1)); M0 = fmaxf(M0, dpp<ROR4>(M0)); M1 = fmaxf(M1, dpp<ROR4>(M1)); M0 = fmaxf(M0, dpp<ROR8>(M0)); M1 = fmaxf(M1, dpp<ROR8>(M1));
    const float e0 = __builtin_amdgcn_exp2f(ubits(gm.x) - M0), e1 = __builtin_amdgcn_exp2f(ubits(gm.z) - M1);
    float l0 = e0 * dpp<XOR1>(ubits(gm.x)), l1 = e1 * dpp<XOR1>(ubits(gm.z));
    l0 += dpp<XOR2>(l0); l1 += dpp<XOR2>(l1); l0 += dpp<ROR4>(l0); l1 += dpp<ROR4>(l1); l0 += dpp<ROR8>(l0); l1 += dpp<ROR8>(l1);
    float o0 = 0.f, o1 = 0.f;
#pragma unroll
    for (int p = 0; p < 8; ++p) { o0 = fmaf(rdlane(e0, 2 * p), ubits(g[p].x), o0); o1 = fmaf(rdlane(e1, 2 * p), ubits(g[p].z), o1); }
    const unsigned ob = pk(o0 * __builtin_amdgcn_rcpf(rdlane(l0, 0)), o1 * __builtin_amdgcn_rcpf(rdlane(l1, 0)));
    ((LAS unsigned short*)(L + L_O))[dim] = (unsigned short)ob; ((LAS unsigned short*)(L + L_O))[DH + dim] = (unsigned short)(ob >> 16);
  } else {
    if (w == 0) load_weights();
    xv0 = a.x[80 * j + 16 * tile + c16]; xv1 = a.x[DM + 80 * j + 16 * tile + c16];
    if (w == 0) { if (ffn) waitvm<30>(); else waitvm<0>(); }
    if (w == NW - 1) { watch<8>(rs, WS_G + h * 8 * 128, 128, lane, tag, CT + 5); STAMP(13); }
  }
  BARRIER();
  STAMP(1);
  if (tilew) {
    const int ybase = WS_Y + (j * 5 + tile) * (NH * 256);
    __builtin_amdgcn_raw_buffer_store_b32(tag, rs, tid == 0 ? WS_D + wg * 128 : OOB, 0, 16);
    v4f d = ctag;
#pragma unroll
    for (int k = 0; k < 6; ++k) {
      const v4u ao = *(const LAS v4u*)(L + L_O + (c16 & 2) * DH + (32 * k + 8 * g4) * 2);
      d = mfma((c16 & 1) ? zero4 : ao, bfrag<160>(L + L_WO + (32 * k + 8 * g4 + tq) * 160 + (16 * tile + 4 * tp) * 2), d);
    }
    __builtin_amdgcn_raw_buffer_store_b128(__builtin_bit_cast(v4u, d), rs, lane < 16 ? ybase + h * 256 + c16 * 16 : OOB, 0, 16);
    STAMP(8);
    if (ffn) {
      v4u g[8];
#pragma unroll
      for (int k = 0; k < 8; ++k) g[k] = zero4;
      for (unsigned sent = 0, spin = 0; sent != 0x11111111u && spin < 4 * SPIN_CAP; ++spin) {
        unsigned m = __builtin_amdgcn_readfirstlane(CT[6]);
        m &= m >> 1; m &= m >> 2; m &= 0x11111111u & ~sent;
#pragma unroll
        for (int k = 0; k < 8; ++k)
          if ((m >> (4 * k)) & 1u) g[k] = __builtin_amdgcn_raw_buffer_load_b128(rs, ybase + lane * 16 + (k & 3) * 1024, (k >> 2) * 4096, 16);
        sent |= m;
      }
      for (int spin = 0; spin < SPIN_CAP; ++spin) {
        bool ok = true;
#pragma unroll
        for (int k = 0; k < 8; ++k)
          if (!__all(g[k].y == tag && g[k].w == tag)) { g[k] = __builtin_amdgcn_raw_buffer_load_b128(rs, ybase + lane * 16 + (k & 3) * 1024, (k >> 2) * 4096, 16); ok = false; }
        if (ok) break;
        asm volatile("" ::: "memory");
      }
      STAMP(2);
      const float s0 = ((ubits(g[0].x) + ubits(g[1].x)) + (ubits(g[2].x) + ubits(g[3].x))) + ((ubits(g[4].x) + ubits(g[5].x)) + (ubits(g[6].x) + ubits(g[7].x)));
      const float s1 = ((ubits(g[0].z) + ubits(g[1].z)) + (ubits(g[2].z) + ubits(g[3].z))) + ((ubits(g[4].z) + ubits(g[5].z)) + (ubits(g[6].z) + ubits(g[7].z)));
      float s = sw16add(s0, s1); s = sw32add(s, s);
      const float hv = bflo((g4 & 1) ? xv1 : xv0) + bfr(s);
      if (lane < 32) ((LAS unsigned short*)(L + L_HS))[g4 * 96 + 16 * tile + c16] = (unsigned short)pk(hv, 0.f);
    }
  } else if (ffn && w == 1) { watch<32>(rs, WS_D + j * 128, 8 * 128, lane, tag, CT + 6); STAMP(14); }
  if (ffn) {
    int eo = WS_E + (cg * 8 + w) * 5120;
    const int eo0 = lane < 32 ? eo + j * 512 + lane * 16 : OOB, eo1 = lane < 8 ? eo + 4096 + j * 128 + lane * 16 : OOB;
    if (w == 0) waitvm<0>();
    BARRIER();
    STAMP(3);
    {
      v4f d = ctag, ds = ctag;
#pragma unroll
      for (int k = 0; k < 3; ++k) {
        v4u ah = *(const LAS v4u*)(L + L_HS + (c16 < 5 ? c16 >> 1 : 0) * 192 + (32 * k + 8 * g4) * 2);
        if ((c16 & 1) || c16 > 4) ah = zero4;
        const int row = 32 * k + 8 * g4 + tq;
        d = mfma(ah, bfrag<128>(L + L_G + (tp >> 1) * 10240 + (row < 80 ? row : tq) * 128 + (8 * w + 4 * (tp & 1)) * 2), d);
        ds = mfma(ah, ah, ds);
      }
      __builtin_amdgcn_raw_buffer_store_b128(__builtin_bit_cast(v4u, d), rs, eo0, 0, 16);
      __builtin_amdgcn_raw_buffer_store_b128(__builtin_bit_cast(v4u, ds), rs, eo1, 0, 16);
    }
    STAMP(9);
    if (w >= 1 && w <= 4) {
      v4u g[5], bdn[3]; int zo[3];
#pragma unroll
      for (int T = 0; T < 3; ++T) {
        const int dt = w - 1 + 4 * T, n = 64 * dt + lane;
        bdn[T] = bfrag<1280>(L + L_WD + tq * 1280 + (64 * (dt < 10 ? dt : 0) + 16 * g4 + 4 * tp) * 2);
        zo[T] = dt < 10 ? WS_Z + ((n >> 2) * NFFN + f) * 64 + (n & 3) * 16 : OOB;
        asm volatile("" : "+v"(zo[T]));
      }
      __builtin_amdgcn_s_sleep(10);
      gather<5>(rs, WS_E + f * 5120 + lane * 16, tag, g);
      __builtin_amdgcn_raw_buffer_store_b32(tag, rs, (tid == 64 && j == 0) ? WS_F + cg * 128 : OOB, 0, 16);
      STAMP(10);
      const float s0 = (ubits(g[0].x) + ubits(g[1].x)) + (ubits(g[2].x) + ubits(g[3].x)), s1 = (ubits(g[0].z) + ubits(g[1].z)) + (ubits(g[2].z) + ubits(g[3].z));
      const float gs = sw32add(s0, s1);
      const float cs0 = sw32add(s0, s0);
      const auto cx = __builtin_amdgcn_permlane16_swap(fbits(cs0), fbits(cs0), false, false);
      const auto gx = __builtin_amdgcn_permlane16_swap(fbits(gs), fbits(gs), false, false);
      float t0 = ubits(g[4].x), t1 = ubits(g[4].z);
      t0 += dpp<ROR8>(t0); t1 += dpp<ROR8>(t1);
      t0 = sw16add(t0, t0); t1 = sw16add(t1, t1);
      const float tt = sw32add(t0, t1) * (1.0f / DM);
      const float mean = dpp<BC + 4>(tt), q0 = dpp<BC + 0>(tt), q1 = dpp<BC + 2>(tt);
      const float rdv = ln_rden(fmaxf((hf ? q1 : q0) - mean * mean, 0.f));
      const float gv = bfr((ubits(gx[0]) - bfr(mean) * ubits(cx[1])) * rdv), uv = dpp<ROR8>(gv);
      const float y2 = (-2.0f * 0.7978845608028654f * 1.4426950408889634f) * (gv + 0.044715f * gv * gv * gv);
      const float act = bfr(gv * __builtin_amdgcn_rcpf(1.0f + __builtin_amdgcn_exp2f(y2))) * uv;
      const unsigned ap = pk(act, dpp<XOR1>(act));
      const auto ax = __builtin_amdgcn_permlane32_swap(ap, ap, false, false);
      const unsigned keep = ((c16 >> 2) == g4 && !(c16 & 1)) ? 0xffffffffu : 0u, k1 = (c16 & 2) ? keep : 0u, k0 = keep ^ k1;
      const v4u b0 = {dppu<BC + 0>(ax[0]), dppu<BC + 2>(ax[0]), dppu<BC + 4>(ax[0]), dppu<BC + 6>(ax[0])};
      const v4u b1 = {dppu<BC + 0>(ax[1]), dppu<BC + 2>(ax[1]), dppu<BC + 4>(ax[1]), dppu<BC + 6>(ax[1])};
      const v4u a2 = (b0 & k0) | (b1 & k1);
#pragma unroll
      for (int T = 0; T < 3; ++T) __builtin_amdgcn_raw_buffer_store_b128(__builtin_bit_cast(v4u, mfma(a2, bdn[T], ctag)), rs, zo[T], 0, 16);
      STAMP(11);
    } else if (w == NW - 1 && cg < 20) {
      const __amdgpu_buffer_rsrc_t ro = __builtin_amdgcn_make_buffer_rsrc((void*)a.out, 0, 2 * DM * 2, 0x00020000);
      const float hv = bflo(((const LAS unsigned short*)(L + L_HS))[(g4 & 1) * 96 + 4 * cg + (lane & 3)]);
      v4u g[12];
      watch<24>(rs, WS_F, 128, lane, tag, CT + 7);
      STAMP(15);
      gather<12>(rs, WS_Z + (20 * j + cg) * (NFFN * 64) + lane * 16, tag, g);
      float s0[4], s1[4];
#pragma unroll
      for (int k = 0; k < 4; ++k) { s0[k] = (ubits(g[k].x) + ubits(g[k + 4].x)) + ubits(g[k + 8].x); s1[k] = (ubits(g[k].z) + ubits(g[k + 4].z)) + ubits(g[k + 8].z); }
      float s = sw16add((s0[0] + s0[1]) + (s0[2] + s0[3]), (s1[0] + s1[1]) + (s1[2] + s1[3]));
      s = sw32add(s, s); s += dpp<ROR4>(s); s += dpp<ROR8>(s);
      const unsigned o = pk(hv + bfr(s), 0.f);
      __builtin_amdgcn_raw_buffer_store_b16((unsigned short)o, ro, (lane < 32 && c16 < 4) ? (g4 * DM + 80 * j + 4 * cg + c16) * 2 : OOB, 0, 0);
      __builtin_amdgcn_raw_buffer_store_b32(tag, rs, (wg == 0 && lane == 0) ? 0 : OOB, 0, 0);
      STAMP(12);
    }
  }
  STAMP_DUMP();
}
}

extern "C" void kernel_launch(void* const* d_in, const int* in_sizes, int n_in, void* d_out, int out_size, void* d_ws, size_t ws_size, hipStream_t stream) {
  Args a;
  a.x = (const unsigned short*)d_in[0]; a.k = (const unsigned short*)d_in[1]; a.v = (const unsigned short*)d_in[2];
  a.wq = (const unsigned short*)d_in[3]; a.wo = (const unsigned short*)d_in[4];
  a.wg = (const unsigned short*)d_in[6]; a.wu = (const unsigned short*)d_in[7]; a.wd = (const unsigned short*)d_in[8];
  a.out = (unsigned short*)d_out; a.ws = (unsigned char*)d_ws;
  hipLaunchKernelGGL(decode_block, dim3(NWG), dim3(NTHR), 0, stream, a);
}
```

```cpp
#include <hip/hip_runtime.h>
#include <hip/hip_bf16.h>
#include <stdint.h>

#define PACE 3
#define NEX 5
#define POLLSLEEP 1
#define REWARM 1
#define STAT1 192
#define LEADN 8
#define WSLEEP 1
#define GPOS 0
#define DPOS 0
#define FPOS 0
#define NACT 7
#define GRAB 1
#define GRABEND 128
#define QDELAY 10
#define EDELAY 10
#define ZDELAY 0
#define MIMIC_SCORE 1

namespace {
typedef unsigned v4u __attribute__((ext_vector_type(4)));
typedef unsigned v2u __attribute__((ext_vector_type(2)));
typedef float v4f __attribute__((ext_vector_type(4)));
typedef float v2f __attribute__((ext_vector_type(2)));
typedef short v4s __attribute__((ext_vector_type(4)));
typedef short v8s __attribute__((ext_vector_type(8)));
typedef __bf16 bf2 __attribute__((ext_vector_type(2)));
typedef __bf16 bf8 __attribute__((ext_vector_type(8)));
#define LAS __attribute__((address_space(3)))
#define CAS __attribute__((address_space(4)))

constexpr int DM = 640, NH = 32, SK = 16384, DH = 192, FF = 1536;
constexpr int NWG = 256, NW = 8, NS = NW - 1, NTHR = NW * 64;
constexpr unsigned ITEMB = 8 * DH * 2;
constexpr unsigned HEADB = (unsigned)SK * DH * 2, B1OFF = NH * HEADB;
constexpr int IPH = SK / 8;
constexpr int S0 = IPH / 8, S1 = STAT1, STAT = S0 + S1;
constexpr int POOL = IPH - 8 * S1, LEAD = LEADN, NEXTRA = NEX;
constexpr unsigned END = 0xF0000000u, EMPTY = 0xFFFFFFFFu;
constexpr int NRED = DM / 4, NFFN = FF / 8;
constexpr int SPIN_CAP = 1 << 17;
constexpr float M_INIT = -3.0e38f;

constexpr int WS_CTR = 4096, CTR_STRIDE = 4352;
constexpr int WS_F = 0x24000, WS_D = 0x28000, WS_G = 0x30000, WS_Q = 0x40000, WS_P = 0x100000, PBLK = 3200, WS_Y = 0x200000, WS_E = 0x280000, WS_Z = 0x400000, WS_BYTES = 0x600000;
constexpr int OOB = (int)0x80000000u;
static_assert(WS_CTR + NH * CTR_STRIDE <= WS_F && WS_F + 24 * 128 <= WS_D && WS_D + NWG * 128 <= WS_G && WS_G + NWG * 128 <= WS_Q && WS_Q + NWG * DH * 16 <= WS_P && WS_P + NWG * PBLK <= WS_Y, "ws map");
static_assert(WS_Y + NWG * 80 * 16 <= WS_E && WS_E + NFFN * 5120 <= WS_Z && WS_Z + NRED * NFFN * 64 <= WS_BYTES, "ws map");

constexpr int L_WO = 0, L_G = L_WO + DH * 160, L_WD = L_G + 24 * 1024, L_QP = L_WD + 8 * 1280;
constexpr int L_QS = L_QP + NW * 2 * DH * 4, RECF = DH + 4, L_REC = L_QS + 2 * DH * 2, L_O = L_REC + NS * 2 * RECF * 4, L_HS = L_O + 2 * DH * 2;
constexpr int L_CT = L_HS + 3 * 192, DYNMAX = POOL + 96, L_DYN = L_CT + 64, L_PF = L_DYN + DYNMAX * 4, L_BYTES = L_PF + NW * 1024;
static_assert(L_BYTES > 81920 && L_BYTES <= 160 * 1024 && L_REC % 16 == 0 && L_O % 16 == 0 && L_HS % 16 == 0 && L_PF % 16 == 0, "one workgroup per CU");

struct Args {
  const unsigned short *x, *k, *v, *wq, *wo, *wg, *wu, *wd;
  unsigned short* out;
  unsigned char* ws;
};

__device__ __forceinline__ float dot2(unsigned a, unsigned b, float c) {
  return __builtin_amdgcn_fdot2_f32_bf16(__builtin_bit_cast(bf2, a), __builtin_bit_cast(bf2, b), c, false);
}
template <int CTRL> __device__ __forceinline__ float dpp(float x) {
  return __builtin_bit_cast(float, __builtin_amdgcn_mov_dpp(__builtin_bit_cast(int, x), CTRL, 0xf, 0xf, true));
}
template <int CTRL> __device__ __forceinline__ unsigned dppu(unsigned x) { return (unsigned)__builtin_amdgcn_mov_dpp((int)x, CTRL, 0xf, 0xf, true); }
constexpr int BC = 0x150;
constexpr int XOR1 = 0xB1, XOR2 = 0x4E, HMIR = 0x141, ROR4 = 0x124, ROR8 = 0x128;
__device__ __forceinline__ float bflo(unsigned x) { return __builtin_bit_cast(float, x << 16); }
__device__ __forceinline__ float bfhi(unsigned x) { return __builtin_bit_cast(float, x & 0xffff0000u); }
__device__ __forceinline__ unsigned pk(float lo, float hi) {
  const bf2 r = {(__bf16)lo, (__bf16)hi};
  return __builtin_bit_cast(unsigned, r);
}
__device__ __forceinline__ float bfr(float x) { return (float)(__bf16)x; }
__device__ __forceinline__ unsigned fbits(float x) { return __builtin_bit_cast(unsigned, x); }
__device__ __forceinline__ float ubits(unsigned x) { return __builtin_bit_cast(float, x); }
__device__ __forceinline__ float sw16add(float a, float b) {
  const auto s = __builtin_amdgcn_permlane16_swap(fbits(a), fbits(b), false, false);
  return ubits(s[0]) + ubits(s[1]);
}
__device__ __forceinline__ float sw32add(float a, float b) {
  const auto s = __builtin_amdgcn_permlane32_swap(fbits(a), fbits(b), false, false);
  return ubits(s[0]) + ubits(s[1]);
}
__device__ __forceinline__ float sw16max(float a) {
  const auto s = __builtin_amdgcn_permlane16_swap(fbits(a), fbits(a), false, false);
  return fmaxf(ubits(s[0]), ubits(s[1]));
}
__device__ __forceinline__ float sw32max(float a) {
  const auto s = __builtin_amdgcn_permlane32_swap(fbits(a), fbits(a), false, false);
  return fmaxf(ubits(s[0]), ubits(s[1]));
}
__device__ __forceinline__ float rowsum16(float s) { s += dpp<XOR1>(s); s += dpp<XOR2>(s); s += dpp<HMIR>(s); s += dpp<ROR8>(s); return s; }
__device__ __forceinline__ float rdlane(float x, int l) { return ubits(__builtin_amdgcn_readlane(fbits(x), l)); }
__device__ __forceinline__ v4u mkrsrc(const void* p, unsigned bytes) {
  const unsigned long long a = (unsigned long long)p;
  return v4u{(unsigned)a, (unsigned)(a >> 32) & 0xffffu, bytes, 0x00020000u};
}
__device__ __forceinline__ v4f mfma(v4u a, v4u b, v4f c) {
  return __builtin_amdgcn_mfma_f32_16x16x32_bf16(__builtin_bit_cast(bf8, a), __builtin_bit_cast(bf8, b), c, 0, 0, 0);
}
__device__ __forceinline__ v2u trread(LAS unsigned char* p) {
  return __builtin_bit_cast(v2u, __builtin_amdgcn_ds_read_tr16_b64_v4i16((LAS v4s*)p));
}
#define BARRIER() asm volatile("s_waitcnt lgkmcnt(0)\n\ts_barrier" ::: "memory")

#define LD3 "s_nop 4\n\tbuffer_load_dwordx4 %0, %3, %4, %5 offen nt\n\tbuffer_load_dwordx4 %1, %3, %4, %5 offen offset:128 nt\n\tbuffer_load_dwordx4 %2, %3, %4, %5 offen offset:256 nt"
__device__ __forceinline__ void ld_first(v4u& d, int voff, v4u rs, unsigned soff) {
  asm volatile("s_nop 4\n\tbuffer_load_dwordx4 %0, %1, %2, %3 offen nt" : "=v"(d) : "v"(voff), "s"(rs), "s"(soff));
}
__device__ __forceinline__ void ld3_first(v4u (&r)[3], int voff, v4u rs, unsigned so) { asm volatile(LD3 : "=&v"(r[0]), "=&v"(r[1]), "=&v"(r[2]) : "v"(voff), "s"(rs), "s"(so)); }
__device__ __forceinline__ void ld3_again(v4u (&r)[3], int voff, v4u rs, unsigned so) { asm volatile(LD3 : "+v"(r[0]), "+v"(r[1]), "+v"(r[2]) : "v"(voff), "s"(rs), "s"(so)); }
template <int N> __device__ __forceinline__ void wait3(v4u (&r)[3]) { asm volatile("s_waitcnt vmcnt(%3)" : "+v"(r[0]), "+v"(r[1]), "+v"(r[2]) : "i"(N)); }
__device__ __forceinline__ void pin3(v4u (&r)[3]) { asm volatile("" : "+v"(r[0]), "+v"(r[1]), "+v"(r[2])); }
template <int N> __device__ __forceinline__ void waitvm() { asm volatile("s_waitcnt vmcnt(%0)" : : "i"(N) : "memory"); }

template <bool NT> __device__ __forceinline__ void dma16(const void* base, int voff, unsigned lds) {
  unsigned keep;
  if (NT) asm volatile("s_mov_b32 %0, m0\n\ts_mov_b32 m0, %3\n\ts_nop 2\n\tglobal_load_lds_dwordx4 %1, %2 nt\n\ts_mov_b32 m0, %0" : "=&s"(keep) : "v"(voff), "s"(base), "s"(lds) : "memory");
  else asm volatile("s_mov_b32 %0, m0\n\ts_mov_b32 m0, %3\n\ts_nop 2\n\tglobal_load_lds_dwordx4 %1, %2\n\ts_mov_b32 m0, %0" : "=&s"(keep) : "v"(voff), "s"(base), "s"(lds) : "memory");
}

__device__ __forceinline__ void put(__amdgpu_buffer_rsrc_t rs, int off, unsigned a, unsigned b, unsigned tag) {
  __builtin_amdgcn_raw_buffer_store_b128(v4u{a, tag, b, tag}, rs, off, 0, 16);
}
#define STAMP(n)
#define SSTAMP(n)
#define STAMP_INIT()
#define STAMP_DUMP()
#define GDBG
#define GSTAMP()
#define GARG(n)
template <int N> __device__ __forceinline__ void gather(__amdgpu_buffer_rsrc_t rs, int off, unsigned tag, v4u (&t)[N] GDBG) {
  bool done = false;
#pragma unroll
  for (int k = 0; k < N; ++k) t[k] = v4u{0u, 0u, 0u, 0u};
  for (int spin = 0; spin < SPIN_CAP; ++spin) {
    if (!done) {
#pragma unroll
      for (int k = 0; k < N; ++k) t[k] = __builtin_amdgcn_raw_buffer_load_b128(rs, off + (k & 3) * 1024, (k >> 2) * 4096, 16);
      unsigned mn = tag, mx = tag;
#pragma unroll
      for (int k = 0; k < N; ++k) { mn = min(mn, min(t[k].y, t[k].w)); mx = max(mx, max(t[k].y, t[k].w)); }
      done = mn == mx;
    }
    GSTAMP();
    if (__all(done)) break;
    asm volatile("" ::: "memory");
    __builtin_amdgcn_s_sleep(POLLSLEEP);
  }
}

template <int N> __device__ __forceinline__ void watch(__amdgpu_buffer_rsrc_t rs, int off, int stride, int lane, unsigned tag, volatile LAS unsigned* mask) {
  bool seen = lane >= N;
  for (int spin = 0; spin < SPIN_CAP; ++spin) {
    if (!seen) seen = __builtin_amdgcn_raw_buffer_load_b32(rs, off + lane * stride, 0, 16) == tag;
    const unsigned long long m = __ballot(seen);
    if (lane == 0) *mask = (unsigned)m;
    if (m == ~0ull) break;
    asm volatile("" ::: "memory");
    __builtin_amdgcn_s_sleep(WSLEEP);
  }
  if (lane == 0) *mask = ~0u;
}
#define hint(p, D) do { for (int sp_ = 0; sp_ < SPIN_CAP && *(p) == 0u; ++sp_) __builtin_amdgcn_s_sleep(1); __builtin_amdgcn_s_sleep(D); } while (0)

__device__ __forceinline__ float ln_rden(float var) { return __builtin_amdgcn_rcpf(bfr(__builtin_amdgcn_sqrtf(bfr(bfr(var) + 1e-5f)))); }
__device__ __forceinline__ float ln_val(float x, float mean_b, float rden) { return bfr(bfr(x - mean_b) * rden); }

struct Soft { float m, l, acc[24]; };
__device__ __forceinline__ float score(const v4u (&kk)[3], const v4u (&q)[3]) {
  float s0 = 0.f, s1 = 0.f;
#pragma unroll
  for (int j = 0; j < 3; ++j) {
    s0 = dot2(kk[j].x, q[j].x, s0); s1 = dot2(kk[j].y, q[j].y, s1);
    s0 = dot2(kk[j].z, q[j].z, s0); s1 = dot2(kk[j].w, q[j].w, s1);
  }
  return s0 + s1;
}
__device__ __forceinline__ float soft(Soft& st, float s) {
  s += dpp<XOR1>(s); s += dpp<XOR2>(s); s += dpp<HMIR>(s);
  s = bfr(bfr(s) * 0.07216878364870322f) * 1.4426950408889634f;
  if (__any(s > st.m)) {
    const float mn = fmaxf(st.m, s), al = __builtin_amdgcn_exp2f(st.m - mn);
    st.l *= al;
#pragma unroll
    for (int k = 0; k < 24; ++k) st.acc[k] *= al;
    st.m = mn;
  }
  const float p = __builtin_amdgcn_exp2f(s - st.m);
  st.l += p;
  return p;
}
__device__ __forceinline__ void pv(Soft& st, float p, const v4u (&vv)[3]) {
#pragma unroll
  for (int j = 0; j < 3; ++j)
#pragma unroll
    for (int e = 0; e < 4; ++e) {
      st.acc[j * 8 + 2 * e] = fmaf(p, bflo(vv[j][e]), st.acc[j * 8 + 2 * e]);
      st.acc[j * 8 + 2 * e + 1] = fmaf(p, bfhi(vv[j][e]), st.acc[j * 8 + 2 * e + 1]);
    }
}

__global__ void __launch_bounds__(NTHR, 2) decode_block(Args a) {
  __shared__ __attribute__((aligned(16))) unsigned char smem[L_BYTES];
  LAS unsigned char* L = (LAS unsigned char*)smem;
  volatile LAS unsigned* CT = (volatile LAS unsigned*)(L + L_CT);
  volatile LAS unsigned* DYN = (volatile LAS unsigned*)(L + L_DYN);
  const int tid = threadIdx.x, lane = tid & 63, w = __builtin_amdgcn_readfirstlane(tid >> 6);
  const int wg = blockIdx.x, h = wg >> 3, j = wg & 7;
  const int hf = lane >> 5, l5 = lane & 31;
  const bool ffn = ((h + j) & 3) != 3;
  const int cg = 3 * (h >> 2) + (h & 3) - ((h & 3) > ((3 - j) & 3) ? 1 : 0), f = 8 * cg + j;
  const __amdgpu_buffer_rsrc_t rs = __builtin_amdgcn_make_buffer_rsrc((void*)a.ws, 0, WS_BYTES, 0x00020000);
  unsigned* ctr = (unsigned*)(a.ws + WS_CTR + h * CTR_STRIDE);
  STAMP_INIT();
  STAMP(0);

  unsigned long long pc; unsigned len;
  asm volatile("s_getpc_b64 %0\n.Ldb_here:\n\ts_mov_b32 %1, .Ldb_end-.Ldb_here" : "=s"(pc), "=s"(len));
  const v4u rc = mkrsrc((const void*)pc, len);
  {
    unsigned keep;
    const int vo = w * 1024 + lane * 16;
    asm volatile("s_mov_b32 %0, m0\n\ts_mov_b32 m0, %3\n\ts_nop 2\n\tbuffer_load_dwordx4 %1, %2, 0 offen lds\n\tbuffer_load_dwordx4 %1, %2, %4 offen lds\n\t"
                 "buffer_load_dwordx4 %1, %2, %5 offen lds\n\tbuffer_load_dwordx4 %1, %2, %6 offen lds\n\ts_mov_b32 m0, %0"
                 : "=&s"(keep) : "v"(vo), "s"(rc), "s"((unsigned)(uintptr_t)L + L_PF + w * 1024), "s"(8192), "s"(16384), "s"(24576) : "memory");
  }
  unsigned epoch;
  {
    const int zero = 0;
    if (w == 0) {
      __builtin_amdgcn_s_setprio(3);
      if (j == 0) asm volatile("s_nop 4\n\tglobal_store_dword %0, %0, %1 sc1" : : "v"(zero), "s"(ctr) : "memory");
    }
    asm volatile("s_nop 4\n\tglobal_load_dword %0, %1, %2 sc1" : "=v"(epoch) : "v"(zero), "s"(a.ws));
  }
  v4u wqr[5], xa, xb; v2u xc;
  {
    const size_t rowoff = (size_t)(80 * j + 10 * w) * (NH * DH * 2) + h * (DH * 2);
    const v4u rw = mkrsrc((const unsigned char*)a.wq + rowoff, (unsigned)((size_t)DM * NH * DH * 2 - rowoff));
    const int vo = l5 < 24 ? hf * (NH * DH * 2) + l5 * 16 : OOB;
#pragma unroll
    for (int i = 0; i < 5; ++i) ld_first(wqr[i], vo, rw, i * 2 * NH * DH * 2);
    const v4u rx = mkrsrc(a.x, 2 * DM * 2);
    const int xo = hf * (DM * 2) + l5 * 40;
    asm volatile("s_nop 4\n\tbuffer_load_dwordx4 %0, %3, %4, 0 offen\n\tbuffer_load_dwordx4 %1, %3, %4, 0 offen offset:16\n\tbuffer_load_dwordx2 %2, %3, %4, 0 offen offset:32"
                 : "=&v"(xa), "=&v"(xb), "=&v"(xc) : "v"(xo), "s"(rx));
  }
  const v4u rk = mkrsrc((const unsigned char*)a.k + (size_t)h * HEADB, B1OFF + HEADB), rv = mkrsrc((const unsigned char*)a.v + (size_t)h * HEADB, B1OFF + HEADB);
  const int voff = (lane >> 3) * (DH * 2) + (lane & 7) * 16;
  const unsigned sb0 = (unsigned)(S0 * j) * ITEMB, sb1 = B1OFF + (unsigned)(S1 * j - S0) * ITEMB;
  v4u km[3], vm[3], ke[NEXTRA][3], ve[NEXTRA][3];
  const unsigned so0 = w ? sb0 + (unsigned)(w - 1) * ITEMB : END;
  asm volatile("s_barrier" ::: "memory");
  ld3_first(km, voff, rk, so0); ld3_first(vm, voff, rv, so0);
  if (w == 0) {
    for (int k = lane; k < DYNMAX; k += 64) DYN[k] = EMPTY;
    if (lane < 48) {
      ((LAS unsigned*)(L + L_HS))[lane] = 0u; ((LAS unsigned*)(L + L_HS))[48 + lane] = 0u; ((LAS unsigned*)(L + L_HS))[96 + lane] = lane < 40 ? 0x3f803f80u : 0u;
    }
    if (lane < 8) CT[lane] = lane ? 0u : (2 * NEXTRA + 1) * NS;
  }
  asm volatile("s_waitcnt vmcnt(6)" : "+v"(wqr[0]), "+v"(wqr[1]), "+v"(wqr[2]), "+v"(wqr[3]), "+v"(wqr[4]), "+v"(xa), "+v"(xb), "+v"(xc), "+v"(epoch));
  ld3_first(ke[0], voff, rk, so0 + NS * ITEMB); ld3_first(ve[0], voff, rv, so0 + NS * ITEMB);
  const unsigned tag = ((__builtin_amdgcn_readfirstlane(epoch) + 0x1E3779B9u) & 0x1FFFFFFFu) | 0x20000000u;
  SSTAMP(1);
  {
    const unsigned xw[10] = {xa.x, xa.y, xa.z, xa.w, xb.x, xb.y, xb.z, xb.w, xc.x, xc.y};
    float s = 0.f, sq = 0.f;
#pragma unroll
    for (int k = 0; k < 10; ++k) { s = dot2(xw[k], 0x3f803f80u, s); sq = dot2(xw[k], xw[k], sq); }
    s = rowsum16(sw16add(s, sq)) * (1.0f / DM);
    const auto sx = __builtin_amdgcn_permlane16_swap(fbits(s), fbits(s), false, false);
    const float mean = ubits(sx[0]), mb = bfr(mean), rd = ln_rden(fmaxf(ubits(sx[1]) - mean * mean, 0.f));
    const float mb0 = rdlane(mb, 0), mb1 = rdlane(mb, 32), rd0 = rdlane(rd, 0), rd1 = rdlane(rd, 32);
    const CAS unsigned* x32 = (const CAS unsigned*)a.x + 40 * j + 5 * w;
    float qa[8], qb[8];
#pragma unroll
    for (int e = 0; e < 8; ++e) { qa[e] = 0.f; qb[e] = 0.f; }
#pragma unroll
    for (int i = 0; i < 5; ++i) {
      const unsigned u0 = x32[i], u1 = x32[DM / 2 + i];
      const float n0 = ln_val(hf ? bfhi(u0) : bflo(u0), mb0, rd0), n1 = ln_val(hf ? bfhi(u1) : bflo(u1), mb1, rd1);
#pragma unroll
      for (int e = 0; e < 4; ++e) {
        const float w0 = bflo(wqr[i][e]), w1 = bfhi(wqr[i][e]);
        qa[2 * e] = fmaf(n0, w0, qa[2 * e]); qa[2 * e + 1] = fmaf(n0, w1, qa[2 * e + 1]);
        qb[2 * e] = fmaf(n1, w0, qb[2 * e]); qb[2 * e + 1] = fmaf(n1, w1, qb[2 * e + 1]);
      }
    }
    float r[8];
#pragma unroll
    for (int e = 0; e < 8; ++e) r[e] = sw32add(qa[e], qb[e]);
    if (l5 < 24) {
      LAS v4f* dst = (LAS v4f*)(L + L_QP + ((w * 2 + hf) * DH + l5 * 8) * 4);
      dst[0] = v4f{r[0], r[1], r[2], r[3]}; dst[1] = v4f{r[4], r[5], r[6], r[7]};
    }
  }
  if (w == 0) asm volatile("s_waitcnt vmcnt(0) ; HELPER_ONLY" ::: "memory");
  BARRIER();
  SSTAMP(2);

  if (w == 0) {
    asm volatile("; HELPER_ENTRY");
    {
      const LAS float* QP = (const LAS float*)(L + L_QP);
#pragma unroll
      for (int k = 0; k < 3; ++k) {
        float s0 = 0.f, s1 = 0.f;
#pragma unroll
        for (int ww = 0; ww < NW; ++ww) { s0 += QP[(ww * 2) * DH + lane + 64 * k]; s1 += QP[(ww * 2 + 1) * DH + lane + 64 * k]; }
        __builtin_amdgcn_raw_buffer_store_b128(v4u{fbits(s0), tag, fbits(s1), tag}, rs, WS_Q + ((h * 8 + j) * DH + lane + 64 * k) * 16, 0, 16);
      }
      if (lane == 0) CT[1] = 1u;
      SSTAMP(13);
    }
    const int g4 = lane >> 4, tq = (lane & 15) >> 2, tp = lane & 3;
    {
      const unsigned lb = (unsigned)(uintptr_t)L;
      const unsigned char* wo = (const unsigned char*)a.wo + (size_t)h * DH * (DM * 2) + j * 160;
#pragma unroll 1
      for (int k = 0; k < 30; ++k) { const int c = k * 64 + lane, r = c / 10; dma16<true>(wo, r * (DM * 2) + (c - r * 10) * 16, lb + L_WO + k * 1024); }
      if (ffn) {
        const size_t o0 = (size_t)80 * j * (FF * 2) + cg * 128;
        const int vo = (lane >> 3) * (FF * 2) + (lane & 7) * 16;
#pragma unroll 1
        for (int k = 0; k < 10; ++k) {
          dma16<true>((const unsigned char*)a.wd + (size_t)f * 8 * (DM * 2) + k * 1024, lane * 16, lb + L_WD + k * 1024);
          dma16<true>((const unsigned char*)a.wg + o0 + k * 8 * FF * 2, vo, lb + L_G + k * 1024);
          dma16<true>((const unsigned char*)a.wu + o0 + k * 8 * FF * 2, vo, lb + L_G + 10240 + k * 1024);
        }
      }
    }
    BARRIER();
    SSTAMP(3);
    waitvm<0>();
    {
      v4u fr[30];
#pragma unroll
      for (int k = 0; k < 30; ++k) {
        LAS unsigned char* p = L + L_WO + (32 * (k % 6) + 8 * g4 + tq) * 160 + (16 * (k / 6) + 4 * tp) * 2;
        const v2u lo = trread(p), hi = trread(p + 4 * 160);
        fr[k] = v4u{lo.x, lo.y, hi.x, hi.y};
      }
      asm volatile("s_waitcnt lgkmcnt(0)" ::: "memory");
#pragma unroll
      for (int k = 0; k < 30; ++k) *(LAS v4u*)(L + L_WO + k * 1024 + lane * 16) = fr[k];
#pragma unroll
      for (int k = 0; k < 24; ++k) {
        const int row = 32 * (k % 3) + 8 * g4 + tq;
        LAS unsigned char* p = L + L_G + (tp >> 1) * 10240 + (row < 80 ? row : tq) * 128 + (8 * (k / 3) + 4 * (tp & 1)) * 2;
        const v2u lo = trread(p), hi = trread(p + 4 * 128);
        fr[k] = v4u{lo.x, lo.y, hi.x, hi.y};
      }
      asm volatile("s_waitcnt lgkmcnt(0)" ::: "memory");
#pragma unroll
      for (int k = 0; k < 24; ++k) *(LAS v4u*)(L + L_G + k * 1024 + lane * 16) = fr[k];
#pragma unroll
      for (int T = 0; T < 10; ++T) {
        LAS unsigned char* p = L + L_WD + tq * 1280 + (64 * T + 16 * g4 + 4 * tp) * 2;
        const v2u lo = trread(p), hi = trread(p + 4 * 1280);
        fr[T] = v4u{lo.x, lo.y, hi.x, hi.y};
      }
      asm volatile("s_waitcnt lgkmcnt(0)" ::: "memory");
#pragma unroll
      for (int T = 0; T < 10; ++T) *(LAS v4u*)(L + L_WD + T * 1024 + lane * 16) = fr[T];
    }
    int filled = 0, grab = GRAB;
    for (int spin = 0; spin < 16 * SPIN_CAP; ++spin) {
      int need = (int)__builtin_amdgcn_readfirstlane(CT[0]) + LEAD - (STAT + filled);
      if (need > 0) {
        if (filled == 0) {
          unsigned keep;
#pragma unroll 1
          for (unsigned o = 0; o < len; o += 1024)
            asm volatile("s_mov_b32 %0, m0\n\ts_mov_b32 m0, %3\n\ts_nop 2\n\tbuffer_load_dwordx4 %1, %2, %4 offen lds\n\ts_mov_b32 m0, %0"
                         : "=&s"(keep) : "v"(lane * 16), "s"(rc), "s"((unsigned)(uintptr_t)L + L_PF), "s"(o) : "memory");
        }
        need = need > 32 ? 32 : need < grab ? grab : need;
        unsigned old = 0;
        if (lane == 0) old = __hip_atomic_fetch_add(ctr, (unsigned)need, __ATOMIC_RELAXED, __HIP_MEMORY_SCOPE_AGENT);
        old = __builtin_amdgcn_readfirstlane(old);
        const bool done = old + need >= (unsigned)POOL;
        if (lane < (done ? need + NW : need)) DYN[filled + lane] = (lane < need && old + lane < (unsigned)POOL) ? B1OFF + (8 * S1 + old + lane) * ITEMB : END;
        filled += need;
        if (old + GRABEND >= (unsigned)POOL) grab = 1;
        if (done) break;
      } else __builtin_amdgcn_s_sleep(2);
    }
    waitvm<0>();
    __builtin_amdgcn_s_setprio(0);
  } else {
    asm volatile("; STREAM_ENTRY");
    if (w == NW - 1) {
      hint(CT + 1, QDELAY);
      v4u g[24];
      gather<24>(rs, WS_Q + (h * 8 * DH + lane) * 16, tag, g);
      SSTAMP(13);
      LAS unsigned short* QS = (LAS unsigned short*)(L + L_QS);
#pragma unroll
      for (int k = 0; k < 3; ++k) {
        float s0 = 0.f, s1 = 0.f;
#pragma unroll
        for (int p = 0; p < 8; ++p) { s0 += ubits(g[p * 3 + k].x); s1 += ubits(g[p * 3 + k].z); }
        const unsigned u = pk(s0, s1);
        QS[lane + 64 * k] = (unsigned short)u; QS[DH + lane + 64 * k] = (unsigned short)(u >> 16);
      }
    }
#pragma unroll
    for (int e = 1; e < NEXTRA; ++e) {
      if (w != NW - 1) waitvm<PACE>();
      ld3_first(ke[e], voff, rk, so0 + (e + 1) * NS * ITEMB);
      if (w != NW - 1) waitvm<PACE>();
      ld3_first(ve[e], voff, rv, so0 + (e + 1) * NS * ITEMB);
    }
    BARRIER();
    SSTAMP(3);
    v4u q[3];
#pragma unroll
    for (int jj = 0; jj < 3; ++jj) q[jj] = *(const LAS v4u*)(L + L_QS + ((lane & 7) + 8 * jj) * 16);
    Soft st;
    st.m = M_INIT; st.l = 0.f;
#pragma unroll
    for (int k = 0; k < 24; ++k) st.acc[k] = 0.f;
    auto step = [&](unsigned nxt) {
      wait3<3>(km);
      float s = score(km, q);
      asm volatile("" : "+v"(s));
      ld3_again(km, voff, rk, nxt);
      const float p = soft(st, s);
      wait3<3>(vm);
      pv(st, p, vm);
      __builtin_amdgcn_sched_barrier(0);
      ld3_again(vm, voff, rv, nxt);
    };
#pragma unroll
    for (int e = 0; e < NEXTRA; ++e) {
      step(so0 + (NEXTRA + 1 + e) * NS * ITEMB);
      pin3(ke[e]); pin3(ve[e]);
      const float pe = soft(st, score(ke[e], q));
      pv(st, pe, ve[e]);
    }
    unsigned cur = so0;
#pragma unroll 1
    for (int b = 0; b < 2; ++b) {
      while (cur != END && (cur >= B1OFF) == (b == 1)) {
        unsigned t = 0;
        if (lane == 0 && w <= NACT) t = __hip_atomic_fetch_add((LAS unsigned*)(L + L_CT), 1u, __ATOMIC_RELAXED, __HIP_MEMORY_SCOPE_WORKGROUP);
        t = __builtin_amdgcn_readfirstlane(t);
        if (w > NACT) cur = END;
        else if (t < (unsigned)STAT) cur = (t < (unsigned)S0 ? sb0 : sb1) + t * ITEMB;
        else {
          int spin = 0;
          while ((cur = __builtin_amdgcn_readfirstlane(DYN[t - STAT])) == EMPTY) {
            __builtin_amdgcn_s_sleep(1);
            if (++spin > SPIN_CAP) { cur = END; break; }
          }
        }
        step(cur);
      }
      wait3<0>(km); wait3<0>(vm);
      if (GPOS == 0 && b == 1) {
        unsigned t = 0;
        if (lane == 0) t = __hip_atomic_fetch_add((LAS unsigned*)(L + L_CT) + 4, 1u, __ATOMIC_RELAXED, __HIP_MEMORY_SCOPE_WORKGROUP);
        if (__builtin_amdgcn_readfirstlane(t) == NS - 1) __builtin_amdgcn_raw_buffer_store_b32(tag, rs, lane == 0 ? WS_G + wg * 128 : OOB, 0, 16);
      }
      float M = fmaxf(st.m, dpp<ROR8>(st.m)); M = sw32max(sw16max(M));
      const float sc = __builtin_amdgcn_exp2f(st.m - M);
      float l = st.l * sc; l += dpp<ROR8>(l); l = sw16add(l, l); l = sw32add(l, l);
      float t1[12], t2[6];
#pragma unroll
      for (int k = 0; k < 12; ++k) t1[k] = sw16add(st.acc[k] * sc, st.acc[k + 12] * sc);
#pragma unroll
      for (int k = 0; k < 6; ++k) { t2[k] = sw32add(t1[k], t1[k + 6]); t2[k] += dpp<ROR8>(t2[k]); }
      LAS float* rec = (LAS float*)(L + L_REC) + ((w - 1) * 2 + b) * RECF;
      if (!(lane & 8)) {
        const int a0 = 6 * hf + 12 * ((lane >> 4) & 1);
#pragma unroll
        for (int k = 0; k < 6; k += 2) { const int ai = a0 + k; *(LAS v2f*)(rec + 4 + ((lane & 7) + 8 * (ai >> 3)) * 8 + (ai & 7)) = v2f{t2[k], t2[k + 1]}; }
      }
      if (lane == 0) *(LAS v2f*)rec = v2f{M, l};
#pragma unroll
      for (int jj = 0; jj < 3; ++jj) q[jj] = *(const LAS v4u*)(L + L_QS + DH * 2 + ((lane & 7) + 8 * jj) * 16);
      st.m = M_INIT; st.l = 0.f;
#pragma unroll
      for (int k = 0; k < 24; ++k) st.acc[k] = 0.f;
    }
  }
  STAMP(4);
  const int g4 = lane >> 4, c16 = lane & 15;
  const v4f ctag = {0.f, ubits(tag), 0.f, ubits(tag)};
  const v4u zero4 = {0u, 0u, 0u, 0u};
  BARRIER();
  STAMP(5);
  const bool tilew = w == 0 || w >= 4;
  const int tile = w ? w - 3 : 0;
  v4u bw[6]; unsigned xv0 = 0, xv1 = 0;
  if (!tilew) {
    const int dim = tid - 64;
    if (GPOS == 1) __builtin_amdgcn_raw_buffer_store_b32(tag, rs, dim == 0 ? WS_G + wg * 128 : OOB, 0, 16);
    {
      const LAS float* rec = (const LAS float*)(L + L_REC);
      v2f ml = *(const LAS v2f*)(rec + (c16 < 2 * NS ? c16 : 0) * RECF);
      float av[2 * NS];
#pragma unroll
      for (int r = 0; r < 2 * NS; ++r) av[r] = rec[r * RECF + 4 + dim];
      if (c16 >= 2 * NS) ml = v2f{M_INIT, 0.f};
      float M = fmaxf(ml.x, dpp<XOR2>(ml.x)); M = fmaxf(M, dpp<ROR4>(M)); M = fmaxf(M, dpp<ROR8>(M));
      const float e = __builtin_amdgcn_exp2f(ml.x - M);
      float ls = e * ml.y; ls += dpp<XOR2>(ls); ls += dpp<ROR4>(ls); ls += dpp<ROR8>(ls);
      float o0 = 0.f, o1 = 0.f;
#pragma unroll
      for (int r = 0; r < 2 * NS; r += 2) { o0 = fmaf(rdlane(e, r), av[r], o0); o1 = fmaf(rdlane(e, r + 1), av[r + 1], o1); }
      if (GPOS == 2) __builtin_amdgcn_raw_buffer_store_b32(tag, rs, dim == 0 ? WS_G + wg * 128 : OOB, 0, 16);
      put(rs, WS_P + wg * PBLK + dim * 16, fbits(o0), fbits(o1), tag);
      const float lsx = dpp<XOR1>(ls), Mx = dpp<XOR1>(M);
      put(rs, dim < 2 ? WS_P + wg * PBLK + (DH + dim) * 16 : OOB, fbits((lane & 1) ? lsx : M), fbits((lane & 1) ? ls : Mx), tag);
      STAMP(6);
    }
    v4u g[8], gm = zero4;
#pragma unroll
    for (int p = 0; p < 8; ++p) g[p] = zero4;
    const int po = WS_P + h * 8 * PBLK + dim * 16, pm = WS_P + (h * 8 + ((lane >> 1) & 7)) * PBLK + (DH + (lane & 1)) * 16;
    for (unsigned sent = 0, spin = 0; sent != 0xffu && spin < 4 * SPIN_CAP; ++spin) {
      const unsigned want = __builtin_amdgcn_readfirstlane(CT[5]) & ~sent & 0xffu;
#pragma unroll
      for (int p = 0; p < 8; ++p)
        if ((want >> p) & 1u) g[p] = __builtin_amdgcn_raw_buffer_load_b128(rs, po, p * PBLK, 16);
      sent |= want;
    }
    for (int spin = 0; spin < SPIN_CAP; ++spin) {
      gm = __builtin_amdgcn_raw_buffer_load_b128(rs, pm, 0, 16);
      bool ok = true;
#pragma unroll
      for (int p = 0; p < 8; ++p)
        if (!__all(g[p].y == tag && g[p].w == tag)) { g[p] = __builtin_amdgcn_raw_buffer_load_b128(rs, po, p * PBLK, 16); ok = false; }
      if (ok && __all(gm.y == tag && gm.w == tag)) break;
      asm volatile("" ::: "memory");
    }
    STAMP(7);
    float M0 = ubits(gm.x), M1 = ubits(gm.z);
    M0 = fmaxf(M0, dpp<XOR2>(M0)); M1 = fmaxf(M1, dpp<XOR2>(M1)); M0 = fmaxf(M0, dpp<ROR4>(M0)); M1 = fmaxf(M1, dpp<ROR4>(M1)); M0 = fmaxf(M0, dpp<ROR8>(M0)); M1 = fmaxf(M1, dpp<ROR8>(M1));
    const float e0 = __builtin_amdgcn_exp2f(ubits(gm.x) - M0), e1 = __builtin_amdgcn_exp2f(ubits(gm.z) - M1);
    float l0 = e0 * dpp<XOR1>(ubits(gm.x)), l1 = e1 * dpp<XOR1>(ubits(gm.z));
    l0 += dpp<XOR2>(l0); l1 += dpp<XOR2>(l1); l0 += dpp<ROR4>(l0); l1 += dpp<ROR4>(l1); l0 += dpp<ROR8>(l0); l1 += dpp<ROR8>(l1);
    float o0 = 0.f, o1 = 0.f;
#pragma unroll
    for (int p = 0; p < 8; ++p) { o0 = fmaf(rdlane(e0, 2 * p), ubits(g[p].x), o0); o1 = fmaf(rdlane(e1, 2 * p), ubits(g[p].z), o1); }
    const unsigned ob = pk(o0 * __builtin_amdgcn_rcpf(rdlane(l0, 0)), o1 * __builtin_amdgcn_rcpf(rdlane(l1, 0)));
    ((LAS unsigned short*)(L + L_O))[dim] = (unsigned short)ob; ((LAS unsigned short*)(L + L_O))[DH + dim] = (unsigned short)(ob >> 16);
  } else {
#pragma unroll
    for (int k = 0; k < 6; ++k) bw[k] = *(const LAS v4u*)(L + L_WO + (tile * 6 + k) * 1024 + lane * 16);
    xv0 = a.x[80 * j + 16 * tile + c16]; xv1 = a.x[DM + 80 * j + 16 * tile + c16];
    asm volatile("" : "+v"(xv0), "+v"(xv1));
    if (w == NW - 1) { watch<8>(rs, WS_G + h * 8 * 128, 128, lane, tag, CT + 5); STAMP(13); }
  }
  BARRIER();
  STAMP(1);
  if (tilew) {
    const int ybase = WS_Y + (j * 5 + tile) * (NH * 256);
    if (DPOS == 0) __builtin_amdgcn_raw_buffer_store_b32(tag, rs, tid == 0 ? WS_D + wg * 128 : OOB, 0, 16);
    v4f d = ctag;
#pragma unroll
    for (int k = 0; k < 6; ++k) {
      const v4u ao = *(const LAS v4u*)(L + L_O + (c16 & 2) * DH + (32 * k + 8 * g4) * 2);
      d = mfma((c16 & 1) ? zero4 : ao, bw[k], d);
    }
    if (DPOS == 1) { asm volatile("" : "+v"(d)); __builtin_amdgcn_raw_buffer_store_b32(tag, rs, tid == 0 ? WS_D + wg * 128 : OOB, 0, 16); }
    __builtin_amdgcn_raw_buffer_store_b128(__builtin_bit_cast(v4u, d), rs, lane < 16 ? ybase + h * 256 + c16 * 16 : OOB, 0, 16);
    STAMP(8);
    if (ffn) {
      v4u g[8];
#pragma unroll
      for (int k = 0; k < 8; ++k) g[k] = zero4;
      for (unsigned sent = 0, spin = 0; sent != 0x11111111u && spin < 4 * SPIN_CAP; ++spin) {
        unsigned m = __builtin_amdgcn_readfirstlane(CT[6]);
        m &= m >> 1; m &= m >> 2; m &= 0x11111111u & ~sent;
#pragma unroll
        for (int k = 0; k < 8; ++k)
          if ((m >> (4 * k)) & 1u) g[k] = __builtin_amdgcn_raw_buffer_load_b128(rs, ybase + lane * 16 + (k & 3) * 1024, (k >> 2) * 4096, 16);
        sent |= m;
      }
      for (int spin = 0; spin < SPIN_CAP; ++spin) {
        bool ok = true;
#pragma unroll
        for (int k = 0; k < 8; ++k)
          if (!__all(g[k].y == tag && g[k].w == tag)) { g[k] = __builtin_amdgcn_raw_buffer_load_b128(rs, ybase + lane * 16 + (k & 3) * 1024, (k >> 2) * 4096, 16); ok = false; }
        if (ok) break;
        asm volatile("" ::: "memory");
      }
      STAMP(2);
      const float s0 = ((ubits(g[0].x) + ubits(g[1].x)) + (ubits(g[2].x) + ubits(g[3].x))) + ((ubits(g[4].x) + ubits(g[5].x)) + (ubits(g[6].x) + ubits(g[7].x)));
      const float s1 = ((ubits(g[0].z) + ubits(g[1].z)) + (ubits(g[2].z) + ubits(g[3].z))) + ((ubits(g[4].z) + ubits(g[5].z)) + (ubits(g[6].z) + ubits(g[7].z)));
      float s = sw16add(s0, s1); s = sw32add(s, s);
      const float hv = bflo((g4 & 1) ? xv1 : xv0) + bfr(s);
      if (lane < 32) ((LAS unsigned short*)(L + L_HS))[g4 * 96 + 16 * tile + c16] = (unsigned short)pk(hv, 0.f);
    }
  } else if (ffn && w == 1) { watch<32>(rs, WS_D + j * 128, 8 * 128, lane, tag, CT + 6); STAMP(14); }
  if (ffn) {
    v4u bg[3], bdn[3]; int zo[3];
#pragma unroll
    for (int k = 0; k < 3; ++k) bg[k] = *(const LAS v4u*)(L + L_G + (w * 3 + k) * 1024 + lane * 16);
    const int u = (w - 1) & 3;
#pragma unroll
    for (int T = 0; T < 3; ++T) {
      const int dt = u + 4 * T, n = 64 * dt + lane;
      bdn[T] = *(const LAS v4u*)(L + L_WD + (dt < 10 ? dt : 0) * 1024 + lane * 16);
      zo[T] = dt < 10 ? WS_Z + ((n >> 2) * NFFN + f) * 64 + (n & 3) * 16 : OOB;
      asm volatile("" : "+v"(zo[T]));
    }
    int eo = WS_E + (cg * 8 + w) * 5120;
    const int eo0 = lane < 32 ? eo + j * 512 + lane * 16 : OOB, eo1 = lane < 8 ? eo + 4096 + j * 128 + lane * 16 : OOB;
    BARRIER();
    STAMP(3);
    {
      v4f d = ctag, ds = ctag;
#pragma unroll
      for (int k = 0; k < 3; ++k) {
        v4u ah = *(const LAS v4u*)(L + L_HS + (c16 < 5 ? c16 >> 1 : 0) * 192 + (32 * k + 8 * g4) * 2);
        if ((c16 & 1) || c16 > 4) ah = zero4;
        d = mfma(ah, bg[k], d); ds = mfma(ah, ah, ds);
      }
      __builtin_amdgcn_raw_buffer_store_b128(__builtin_bit_cast(v4u, d), rs, eo0, 0, 16);
      __builtin_amdgcn_raw_buffer_store_b128(__builtin_bit_cast(v4u, ds), rs, eo1, 0, 16);
    }
    STAMP(9);
    if (w >= 1 && w <= 4) {
      v4u g[5];
      __builtin_amdgcn_s_sleep(EDELAY);
      gather<5>(rs, WS_E + f * 5120 + lane * 16, tag, g);
      if (FPOS == 0) __builtin_amdgcn_raw_buffer_store_b32(tag, rs, (tid == 64 && j == 0) ? WS_F + cg * 128 : OOB, 0, 16);
      STAMP(10);
      const float s0 = (ubits(g[0].x) + ubits(g[1].x)) + (ubits(g[2].x) + ubits(g[3].x)), s1 = (ubits(g[0].z) + ubits(g[1].z)) + (ubits(g[2].z) + ubits(g[3].z));
      const float gs = sw32add(s0, s1);
      const float cs0 = sw32add(s0, s0);
      const auto cx = __builtin_amdgcn_permlane16_swap(fbits(cs0), fbits(cs0), false, false);
      const auto gx = __builtin_amdgcn_permlane16_swap(fbits(gs), fbits(gs), false, false);
      float t0 = ubits(g[4].x), t1 = ubits(g[4].z);
      t0 += dpp<ROR8>(t0); t1 += dpp<ROR8>(t1);
      t0 = sw16add(t0, t0); t1 = sw16add(t1, t1);
      const float tt = sw32add(t0, t1) * (1.0f / DM);
      const float mean = dpp<BC + 4>(tt), q0 = dpp<BC + 0>(tt), q1 = dpp<BC + 2>(tt);
      const float rdv = ln_rden(fmaxf((hf ? q1 : q0) - mean * mean, 0.f));
      const float gv = bfr((ubits(gx[0]) - bfr(mean) * ubits(cx[1])) * rdv), uv = dpp<ROR8>(gv);
      const float y2 = (-2.0f * 0.7978845608028654f * 1.4426950408889634f) * (gv + 0.044715f * gv * gv * gv);
      const float act = bfr(gv * __builtin_amdgcn_rcpf(1.0f + __builtin_amdgcn_exp2f(y2))) * uv;
      const unsigned ap = pk(act, dpp<XOR1>(act));
      const auto ax = __builtin_amdgcn_permlane32_swap(ap, ap, false, false);
      const unsigned keep = ((c16 >> 2) == g4 && !(c16 & 1)) ? 0xffffffffu : 0u, k1 = (c16 & 2) ? keep : 0u, k0 = keep ^ k1;
      const v4u b0 = {dppu<BC + 0>(ax[0]), dppu<BC + 2>(ax[0]), dppu<BC + 4>(ax[0]), dppu<BC + 6>(ax[0])};
      const v4u b1 = {dppu<BC + 0>(ax[1]), dppu<BC + 2>(ax[1]), dppu<BC + 4>(ax[1]), dppu<BC + 6>(ax[1])};
      v4u a2 = (b0 & k0) | (b1 & k1);
      if (FPOS == 1) { asm volatile("" : "+v"(a2)); __builtin_amdgcn_raw_buffer_store_b32(tag, rs, (tid == 64 && j == 0) ? WS_F + cg * 128 : OOB, 0, 16); }
#pragma unroll
      for (int T = 0; T < 3; ++T) __builtin_amdgcn_raw_buffer_store_b128(__builtin_bit_cast(v4u, mfma(a2, bdn[T], ctag)), rs, zo[T], 0, 16);
      STAMP(11);
    } else if (w == NW - 1 && cg < 20) {
      const __amdgpu_buffer_rsrc_t ro = __builtin_amdgcn_make_buffer_rsrc((void*)a.out, 0, 2 * DM * 2, 0x00020000);
      const float hv = bflo(((const LAS unsigned short*)(L + L_HS))[(g4 & 1) * 96 + 4 * cg + (lane & 3)]);
      v4u g[12];
      watch<24>(rs, WS_F, 128, lane, tag, CT + 7);
      STAMP(15);
      __builtin_amdgcn_s_sleep(ZDELAY);
      gather<12>(rs, WS_Z + (20 * j + cg) * (NFFN * 64) + lane * 16, tag, g);
      float s0[4], s1[4];
#pragma unroll
      for (int k = 0; k < 4; ++k) { s0[k] = (ubits(g[k].x) + ubits(g[k + 4].x)) + ubits(g[k + 8].x); s1[k] = (ubits(g[k].z) + ubits(g[k + 4].z)) + ubits(g[k + 8].z); }
      float s = sw16add((s0[0] + s0[1]) + (s0[2] + s0[3]), (s1[0] + s1[1]) + (s1[2] + s1[3]));
      s = sw32add(s, s); s += dpp<ROR4>(s); s += dpp<ROR8>(s);
      const unsigned o = pk(hv + bfr(s), 0.f);
      __builtin_amdgcn_raw_buffer_store_b16((unsigned short)o, ro, (lane < 32 && c16 < 4) ? (g4 * DM + 80 * j + 4 * cg + c16) * 2 : OOB, 0, 0);
      __builtin_amdgcn_raw_buffer_store_b32(tag, rs, (wg == 0 && lane == 0) ? 0 : OOB, 0, 0);
      STAMP(12);
    }
  }
  STAMP_DUMP();
  asm volatile(".Ldb_end:");
}
}

extern "C" void kernel_launch(void* const* d_in, const int* in_sizes, int n_in, void* d_out, int out_size, void* d_ws, size_t ws_size, hipStream_t stream) {
  Args a;
  a.x = (const unsigned short*)d_in[0]; a.k = (const unsigned short*)d_in[1]; a.v = (const unsigned short*)d_in[2];
  a.wq = (const unsigned short*)d_in[3]; a.wo = (const unsigned short*)d_in[4];
  a.wg = (const unsigned short*)d_in[6]; a.wu = (const unsigned short*)d_in[7]; a.wd = (const unsigned short*)d_in[8];
  a.out = (unsigned short*)d_out; a.ws = (unsigned char*)d_ws;
  hipLaunchKernelGGL(decode_block, dim3(NWG), dim3(NTHR), 0, stream, a);
}
```

```cpp
#include <hip/hip_runtime.h>
#include <hip/hip_bf16.h>
#include <stdint.h>

namespace {
typedef unsigned v4u __attribute__((ext_vector_type(4)));
typedef unsigned v2u __attribute__((ext_vector_type(2)));
typedef float v4f __attribute__((ext_vector_type(4)));
typedef float v2f __attribute__((ext_vector_type(2)));
typedef short v4s __attribute__((ext_vector_type(4)));
typedef __bf16 bf2 __attribute__((ext_vector_type(2)));
typedef __bf16 bf8 __attribute__((ext_vector_type(8)));
#define LAS __attribute__((address_space(3)))
#define CAS __attribute__((address_space(4)))

constexpr int DM = 640, NH = 32, SK = 16384, DH = 192, FF = 1536;
constexpr int NWG = 256, NW = 8, NS = NW - 1, NTHR = NW * 64;
constexpr unsigned ITEMB = 8 * DH * 2;
constexpr unsigned HEADB = (unsigned)SK * DH * 2, B1OFF = NH * HEADB;
constexpr int IPH = SK / 8;
constexpr int S0 = IPH / 8, S1 = 192, STAT = S0 + S1;
constexpr int POOL = IPH - 8 * S1, LEAD = 8;
constexpr int NEXTRA = 5, NACT = 5;
constexpr unsigned END = 0xF0000000u, EMPTY = 0xFFFFFFFFu;
constexpr int NRED = DM / 4, NFFN = FF / 8;
constexpr int SPIN_CAP = 1 << 15;
constexpr float M_INIT = -3.0e38f;

constexpr int WS_CTR = 4096, CTR_STRIDE = 4352;
constexpr int WS_F = 0x24000, WS_D = 0x28000, WS_G = 0x30000, WS_Q = 0x40000, WS_P = 0x100000, PBLK = 3200, WS_Y = 0x200000, WS_E = 0x280000, WS_Z = 0x400000, WS_BYTES = 0x600000;
constexpr int OOB = (int)0x80000000u;
static_assert(WS_CTR + NH * CTR_STRIDE <= WS_F && WS_F + 24 * 128 <= WS_D && WS_D + NWG * 128 <= WS_G && WS_G + NWG * 128 <= WS_Q && WS_Q + NWG * DH * 16 <= WS_P && WS_P + NWG * PBLK <= WS_Y, "ws map");
static_assert(WS_Y + NWG * 80 * 16 <= WS_E && WS_E + NFFN * 5120 <= WS_Z && WS_Z + NRED * NFFN * 64 <= WS_BYTES, "ws map");

constexpr int L_WO = 0, L_G = L_WO + DH * 160, L_WD = L_G + 2 * 80 * 128, L_QP = L_WD + 8 * 1280;
constexpr int L_QS = L_QP + NW * 2 * DH * 4, RECF = DH + 4, L_REC = L_QS + 2 * DH * 2, L_O = L_REC + NS * 2 * RECF * 4, L_HS = L_O + 2 * DH * 2;
constexpr int L_CT = L_HS + 3 * 192, DYNMAX = POOL + 96, L_DYN = L_CT + 64, L_BYTES = L_DYN + DYNMAX * 4;
static_assert(L_BYTES > 81920 && L_BYTES <= 160 * 1024 && L_REC % 16 == 0 && L_O % 16 == 0 && L_HS % 16 == 0, "one workgroup per CU");

struct Args {
  const unsigned short *x, *k, *v, *wq, *wo, *wg, *wu, *wd;
  unsigned short* out;
  unsigned char* ws;
};

__device__ __forceinline__ float dot2(unsigned a, unsigned b, float c) {
  return __builtin_amdgcn_fdot2_f32_bf16(__builtin_bit_cast(bf2, a), __builtin_bit_cast(bf2, b), c, false);
}
template <int CTRL> __device__ __forceinline__ float dpp(float x) {
  return __builtin_bit_cast(float, __builtin_amdgcn_mov_dpp(__builtin_bit_cast(int, x), CTRL, 0xf, 0xf, true));
}
template <int CTRL> __device__ __forceinline__ unsigned dppu(unsigned x) { return (unsigned)__builtin_amdgcn_mov_dpp((int)x, CTRL, 0xf, 0xf, true); }
constexpr int BC = 0x150;
constexpr int XOR1 = 0xB1, XOR2 = 0x4E, HMIR = 0x141, ROR4 = 0x124, ROR8 = 0x128;
__device__ __forceinline__ float bflo(unsigned x) { return __builtin_bit_cast(float, x << 16); }
__device__ __forceinline__ float bfhi(unsigned x) { return __builtin_bit_cast(float, x & 0xffff0000u); }
__device__ __forceinline__ unsigned pk(float lo, float hi) {
  const bf2 r = {(__bf16)lo, (__bf16)hi};
  return __builtin_bit_cast(unsigned, r);
}
__device__ __forceinline__ float bfr(float x) { return (float)(__bf16)x; }
__device__ __forceinline__ unsigned fbits(float x) { return __builtin_bit_cast(unsigned, x); }
__device__ __forceinline__ float ubits(unsigned x) { return __builtin_bit_cast(float, x); }
__device__ __forceinline__ float sw16add(float a, float b) {
  const auto s = __builtin_amdgcn_permlane16_swap(fbits(a), fbits(b), false, false);
  return ubits(s[0]) + ubits(s[1]);
}
__device__ __forceinline__ float sw32add(float a, float b) {
  const auto s = __builtin_amdgcn_permlane32_swap(fbits(a), fbits(b), false, false);
  return ubits(s[0]) + ubits(s[1]);
}
__device__ __forceinline__ float sw16max(float a) {
  const auto s = __builtin_amdgcn_permlane16_swap(fbits(a), fbits(a), false, false);
  return fmaxf(ubits(s[0]), ubits(s[1]));
}
__device__ __forceinline__ float sw32max(float a) {
  const auto s = __builtin_amdgcn_permlane32_swap(fbits(a), fbits(a), false, false);
  return fmaxf(ubits(s[0]), ubits(s[1]));
}
__device__ __forceinline__ float rowsum16(float s) { s += dpp<XOR1>(s); s += dpp<XOR2>(s); s += dpp<HMIR>(s); s += dpp<ROR8>(s); return s; }
__device__ __forceinline__ float rdlane(float x, int l) { return ubits(__builtin_amdgcn_readlane(fbits(x), l)); }
__device__ __forceinline__ v4u mkrsrc(const void* p, unsigned bytes) {
  const unsigned long long a = (unsigned long long)p;
  return v4u{(unsigned)a, (unsigned)(a >> 32) & 0xffffu, bytes, 0x00020000u};
}
__device__ __forceinline__ v4f mfma(v4u a, v4u b, v4f c) {
  return __builtin_amdgcn_mfma_f32_16x16x32_bf16(__builtin_bit_cast(bf8, a), __builtin_bit_cast(bf8, b), c, 0, 0, 0);
}
template <int STRIDE> __device__ __forceinline__ v4u bfrag(LAS unsigned char* p) {
  const v2u lo = __builtin_bit_cast(v2u, __builtin_amdgcn_ds_read_tr16_b64_v4i16((LAS v4s*)p));
  const v2u hi = __builtin_bit_cast(v2u, __builtin_amdgcn_ds_read_tr16_b64_v4i16((LAS v4s*)(p + 4 * STRIDE)));
  return v4u{lo.x, lo.y, hi.x, hi.y};
}
#define BARRIER() asm volatile("s_waitcnt lgkmcnt(0)\n\ts_barrier" ::: "memory")

#define LD3 "s_nop 4\n\tbuffer_load_dwordx4 %0, %3, %4, %5 offen nt\n\tbuffer_load_dwordx4 %1, %3, %4, %5 offen offset:128 nt\n\tbuffer_load_dwordx4 %2, %3, %4, %5 offen offset:256 nt"
__device__ __forceinline__ void ld_first(v4u& d, int voff, v4u rs, unsigned soff) {
  asm volatile("s_nop 4\n\tbuffer_load_dwordx4 %0, %1, %2, %3 offen nt" : "=v"(d) : "v"(voff), "s"(rs), "s"(soff));
}
__device__ __forceinline__ void ld3_first(v4u (&r)[3], int voff, v4u rs, unsigned so) { asm volatile(LD3 : "=&v"(r[0]), "=&v"(r[1]), "=&v"(r[2]) : "v"(voff), "s"(rs), "s"(so)); }
__device__ __forceinline__ void ld3_again(v4u (&r)[3], int voff, v4u rs, unsigned so) { asm volatile(LD3 : "+v"(r[0]), "+v"(r[1]), "+v"(r[2]) : "v"(voff), "s"(rs), "s"(so)); }
template <int N> __device__ __forceinline__ void wait3(v4u (&r)[3]) { asm volatile("s_waitcnt vmcnt(%3)" : "+v"(r[0]), "+v"(r[1]), "+v"(r[2]) : "i"(N)); }
__device__ __forceinline__ void pin3(v4u (&r)[3]) { asm volatile("" : "+v"(r[0]), "+v"(r[1]), "+v"(r[2])); }
template <int N> __device__ __forceinline__ void waitvm() { asm volatile("s_waitcnt vmcnt(%0)" : : "i"(N) : "memory"); }

__device__ __forceinline__ void dma16(const void* base, int voff, unsigned lds) {
  unsigned keep;
  asm volatile("s_mov_b32 %0, m0\n\ts_mov_b32 m0, %3\n\ts_nop 2\n\tglobal_load_lds_dwordx4 %1, %2 nt\n\ts_mov_b32 m0, %0" : "=&s"(keep) : "v"(voff), "s"(base), "s"(lds) : "memory");
}

__device__ __forceinline__ void put(__amdgpu_buffer_rsrc_t rs, int off, unsigned a, unsigned b, unsigned tag) {
  __builtin_amdgcn_raw_buffer_store_b128(v4u{a, tag, b, tag}, rs, off, 0, 16);
}
__shared__ unsigned g_tmo;
#define TMO(c) do { g_tmo = (c); } while (0)
#define STAMP(n)
#define SSTAMP(n)
#define STAMP_INIT()
#define STAMP_DUMP()
template <int N> __device__ __forceinline__ void gather(__amdgpu_buffer_rsrc_t rs, int off, unsigned tag, v4u (&t)[N]) {
  bool done = false;
#pragma unroll
  for (int k = 0; k < N; ++k) t[k] = v4u{0u, 0u, 0u, 0u};
  for (int spin = 0; spin < SPIN_CAP; ++spin) {
    if (!done) {
#pragma unroll
      for (int k = 0; k < N; ++k) t[k] = __builtin_amdgcn_raw_buffer_load_b128(rs, off + (k & 3) * 1024, (k >> 2) * 4096, 16);
      unsigned mn = tag, mx = tag;
#pragma unroll
      for (int k = 0; k < N; ++k) { mn = min(mn, min(t[k].y, t[k].w)); mx = max(mx, max(t[k].y, t[k].w)); }
      done = mn == mx;
    }
    if (__all(done)) break;
    if (spin == SPIN_CAP - 1) TMO(10 + N);
    asm volatile("" ::: "memory");
    __builtin_amdgcn_s_sleep(1);
  }
}

template <int N> __device__ __forceinline__ void watch(__amdgpu_buffer_rsrc_t rs, int off, int stride, int lane, unsigned tag, volatile LAS unsigned* mask) {
  bool seen = lane >= N;
  for (int spin = 0; spin < SPIN_CAP; ++spin) {
    if (!seen) seen = __builtin_amdgcn_raw_buffer_load_b32(rs, off + lane * stride, 0, 16) == tag;
    const unsigned long long m = __ballot(seen);
    if (lane == 0) *mask = (unsigned)m;
    if (m == ~0ull) break;
    if (spin == SPIN_CAP - 1) TMO(100 + N);
    asm volatile("" ::: "memory");
    __builtin_amdgcn_s_sleep(1);
  }
  if (lane == 0) *mask = ~0u;
}
#define hint(p, D) do { for (int sp_ = 0; sp_ < SPIN_CAP && *(p) == 0u; ++sp_) __builtin_amdgcn_s_sleep(1); __builtin_amdgcn_s_sleep(D); } while (0)

__device__ __forceinline__ float ln_rden(float var) { return __builtin_amdgcn_rcpf(bfr(__builtin_amdgcn_sqrtf(bfr(bfr(var) + 1e-5f)))); }
__device__ __forceinline__ float ln_val(float x, float mean_b, float rden) { return bfr(bfr(x - mean_b) * rden); }

struct Soft { float m, l, acc[24]; };
__device__ __forceinline__ float score(const v4u (&kk)[3], const v4u (&q)[3]) {
  float s0 = 0.f, s1 = 0.f;
#pragma unroll
  for (int j = 0; j < 3; ++j) {
    s0 = dot2(kk[j].x, q[j].x, s0); s1 = dot2(kk[j].y, q[j].y, s1);
    s0 = dot2(kk[j].z, q[j].z, s0); s1 = dot2(kk[j].w, q[j].w, s1);
  }
  return s0 + s1;
}
__device__ __forceinline__ float soft(Soft& st, float s) {
  s += dpp<XOR1>(s); s += dpp<XOR2>(s); s += dpp<HMIR>(s);
  s = bfr(bfr(s) * 0.07216878364870322f) * 1.4426950408889634f;
  if (__any(s > st.m)) {
    const float mn = fmaxf(st.m, s), al = __builtin_amdgcn_exp2f(st.m - mn);
    st.l *= al;
#pragma unroll
    for (int k = 0; k < 24; ++k) st.acc[k] *= al;
    st.m = mn;
  }
  const float p = __builtin_amdgcn_exp2f(s - st.m);
  st.l += p;
  return p;
}
__device__ __forceinline__ void pv(Soft& st, float p, const v4u (&vv)[3]) {
#pragma unroll
  for (int j = 0; j < 3; ++j)
#pragma unroll
    for (int e = 0; e < 4; ++e) {
      st.acc[j * 8 + 2 * e] = fmaf(p, bflo(vv[j][e]), st.acc[j * 8 + 2 * e]);
      st.acc[j * 8 + 2 * e + 1] = fmaf(p, bfhi(vv[j][e]), st.acc[j * 8 + 2 * e + 1]);
    }
}

__global__ void __launch_bounds__(NTHR, 2) decode_block(Args a) {
  __shared__ __attribute__((aligned(16))) unsigned char smem[L_BYTES];
  LAS unsigned char* L = (LAS unsigned char*)smem;
  volatile LAS unsigned* CT = (volatile LAS unsigned*)(L + L_CT);
  volatile LAS unsigned* DYN = (volatile LAS unsigned*)(L + L_DYN);
  const int tid = threadIdx.x, lane = tid & 63, w = __builtin_amdgcn_readfirstlane(tid >> 6);
  const int wg = blockIdx.x, h = wg >> 3, j = wg & 7;
  const int hf = lane >> 5, l5 = lane & 31;
  const bool ffn = ((h + j) & 3) != 3;
  const int cg = 3 * (h >> 2) + (h & 3) - ((h & 3) > ((3 - j) & 3) ? 1 : 0), f = 8 * cg + j;
  const __amdgpu_buffer_rsrc_t rs = __builtin_amdgcn_make_buffer_rsrc((void*)a.ws, 0, WS_BYTES, 0x00020000);
  unsigned* ctr = (unsigned*)(a.ws + WS_CTR + h * CTR_STRIDE);
  if (tid == 0) g_tmo = 0;
  const unsigned long long dg_t0 = __builtin_amdgcn_s_memrealtime();
  STAMP_INIT();
  STAMP(0);

  auto load_weights = [&]() {
    const unsigned lb = (unsigned)(uintptr_t)L;
    const unsigned char* wo = (const unsigned char*)a.wo + (size_t)h * DH * (DM * 2) + j * 160;
    int vo = (lane / 10) * (DM * 2) + (lane % 10) * 16, c10 = lane % 10;
#pragma unroll 2
    for (int k = 0; k < 30; ++k) {
      dma16(wo, vo, lb + L_WO + k * 1024);
      c10 += 4; vo += 6 * (DM * 2) + 64;
      if (c10 >= 10) { c10 -= 10; vo += DM * 2 - 160; }
    }
    if (ffn) {
      const size_t o0 = (size_t)80 * j * (FF * 2) + cg * 128;
      const int vg = (lane >> 3) * (FF * 2) + (lane & 7) * 16;
#pragma unroll 1
      for (int k = 0; k < 10; ++k) {
        dma16((const unsigned char*)a.wg + o0 + k * 8 * FF * 2, vg, lb + L_G + k * 1024);
        dma16((const unsigned char*)a.wu + o0 + k * 8 * FF * 2, vg, lb + L_G + 10240 + k * 1024);
        dma16((const unsigned char*)a.wd + (size_t)f * 8 * (DM * 2) + k * 1024, lane * 16, lb + L_WD + k * 1024);
      }
    }
  };
  unsigned epoch;
  {
    const int zero = 0;
    if (w == 0) {
      __builtin_amdgcn_s_setprio(3);
      if (j == 0) asm volatile("s_nop 4\n\tglobal_store_dword %0, %0, %1 sc1" : : "v"(zero), "s"(ctr) : "memory");
    }
    asm volatile("s_nop 4\n\tglobal_load_dword %0, %1, %2 sc1" : "=v"(epoch) : "v"(zero), "s"(a.ws));
  }
  v4u wqr[5], xa, xb; v2u xc;
  {
    const size_t rowoff = (size_t)(80 * j + 10 * w) * (NH * DH * 2) + h * (DH * 2);
    const v4u rw = mkrsrc((const unsigned char*)a.wq + rowoff, (unsigned)((size_t)DM * NH * DH * 2 - rowoff));
    const int vo = l5 < 24 ? hf * (NH * DH * 2) + l5 * 16 : OOB;
#pragma unroll
    for (int i = 0; i < 5; ++i) ld_first(wqr[i], vo, rw, i * 2 * NH * DH * 2);
    const v4u rx = mkrsrc(a.x, 2 * DM * 2);
    const int xo = hf * (DM * 2) + l5 * 40;
    asm volatile("s_nop 4\n\tbuffer_load_dwordx4 %0, %3, %4, 0 offen\n\tbuffer_load_dwordx4 %1, %3, %4, 0 offen offset:16\n\tbuffer_load_dwordx2 %2, %3, %4, 0 offen offset:32"
                 : "=&v"(xa), "=&v"(xb), "=&v"(xc) : "v"(xo), "s"(rx));
  }
  const v4u rk = mkrsrc((const unsigned char*)a.k + (size_t)h * HEADB, B1OFF + HEADB), rv = mkrsrc((const unsigned char*)a.v + (size_t)h * HEADB, B1OFF + HEADB);
  const int voff = (lane >> 3) * (DH * 2) + (lane & 7) * 16;
  const unsigned sb0 = (unsigned)(S0 * j) * ITEMB, sb1 = B1OFF + (unsigned)(S1 * j - S0) * ITEMB;
  v4u km[3], vm[3], ke[NEXTRA][3], ve[NEXTRA][3];
  const unsigned so0 = w ? sb0 + (unsigned)(w - 1) * ITEMB : END;
  asm volatile("s_barrier" ::: "memory");
  ld3_first(km, voff, rk, so0); ld3_first(vm, voff, rv, so0);
  if (w == 0) {
    for (int k = lane; k < DYNMAX; k += 64) DYN[k] = EMPTY;
    if (lane < 48) {
      ((LAS unsigned*)(L + L_HS))[lane] = 0u; ((LAS unsigned*)(L + L_HS))[48 + lane] = 0u; ((LAS unsigned*)(L + L_HS))[96 + lane] = lane < 40 ? 0x3f803f80u : 0u;
    }
    if (lane < 8) CT[lane] = lane ? 0u : (2 * NEXTRA + 1) * NS;
  }
  asm volatile("s_waitcnt vmcnt(6)" : "+v"(wqr[0]), "+v"(wqr[1]), "+v"(wqr[2]), "+v"(wqr[3]), "+v"(wqr[4]), "+v"(xa), "+v"(xb), "+v"(xc), "+v"(epoch));
  ld3_first(ke[0], voff, rk, so0 + NS * ITEMB); ld3_first(ve[0], voff, rv, so0 + NS * ITEMB);
  const unsigned tag = ((__builtin_amdgcn_readfirstlane(epoch) + 0x1E3779B9u) & 0x1FFFFFFFu) | 0x20000000u;
  SSTAMP(1);
  {
    const unsigned xw[10] = {xa.x, xa.y, xa.z, xa.w, xb.x, xb.y, xb.z, xb.w, xc.x, xc.y};
    float s = 0.f, sq = 0.f;
#pragma unroll
    for (int k = 0; k < 10; ++k) { s = dot2(xw[k], 0x3f803f80u, s); sq = dot2(xw[k], xw[k], sq); }
    s = rowsum16(sw16add(s, sq)) * (1.0f / DM);
    const auto sx = __builtin_amdgcn_permlane16_swap(fbits(s), fbits(s), false, false);
    const float mean = ubits(sx[0]), mb = bfr(mean), rd = ln_rden(fmaxf(ubits(sx[1]) - mean * mean, 0.f));
    const float mb0 = rdlane(mb, 0), mb1 = rdlane(mb, 32), rd0 = rdlane(rd, 0), rd1 = rdlane(rd, 32);
    const CAS unsigned* x32 = (const CAS unsigned*)a.x + 40 * j + 5 * w;
    float qa[8], qb[8];
#pragma unroll
    for (int e = 0; e < 8; ++e) { qa[e] = 0.f; qb[e] = 0.f; }
#pragma unroll
    for (int i = 0; i < 5; ++i) {
      const unsigned u0 = x32[i], u1 = x32[DM / 2 + i];
      const float n0 = ln_val(hf ? bfhi(u0) : bflo(u0), mb0, rd0), n1 = ln_val(hf ? bfhi(u1) : bflo(u1), mb1, rd1);
#pragma unroll
      for (int e = 0; e < 4; ++e) {
        const float w0 = bflo(wqr[i][e]), w1 = bfhi(wqr[i][e]);
        qa[2 * e] = fmaf(n0, w0, qa[2 * e]); qa[2 * e + 1] = fmaf(n0, w1, qa[2 * e + 1]);
        qb[2 * e] = fmaf(n1, w0, qb[2 * e]); qb[2 * e + 1] = fmaf(n1, w1, qb[2 * e + 1]);
      }
    }
    float r[8];
#pragma unroll
    for (int e = 0; e < 8; ++e) r[e] = sw32add(qa[e], qb[e]);
    if (l5 < 24) {
      LAS v4f* dst = (LAS v4f*)(L + L_QP + ((w * 2 + hf) * DH + l5 * 8) * 4);
      dst[0] = v4f{r[0], r[1], r[2], r[3]}; dst[1] = v4f{r[4], r[5], r[6], r[7]};
    }
  }
  if (w == 0) asm volatile("s_waitcnt vmcnt(0) ; HELPER_ONLY" ::: "memory");
  BARRIER();
  SSTAMP(2);

  if (w == 0) {
    asm volatile("; HELPER_ENTRY");
    {
      const LAS float* QP = (const LAS float*)(L + L_QP);
#pragma unroll
      for (int k = 0; k < 3; ++k) {
        float s0 = 0.f, s1 = 0.f;
#pragma unroll
        for (int ww = 0; ww < NW; ++ww) { s0 += QP[(ww * 2) * DH + lane + 64 * k]; s1 += QP[(ww * 2 + 1) * DH + lane + 64 * k]; }
        __builtin_amdgcn_raw_buffer_store_b128(v4u{fbits(s0), tag, fbits(s1), tag}, rs, WS_Q + ((h * 8 + j) * DH + lane + 64 * k) * 16, 0, 16);
      }
      if (lane == 0) CT[1] = 1u;
      SSTAMP(13);
    }
    BARRIER();
    SSTAMP(3);
    int filled = 0;
    for (int spin = 0; spin < 16 * SPIN_CAP; ++spin) {
      int need = (int)__builtin_amdgcn_readfirstlane(CT[0]) + LEAD - (STAT + filled);
      if (need > 0) {
        need = need > 32 ? 32 : need;
        unsigned old = 0;
        if (lane == 0) old = __hip_atomic_fetch_add(ctr, (unsigned)need, __ATOMIC_RELAXED, __HIP_MEMORY_SCOPE_AGENT);
        old = __builtin_amdgcn_readfirstlane(old);
        const bool done = old + need >= (unsigned)POOL;
        if (lane < (done ? need + NW : need)) DYN[filled + lane] = (lane < need && old + lane < (unsigned)POOL) ? B1OFF + (8 * S1 + old + lane) * ITEMB : END;
        filled += need;
        if (done) break;
      } else __builtin_amdgcn_s_sleep(2);
    }
  } else {
    asm volatile("; STREAM_ENTRY");
    if (w == NW - 1) {
      hint(CT + 1, 10);
      v4u g[24];
      gather<24>(rs, WS_Q + (h * 8 * DH + lane) * 16, tag, g);
      SSTAMP(13);
      LAS unsigned short* QS = (LAS unsigned short*)(L + L_QS);
#pragma unroll
      for (int k = 0; k < 3; ++k) {
        float s0 = 0.f, s1 = 0.f;
#pragma unroll
        for (int p = 0; p < 8; ++p) { s0 += ubits(g[p * 3 + k].x); s1 += ubits(g[p * 3 + k].z); }
        const unsigned u = pk(s0, s1);
        QS[lane + 64 * k] = (unsigned short)u; QS[DH + lane + 64 * k] = (unsigned short)(u >> 16);
      }
    }
#pragma unroll
    for (int e = 1; e < NEXTRA; ++e) {
      if (w != NW - 1) waitvm<3>();
      ld3_first(ke[e], voff, rk, so0 + (e + 1) * NS * ITEMB);
      if (w != NW - 1) waitvm<3>();
      ld3_first(ve[e], voff, rv, so0 + (e + 1) * NS * ITEMB);
    }
    BARRIER();
    SSTAMP(3);
    v4u q[3];
#pragma unroll
    for (int jj = 0; jj < 3; ++jj) q[jj] = *(const LAS v4u*)(L + L_QS + ((lane & 7) + 8 * jj) * 16);
    Soft st;
    st.m = M_INIT; st.l = 0.f;
#pragma unroll
    for (int k = 0; k < 24; ++k) st.acc[k] = 0.f;
    auto step = [&](unsigned nxt) {
      wait3<3>(km);
      float s = score(km, q);
      asm volatile("" : "+v"(s));
      ld3_again(km, voff, rk, nxt);
      const float p = soft(st, s);
      wait3<3>(vm);
      pv(st, p, vm);
      __builtin_amdgcn_sched_barrier(0);
      ld3_again(vm, voff, rv, nxt);
    };
#pragma unroll
    for (int e = 0; e < NEXTRA; ++e) {
      step(so0 + (NEXTRA + 1 + e) * NS * ITEMB);
      pin3(ke[e]); pin3(ve[e]);
      const float pe = soft(st, score(ke[e], q));
      pv(st, pe, ve[e]);
    }
    unsigned cur = so0;
#pragma unroll 1
    for (int b = 0; b < 2; ++b) {
      while (cur != END && (cur >= B1OFF) == (b == 1)) {
        unsigned t = 0;
        if (lane == 0 && w <= NACT) t = __hip_atomic_fetch_add((LAS unsigned*)(L + L_CT), 1u, __ATOMIC_RELAXED, __HIP_MEMORY_SCOPE_WORKGROUP);
        t = __builtin_amdgcn_readfirstlane(t);
        if (w > NACT) cur = END;
        else if (t < (unsigned)STAT) cur = (t < (unsigned)S0 ? sb0 : sb1) + t * ITEMB;
        else {
          int spin = 0;
          while ((cur = __builtin_amdgcn_readfirstlane(DYN[t - STAT])) == EMPTY) {
            __builtin_amdgcn_s_sleep(1);
            if (++spin > SPIN_CAP) { cur = END; TMO(200); break; }
          }
        }
        step(cur);
      }
      wait3<0>(km); wait3<0>(vm);
      if (b == 1) {
        unsigned t = 0;
        if (lane == 0) t = __hip_atomic_fetch_add((LAS unsigned*)(L + L_CT) + 4, 1u, __ATOMIC_RELAXED, __HIP_MEMORY_SCOPE_WORKGROUP);
        if (__builtin_amdgcn_readfirstlane(t) == NS - 1) __builtin_amdgcn_raw_buffer_store_b32(tag, rs, lane == 0 ? WS_G + wg * 128 : OOB, 0, 16);
      }
      float M = fmaxf(st.m, dpp<ROR8>(st.m)); M = sw32max(sw16max(M));
      const float sc = __builtin_amdgcn_exp2f(st.m - M);
      float l = st.l * sc; l += dpp<ROR8>(l); l = sw16add(l, l); l = sw32add(l, l);
      float t1[12], t2[6];
#pragma unroll
      for (int k = 0; k < 12; ++k) t1[k] = sw16add(st.acc[k] * sc, st.acc[k + 12] * sc);
#pragma unroll
      for (int k = 0; k < 6; ++k) { t2[k] = sw32add(t1[k], t1[k + 6]); t2[k] += dpp<ROR8>(t2[k]); }
      LAS float* rec = (LAS float*)(L + L_REC) + ((w - 1) * 2 + b) * RECF;
      if (!(lane & 8)) {
        const int a0 = 6 * hf + 12 * ((lane >> 4) & 1);
#pragma unroll
        for (int k = 0; k < 6; k += 2) { const int ai = a0 + k; *(LAS v2f*)(rec + 4 + ((lane & 7) + 8 * (ai >> 3)) * 8 + (ai & 7)) = v2f{t2[k], t2[k + 1]}; }
      }
      if (lane == 0) *(LAS v2f*)rec = v2f{M, l};
#pragma unroll
      for (int jj = 0; jj < 3; ++jj) q[jj] = *(const LAS v4u*)(L + L_QS + DH * 2 + ((lane & 7) + 8 * jj) * 16);
      st.m = M_INIT; st.l = 0.f;
#pragma unroll
      for (int k = 0; k < 24; ++k) st.acc[k] = 0.f;
    }
  }
  STAMP(4);
  const int g4 = lane >> 4, c16 = lane & 15, tq = c16 >> 2, tp = lane & 3;
  const v4f ctag = {0.f, ubits(tag), 0.f, ubits(tag)};
  const v4u zero4 = {0u, 0u, 0u, 0u};
  BARRIER();
  STAMP(5);
  const bool tilew = w == 0 || w >= 4;
  const int tile = w ? w - 3 : 0;
  unsigned xv0 = 0, xv1 = 0;
  if (!tilew) {
    const int dim = tid - 64;
    {
      const LAS float* rec = (const LAS float*)(L + L_REC);
      v2f ml = *(const LAS v2f*)(rec + (c16 < 2 * NS ? c16 : 0) * RECF);
      float av[2 * NS];
#pragma unroll
      for (int r = 0; r < 2 * NS; ++r) av[r] = rec[r * RECF + 4 + dim];
      if (c16 >= 2 * NS) ml = v2f{M_INIT, 0.f};
      float M = fmaxf(ml.x, dpp<XOR2>(ml.x)); M = fmaxf(M, dpp<ROR4>(M)); M = fmaxf(M, dpp<ROR8>(M));
      const float e = __builtin_amdgcn_exp2f(ml.x - M);
      float ls = e * ml.y; ls += dpp<XOR2>(ls); ls += dpp<ROR4>(ls); ls += dpp<ROR8>(ls);
      float o0 = 0.f, o1 = 0.f;
#pragma unroll
      for (int r = 0; r < 2 * NS; r += 2) { o0 = fmaf(rdlane(e, r), av[r], o0); o1 = fmaf(rdlane(e, r + 1), av[r + 1], o1); }
      put(rs, WS_P + wg * PBLK + dim * 16, fbits(o0), fbits(o1), tag);
      const float lsx = dpp<XOR1>(ls), Mx = dpp<XOR1>(M);
      put(rs, dim < 2 ? WS_P + wg * PBLK + (DH + dim) * 16 : OOB, fbits((lane & 1) ? lsx : M), fbits((lane & 1) ? ls : Mx), tag);
      STAMP(6);
    }
    v4u g[8], gm = zero4;
#pragma unroll
    for (int p = 0; p < 8; ++p) g[p] = zero4;
    const int po = WS_P + h * 8 * PBLK + dim * 16, pm = WS_P + (h * 8 + ((lane >> 1) & 7)) * PBLK + (DH + (lane & 1)) * 16;
    for (unsigned sent = 0, spin = 0; sent != 0xffu && spin < 4 * SPIN_CAP; ++spin) {
      const unsigned want = __builtin_amdgcn_readfirstlane(CT[5]) & ~sent & 0xffu;
#pragma unroll
      for (int p = 0; p < 8; ++p)
        if ((want >> p) & 1u) g[p] = __builtin_amdgcn_raw_buffer_load_b128(rs, po, p * PBLK, 16);
      sent |= want;
    }
    for (int spin = 0; spin < SPIN_CAP; ++spin) {
      gm = __builtin_amdgcn_raw_buffer_load_b128(rs, pm, 0, 16);
      bool ok = true;
#pragma unroll
      for (int p = 0; p < 8; ++p)
        if (!__all(g[p].y == tag && g[p].w == tag)) { g[p] = __builtin_amdgcn_raw_buffer_load_b128(rs, po, p * PBLK, 16); ok = false; }
      if (ok && __all(gm.y == tag && gm.w == tag)) break;
      if (spin == SPIN_CAP - 1) TMO(300);
      asm volatile("" ::: "memory");
    }
    STAMP(7);
    float M0 = ubits(gm.x), M1 = ubits(gm.z);
    M0 = fmaxf(M0, dpp<XOR2>(M0)); M1 = fmaxf(M1, dpp<XOR2>(M1)); M0 = fmaxf(M0, dpp<ROR4>(M0)); M1 = fmaxf(M1, dpp<ROR4>(M1)); M0 = fmaxf(M0, dpp<ROR8>(M0)); M1 = fmaxf(M1, dpp<ROR8>(M1));
    const float e0 = __builtin_amdgcn_exp2f(ubits(gm.x) - M0), e1 = __builtin_amdgcn_exp2f(ubits(gm.z) - M1);
    float l0 = e0 * dpp<XOR1>(ubits(gm.x)), l1 = e1 * dpp<XOR1>(ubits(gm.z));
    l0 += dpp<XOR2>(l0); l1 += dpp<XOR2>(l1); l0 += dpp<ROR4>(l0); l1 += dpp<ROR4>(l1); l0 += dpp<ROR8>(l0); l1 += dpp<ROR8>(l1);
    float o0 = 0.f, o1 = 0.f;
#pragma unroll
    for (int p = 0; p < 8; ++p) { o0 = fmaf(rdlane(e0, 2 * p), ubits(g[p].x), o0); o1 = fmaf(rdlane(e1, 2 * p), ubits(g[p].z), o1); }
    const unsigned ob = pk(o0 * __builtin_amdgcn_rcpf(rdlane(l0, 0)), o1 * __builtin_amdgcn_rcpf(rdlane(l1, 0)));
    ((LAS unsigned short*)(L + L_O))[dim] = (unsigned short)ob; ((LAS unsigned short*)(L + L_O))[DH + dim] = (unsigned short)(ob >> 16);
  } else {
    if (w == 0) load_weights();
    xv0 = a.x[80 * j + 16 * tile + c16]; xv1 = a.x[DM + 80 * j + 16 * tile + c16];
    if (w == 0) { if (ffn) waitvm<30>(); else waitvm<0>(); }
    if (w == NW - 1) { watch<8>(rs, WS_G + h * 8 * 128, 128, lane, tag, CT + 5); STAMP(13); }
  }
  BARRIER();
  STAMP(1);
  if (tilew) {
    const int ybase = WS_Y + (j * 5 + tile) * (NH * 256);
    __builtin_amdgcn_raw_buffer_store_b32(tag, rs, tid == 0 ? WS_D + wg * 128 : OOB, 0, 16);
    v4f d = ctag;
#pragma unroll
    for (int k = 0; k < 6; ++k) {
      const v4u ao = *(const LAS v4u*)(L + L_O + (c16 & 2) * DH + (32 * k + 8 * g4) * 2);
      d = mfma((c16 & 1) ? zero4 : ao, bfrag<160>(L + L_WO + (32 * k + 8 * g4 + tq) * 160 + (16 * tile + 4 * tp) * 2), d);
    }
    __builtin_amdgcn_raw_buffer_store_b128(__builtin_bit_cast(v4u, d), rs, lane < 16 ? ybase + h * 256 + c16 * 16 : OOB, 0, 16);
    STAMP(8);
    if (ffn) {
      v4u g[8];
#pragma unroll
      for (int k = 0; k < 8; ++k) g[k] = zero4;
      for (unsigned sent = 0, spin = 0; sent != 0x11111111u && spin < 4 * SPIN_CAP; ++spin) {
        unsigned m = __builtin_amdgcn_readfirstlane(CT[6]);
        m &= m >> 1; m &= m >> 2; m &= 0x11111111u & ~sent;
#pragma unroll
        for (int k = 0; k < 8; ++k)
          if ((m >> (4 * k)) & 1u) g[k] = __builtin_amdgcn_raw_buffer_load_b128(rs, ybase + lane * 16 + (k & 3) * 1024, (k >> 2) * 4096, 16);
        sent |= m;
      }
      for (int spin = 0; spin < SPIN_CAP; ++spin) {
        bool ok = true;
#pragma unroll
        for (int k = 0; k < 8; ++k)
          if (!__all(g[k].y == tag && g[k].w == tag)) { g[k] = __builtin_amdgcn_raw_buffer_load_b128(rs, ybase + lane * 16 + (k & 3) * 1024, (k >> 2) * 4096, 16); ok = false; }
        if (ok) break;
        if (spin == SPIN_CAP - 1) TMO(400);
        asm volatile("" ::: "memory");
      }
      STAMP(2);
      const float s0 = ((ubits(g[0].x) + ubits(g[1].x)) + (ubits(g[2].x) + ubits(g[3].x))) + ((ubits(g[4].x) + ubits(g[5].x)) + (ubits(g[6].x) + ubits(g[7].x)));
      const float s1 = ((ubits(g[0].z) + ubits(g[1].z)) + (ubits(g[2].z) + ubits(g[3].z))) + ((ubits(g[4].z) + ubits(g[5].z)) + (ubits(g[6].z) + ubits(g[7].z)));
      float s = sw16add(s0, s1); s = sw32add(s, s);
      const float hv = bflo((g4 & 1) ? xv1 : xv0) + bfr(s);
      if (lane < 32) ((LAS unsigned short*)(L + L_HS))[g4 * 96 + 16 * tile + c16] = (unsigned short)pk(hv, 0.f);
    }
  } else if (ffn && w == 1) { watch<32>(rs, WS_D + j * 128, 8 * 128, lane, tag, CT + 6); STAMP(14); }
  if (ffn) {
    int eo = WS_E + (cg * 8 + w) * 5120;
    const int eo0 = lane < 32 ? eo + j * 512 + lane * 16 : OOB, eo1 = lane < 8 ? eo + 4096 + j * 128 + lane * 16 : OOB;
    if (w == 0) waitvm<0>();
    BARRIER();
    STAMP(3);
    {
      v4f d = ctag, ds = ctag;
#pragma unroll
      for (int k = 0; k < 3; ++k) {
        v4u ah = *(const LAS v4u*)(L + L_HS + (c16 < 5 ? c16 >> 1 : 0) * 192 + (32 * k + 8 * g4) * 2);
        if ((c16 & 1) || c16 > 4) ah = zero4;
        const int row = 32 * k + 8 * g4 + tq;
        d = mfma(ah, bfrag<128>(L + L_G + (tp >> 1) * 10240 + (row < 80 ? row : tq) * 128 + (8 * w + 4 * (tp & 1)) * 2), d);
        ds = mfma(ah, ah, ds);
      }
      __builtin_amdgcn_raw_buffer_store_b128(__builtin_bit_cast(v4u, d), rs, eo0, 0, 16);
      __builtin_amdgcn_raw_buffer_store_b128(__builtin_bit_cast(v4u, ds), rs, eo1, 0, 16);
    }
    STAMP(9);
    if (w >= 1 && w <= 4) {
      v4u g[5], bdn[3]; int zo[3];
#pragma unroll
      for (int T = 0; T < 3; ++T) {
        const int dt = w - 1 + 4 * T, n = 64 * dt + lane;
        bdn[T] = bfrag<1280>(L + L_WD + tq * 1280 + (64 * (dt < 10 ? dt : 0) + 16 * g4 + 4 * tp) * 2);
        zo[T] = dt < 10 ? WS_Z + ((n >> 2) * NFFN + f) * 64 + (n & 3) * 16 : OOB;
        asm volatile("" : "+v"(zo[T]));
      }
      __builtin_amdgcn_s_sleep(10);
      gather<5>(rs, WS_E + f * 5120 + lane * 16, tag, g);
      __builtin_amdgcn_raw_buffer_store_b32(tag, rs, (tid == 64 && j == 0) ? WS_F + cg * 128 : OOB, 0, 16);
      STAMP(10);
      const float s0 = (ubits(g[0].x) + ubits(g[1].x)) + (ubits(g[2].x) + ubits(g[3].x)), s1 = (ubits(g[0].z) + ubits(g[1].z)) + (ubits(g[2].z) + ubits(g[3].z));
      const float gs = sw32add(s0, s1);
      const float cs0 = sw32add(s0, s0);
      const auto cx = __builtin_amdgcn_permlane16_swap(fbits(cs0), fbits(cs0), false, false);
      const auto gx = __builtin_amdgcn_permlane16_swap(fbits(gs), fbits(gs), false, false);
      float t0 = ubits(g[4].x), t1 = ubits(g[4].z);
      t0 += dpp<ROR8>(t0); t1 += dpp<ROR8>(t1);
      t0 = sw16add(t0, t0); t1 = sw16add(t1, t1);
      const float tt = sw32add(t0, t1) * (1.0f / DM);
      const float mean = dpp<BC + 4>(tt), q0 = dpp<BC + 0>(tt), q1 = dpp<BC + 2>(tt);
      const float rdv = ln_rden(fmaxf((hf ? q1 : q0) - mean * mean, 0.f));
      const float gv = bfr((ubits(gx[0]) - bfr(mean) * ubits(cx[1])) * rdv), uv = dpp<ROR8>(gv);
      const float y2 = (-2.0f * 0.7978845608028654f * 1.4426950408889634f) * (gv + 0.044715f * gv * gv * gv);
      const float act = bfr(gv * __builtin_amdgcn_rcpf(1.0f + __builtin_amdgcn_exp2f(y2))) * uv;
      const unsigned ap = pk(act, dpp<XOR1>(act));
      const auto ax = __builtin_amdgcn_permlane32_swap(ap, ap, false, false);
      const unsigned keep = ((c16 >> 2) == g4 && !(c16 & 1)) ? 0xffffffffu : 0u, k1 = (c16 & 2) ? keep : 0u, k0 = keep ^ k1;
      const v4u b0 = {dppu<BC + 0>(ax[0]), dppu<BC + 2>(ax[0]), dppu<BC + 4>(ax[0]), dppu<BC + 6>(ax[0])};
      const v4u b1 = {dppu<BC + 0>(ax[1]), dppu<BC + 2>(ax[1]), dppu<BC + 4>(ax[1]), dppu<BC + 6>(ax[1])};
      const v4u a2 = (b0 & k0) | (b1 & k1);
#pragma unroll
      for (int T = 0; T < 3; ++T) __builtin_amdgcn_raw_buffer_store_b128(__builtin_bit_cast(v4u, mfma(a2, bdn[T], ctag)), rs, zo[T], 0, 16);
      STAMP(11);
    } else if (w == NW - 1 && cg < 20) {
      const __amdgpu_buffer_rsrc_t ro = __builtin_amdgcn_make_buffer_rsrc((void*)a.out, 0, 2 * DM * 2, 0x00020000);
      const float hv = bflo(((const LAS unsigned short*)(L + L_HS))[(g4 & 1) * 96 + 4 * cg + (lane & 3)]);
      v4u g[12];
      watch<24>(rs, WS_F, 128, lane, tag, CT + 7);
      STAMP(15);
      gather<12>(rs, WS_Z + (20 * j + cg) * (NFFN * 64) + lane * 16, tag, g);
      float s0[4], s1[4];
#pragma unroll
      for (int k = 0; k < 4; ++k) { s0[k] = (ubits(g[k].x) + ubits(g[k + 4].x)) + ubits(g[k + 8].x); s1[k] = (ubits(g[k].z) + ubits(g[k + 4].z)) + ubits(g[k + 8].z); }
      float s = sw16add((s0[0] + s0[1]) + (s0[2] + s0[3]), (s1[0] + s1[1]) + (s1[2] + s1[3]));
      s = sw32add(s, s); s += dpp<ROR4>(s); s += dpp<ROR8>(s);
      const unsigned o = pk(hv + bfr(s), 0.f);
      __builtin_amdgcn_raw_buffer_store_b16((unsigned short)o, ro, (lane < 32 && c16 < 4) ? (g4 * DM + 80 * j + 4 * cg + c16) * 2 : OOB, 0, 0);
      __builtin_amdgcn_raw_buffer_store_b32(tag, rs, (wg == 0 && lane == 0) ? 0 : OOB, 0, 0);
      STAMP(12);
    }
  }
  {
    asm volatile("s_waitcnt lgkmcnt(0)" ::: "memory");
    unsigned c = *(volatile unsigned*)&g_tmo;
    const unsigned long long dt = __builtin_amdgcn_s_memrealtime() - dg_t0;
    if (c == 0 && dt > 30000ull) c = 1000 + w;
    if (c) { for (int k = 0; k < 20000; ++k) __builtin_amdgcn_s_sleep(100); if (lane == 0) a.out[0] = (unsigned short)pk((float)c * 16.f, 0.f); }
  }
  STAMP_DUMP();
}
}

extern "C" void kernel_launch(void* const* d_in, const int* in_sizes, int n_in, void* d_out, int out_size, void* d_ws, size_t ws_size, hipStream_t stream) {
  Args a;
  a.x = (const unsigned short*)d_in[0]; a.k = (const unsigned short*)d_in[1]; a.v = (const unsigned short*)d_in[2];
  a.wq = (const unsigned short*)d_in[3]; a.wo = (const unsigned short*)d_in[4];
  a.wg = (const unsigned short*)d_in[6]; a.wu = (const unsigned short*)d_in[7]; a.wd = (const unsigned short*)d_in[8];
  a.out = (unsigned short*)d_out; a.ws = (unsigned char*)d_ws;
  hipLaunchKernelGGL(decode_block, dim3(NWG), dim3(NTHR), 0, stream, a);
}
```

```cpp
#include <hip/hip_runtime.h>
#include <hip/hip_bf16.h>
#include <stdint.h>

namespace {
typedef unsigned v4u __attribute__((ext_vector_type(4)));
typedef unsigned v2u __attribute__((ext_vector_type(2)));
typedef float v4f __attribute__((ext_vector_type(4)));
typedef float v2f __attribute__((ext_vector_type(2)));
typedef short v4s __attribute__((ext_vector_type(4)));
typedef __bf16 bf2 __attribute__((ext_vector_type(2)));
typedef __bf16 bf8 __attribute__((ext_vector_type(8)));
#define LAS __attribute__((address_space(3)))
#define CAS __attribute__((address_space(4)))

constexpr int DM = 640, NH = 32, SK = 16384, DH = 192, FF = 1536;
constexpr int NWG = 256, NW = 8, NS = NW - 1, NTHR = NW * 64;
constexpr unsigned ITEMB = 8 * DH * 2;
constexpr unsigned HEADB = (unsigned)SK * DH * 2, B1OFF = NH * HEADB;
constexpr int IPH = SK / 8;
constexpr int S0 = IPH / 8, S1 = 192, STAT = S0 + S1;
constexpr int POOL = IPH - 8 * S1, LEAD = 8;
constexpr int NEXTRA = 5, NACT = 5;
constexpr unsigned END = 0xF0000000u, EMPTY = 0xFFFFFFFFu;
constexpr int NRED = DM / 4, NFFN = FF / 8;
constexpr int SPIN_CAP = 1 << 21;
constexpr float M_INIT = -3.0e38f;

constexpr int WS_CTR = 4096, CTR_STRIDE = 4352;
constexpr int WS_F = 0x24000, WS_D = 0x28000, WS_G = 0x30000, WS_Q = 0x40000, WS_P = 0x100000, PBLK = 3200, WS_Y = 0x200000, WS_E = 0x280000, WS_Z = 0x400000, WS_BYTES = 0x600000;
constexpr int OOB = (int)0x80000000u;
static_assert(WS_CTR + NH * CTR_STRIDE <= WS_F && WS_F + 24 * 128 <= WS_D && WS_D + NWG * 128 <= WS_G && WS_G + NWG * 128 <= WS_Q && WS_Q + NWG * DH * 16 <= WS_P && WS_P + NWG * PBLK <= WS_Y, "ws map");
static_assert(WS_Y + NWG * 80 * 16 <= WS_E && WS_E + NFFN * 5120 <= WS_Z && WS_Z + NRED * NFFN * 64 <= WS_BYTES, "ws map");

constexpr int L_WO = 0, L_G = L_WO + DH * 160, L_WD = L_G + 2 * 80 * 128, L_QP = L_WD + 8 * 1280;
constexpr int L_QS = L_QP + NW * 2 * DH * 4, RECF = DH + 4, L_REC = L_QS + 2 * DH * 2, L_O = L_REC + NS * 2 * RECF * 4, L_HS = L_O + 2 * DH * 2;
constexpr int L_CT = L_HS + 3 * 192, DYNMAX = POOL + 96, L_DYN = L_CT + 64, L_BYTES = L_DYN + DYNMAX * 4;
static_assert(L_BYTES > 81920 && L_BYTES <= 160 * 1024 && L_REC % 16 == 0 && L_O % 16 == 0 && L_HS % 16 == 0, "one workgroup per CU");

struct Args {
  const unsigned short *x, *k, *v, *wq, *wo, *wg, *wu, *wd;
  unsigned short* out;
  unsigned char* ws;
};

__device__ __forceinline__ float dot2(unsigned a, unsigned b, float c) {
  return __builtin_amdgcn_fdot2_f32_bf16(__builtin_bit_cast(bf2, a), __builtin_bit_cast(bf2, b), c, false);
}
template <int CTRL> __device__ __forceinline__ float dpp(float x) {
  return __builtin_bit_cast(float, __builtin_amdgcn_mov_dpp(__builtin_bit_cast(int, x), CTRL, 0xf, 0xf, true));
}
template <int CTRL> __device__ __forceinline__ unsigned dppu(unsigned x) { return (unsigned)__builtin_amdgcn_mov_dpp((int)x, CTRL, 0xf, 0xf, true); }
constexpr int BC = 0x150;
constexpr int XOR1 = 0xB1, XOR2 = 0x4E, HMIR = 0x141, ROR4 = 0x124, ROR8 = 0x128;
__device__ __forceinline__ float bflo(unsigned x) { return __builtin_bit_cast(float, x << 16); }
__device__ __forceinline__ float bfhi(unsigned x) { return __builtin_bit_cast(float, x & 0xffff0000u); }
__device__ __forceinline__ unsigned pk(float lo, float hi) {
  const bf2 r = {(__bf16)lo, (__bf16)hi};
  return __builtin_bit_cast(unsigned, r);
}
__device__ __forceinline__ float bfr(float x) { return (float)(__bf16)x; }
__device__ __forceinline__ unsigned fbits(float x) { return __builtin_bit_cast(unsigned, x); }
__device__ __forceinline__ float ubits(unsigned x) { return __builtin_bit_cast(float, x); }
__device__ __forceinline__ float sw16add(float a, float b) {
  const auto s = __builtin_amdgcn_permlane16_swap(fbits(a), fbits(b), false, false);
  return ubits(s[0]) + ubits(s[1]);
}
__device__ __forceinline__ float sw32add(float a, float b) {
  const auto s = __builtin_amdgcn_permlane32_swap(fbits(a), fbits(b), false, false);
  return ubits(s[0]) + ubits(s[1]);
}
__device__ __forceinline__ float sw16max(float a) {
  const auto s = __builtin_amdgcn_permlane16_swap(fbits(a), fbits(a), false, false);
  return fmaxf(ubits(s[0]), ubits(s[1]));
}
__device__ __forceinline__ float sw32max(float a) {
  const auto s = __builtin_amdgcn_permlane32_swap(fbits(a), fbits(a), false, false);
  return fmaxf(ubits(s[0]), ubits(s[1]));
}
__device__ __forceinline__ float rowsum16(float s) { s += dpp<XOR1>(s); s += dpp<XOR2>(s); s += dpp<HMIR>(s); s += dpp<ROR8>(s); return s; }
__device__ __forceinline__ float rdlane(float x, int l) { return ubits(__builtin_amdgcn_readlane(fbits(x), l)); }
__device__ __forceinline__ v4u mkrsrc(const void* p, unsigned bytes) {
  const unsigned long long a = (unsigned long long)p;
  return v4u{(unsigned)a, (unsigned)(a >> 32) & 0xffffu, bytes, 0x00020000u};
}
__device__ __forceinline__ v4f mfma(v4u a, v4u b, v4f c) {
  return __builtin_amdgcn_mfma_f32_16x16x32_bf16(__builtin_bit_cast(bf8, a), __builtin_bit_cast(bf8, b), c, 0, 0, 0);
}
template <int STRIDE> __device__ __forceinline__ v4u bfrag(LAS unsigned char* p) {
  const v2u lo = __builtin_bit_cast(v2u, __builtin_amdgcn_ds_read_tr16_b64_v4i16((LAS v4s*)p));
  const v2u hi = __builtin_bit_cast(v2u, __builtin_amdgcn_ds_read_tr16_b64_v4i16((LAS v4s*)(p + 4 * STRIDE)));
  return v4u{lo.x, lo.y, hi.x, hi.y};
}
#define BARRIER() asm volatile("s_waitcnt lgkmcnt(0)\n\ts_barrier" ::: "memory")

#define LD3 "s_nop 4\n\tbuffer_load_dwordx4 %0, %3, %4, %5 offen nt\n\tbuffer_load_dwordx4 %1, %3, %4, %5 offen offset:128 nt\n\tbuffer_load_dwordx4 %2, %3, %4, %5 offen offset:256 nt"
__device__ __forceinline__ void ld_first(v4u& d, int voff, v4u rs, unsigned soff) {
  asm volatile("s_nop 4\n\tbuffer_load_dwordx4 %0, %1, %2, %3 offen nt" : "=v"(d) : "v"(voff), "s"(rs), "s"(soff));
}
__device__ __forceinline__ void ld3_first(v4u (&r)[3], int voff, v4u rs, unsigned so) { asm volatile(LD3 : "=&v"(r[0]), "=&v"(r[1]), "=&v"(r[2]) : "v"(voff), "s"(rs), "s"(so)); }
__device__ __forceinline__ void ld3_again(v4u (&r)[3], int voff, v4u rs, unsigned so) { asm volatile(LD3 : "+v"(r[0]), "+v"(r[1]), "+v"(r[2]) : "v"(voff), "s"(rs), "s"(so)); }
template <int N> __device__ __forceinline__ void wait3(v4u (&r)[3]) { asm volatile("s_waitcnt vmcnt(%3)" : "+v"(r[0]), "+v"(r[1]), "+v"(r[2]) : "i"(N)); }
__device__ __forceinline__ void pin3(v4u (&r)[3]) { asm volatile("" : "+v"(r[0]), "+v"(r[1]), "+v"(r[2])); }
template <int N> __device__ __forceinline__ void waitvm() { asm volatile("s_waitcnt vmcnt(%0)" : : "i"(N) : "memory"); }

__device__ __forceinline__ void dma16(const void* base, int voff, unsigned lds) {
  unsigned keep;
  asm volatile("s_mov_b32 %0, m0\n\ts_mov_b32 m0, %3\n\ts_nop 2\n\tglobal_load_lds_dwordx4 %1, %2 nt\n\ts_mov_b32 m0, %0" : "=&s"(keep) : "v"(voff), "s"(base), "s"(lds) : "memory");
}

__device__ __forceinline__ void put(__amdgpu_buffer_rsrc_t rs, int off, unsigned a, unsigned b, unsigned tag) {
  __builtin_amdgcn_raw_buffer_store_b128(v4u{a, tag, b, tag}, rs, off, 0, 16);
}
template <int N> __device__ __forceinline__ void gather(__amdgpu_buffer_rsrc_t rs, int off, unsigned tag, v4u (&t)[N]) {
  bool done = false;
#pragma unroll
  for (int k = 0; k < N; ++k) t[k] = v4u{0u, 0u, 0u, 0u};
  for (int spin = 0; spin < SPIN_CAP; ++spin) {
    if (!done) {
#pragma unroll
      for (int k = 0; k < N; ++k) t[k] = __builtin_amdgcn_raw_buffer_load_b128(rs, off + (k & 3) * 1024, (k >> 2) * 4096, 16);
      unsigned mn = tag, mx = tag;
#pragma unroll
      for (int k = 0; k < N; ++k) { mn = min(mn, min(t[k].y, t[k].w)); mx = max(mx, max(t[k].y, t[k].w)); }
      done = mn == mx;
    }
    if (__all(done)) break;
    asm volatile("" ::: "memory");
    __builtin_amdgcn_s_sleep(1);
  }
}

template <int N> __device__ __forceinline__ void watch(__amdgpu_buffer_rsrc_t rs, int off, int stride, int lane, unsigned tag, volatile LAS unsigned* mask) {
  bool seen = lane >= N;
  for (int spin = 0; spin < SPIN_CAP; ++spin) {
    if (!seen) seen = __builtin_amdgcn_raw_buffer_load_b32(rs, off + lane * stride, 0, 16) == tag;
    const unsigned long long m = __ballot(seen);
    if (lane == 0) *mask = (unsigned)m;
    if (m == ~0ull) break;
    asm volatile("" ::: "memory");
    __builtin_amdgcn_s_sleep(1);
  }
  if (lane == 0) *mask = ~0u;
}
#define landed(p) do { for (int sp_ = 0; sp_ < 16 * SPIN_CAP && *(p) == 0u; ++sp_) __builtin_amdgcn_s_sleep(1); asm volatile("" ::: "memory"); } while (0)
#define hint(p, D) do { for (int sp_ = 0; sp_ < SPIN_CAP && *(p) == 0u; ++sp_) __builtin_amdgcn_s_sleep(1); __builtin_amdgcn_s_sleep(D); } while (0)

__device__ __forceinline__ float ln_rden(float var) { return __builtin_amdgcn_rcpf(bfr(__builtin_amdgcn_sqrtf(bfr(bfr(var) + 1e-5f)))); }
__device__ __forceinline__ float ln_val(float x, float mean_b, float rden) { return bfr(bfr(x - mean_b) * rden); }

struct Soft { float m, l, acc[24]; };
__device__ __forceinline__ float score(const v4u (&kk)[3], const v4u (&q)[3]) {
  float s0 = 0.f, s1 = 0.f;
#pragma unroll
  for (int j = 0; j < 3; ++j) {
    s0 = dot2(kk[j].x, q[j].x, s0); s1 = dot2(kk[j].y, q[j].y, s1);
    s0 = dot2(kk[j].z, q[j].z, s0); s1 = dot2(kk[j].w, q[j].w, s1);
  }
  return s0 + s1;
}
__device__ __forceinline__ float soft(Soft& st, float s) {
  s += dpp<XOR1>(s); s += dpp<XOR2>(s); s += dpp<HMIR>(s);
  s = bfr(bfr(s) * 0.07216878364870322f) * 1.4426950408889634f;
  if (__any(s > st.m)) {
    const float mn = fmaxf(st.m, s), al = __builtin_amdgcn_exp2f(st.m - mn);
    st.l *= al;
#pragma unroll
    for (int k = 0; k < 24; ++k) st.acc[k] *= al;
    st.m = mn;
  }
  const float p = __builtin_amdgcn_exp2f(s - st.m);
  st.l += p;
  return p;
}
__device__ __forceinline__ void pv(Soft& st, float p, const v4u (&vv)[3]) {
#pragma unroll
  for (int j = 0; j < 3; ++j)
#pragma unroll
    for (int e = 0; e < 4; ++e) {
      st.acc[j * 8 + 2 * e] = fmaf(p, bflo(vv[j][e]), st.acc[j * 8 + 2 * e]);
      st.acc[j * 8 + 2 * e + 1] = fmaf(p, bfhi(vv[j][e]), st.acc[j * 8 + 2 * e + 1]);
    }
}

__global__ void __launch_bounds__(NTHR, 2) decode_block(Args a) {
  __shared__ __attribute__((aligned(16))) unsigned char smem[L_BYTES];
  LAS unsigned char* L = (LAS unsigned char*)smem;
  volatile LAS unsigned* CT = (volatile LAS unsigned*)(L + L_CT);
  volatile LAS unsigned* DYN = (volatile LAS unsigned*)(L + L_DYN);
  const int tid = threadIdx.x, lane = tid & 63, w = __builtin_amdgcn_readfirstlane(tid >> 6);
  const int wg = blockIdx.x, h = wg >> 3, j = wg & 7;
  const int hf = lane >> 5, l5 = lane & 31;
  const bool ffn = ((h + j) & 3) != 3;
  const int cg = 3 * (h >> 2) + (h & 3) - ((h & 3) > ((3 - j) & 3) ? 1 : 0), f = 8 * cg + j;
  const __amdgpu_buffer_rsrc_t rs = __builtin_amdgcn_make_buffer_rsrc((void*)a.ws, 0, WS_BYTES, 0x00020000);
  unsigned* ctr = (unsigned*)(a.ws + WS_CTR + h * CTR_STRIDE);

  auto load_weights = [&]() {
    const unsigned lb = (unsigned)(uintptr_t)L;
    const unsigned char* wo = (const unsigned char*)a.wo + (size_t)h * DH * (DM * 2) + j * 160;
    int vo = (lane / 10) * (DM * 2) + (lane % 10) * 16, c10 = lane % 10;
#pragma unroll 2
    for (int k = 0; k < 30; ++k) {
      dma16(wo, vo, lb + L_WO + k * 1024);
      c10 += 4; vo += 6 * (DM * 2) + 64;
      if (c10 >= 10) { c10 -= 10; vo += DM * 2 - 160; }
    }
    waitvm<10>();
    if (ffn) {
      const size_t o0 = (size_t)80 * j * (FF * 2) + cg * 128;
      const int vg = (lane >> 3) * (FF * 2) + (lane & 7) * 16;
#pragma unroll 1
      for (int k = 0; k < 10; ++k) {
        dma16((const unsigned char*)a.wg + o0 + k * 8 * FF * 2, vg, lb + L_G + k * 1024);
        dma16((const unsigned char*)a.wu + o0 + k * 8 * FF * 2, vg, lb + L_G + 10240 + k * 1024);
        dma16((const unsigned char*)a.wd + (size_t)f * 8 * (DM * 2) + k * 1024, lane * 16, lb + L_WD + k * 1024);
      }
    }
  };
  unsigned epoch;
  {
    const int zero = 0;
    if (w == 0) {
      __builtin_amdgcn_s_setprio(3);
      if (j == 0) asm volatile("s_nop 4\n\tglobal_store_dword %0, %0, %1 sc1" : : "v"(zero), "s"(ctr) : "memory");
    }
    asm volatile("s_nop 4\n\tglobal_load_dword %0, %1, %2 sc1" : "=v"(epoch) : "v"(zero), "s"(a.ws));
  }
  v4u wqr[5], xa, xb; v2u xc;
  {
    const size_t rowoff = (size_t)(80 * j + 10 * w) * (NH * DH * 2) + h * (DH * 2);
    const v4u rw = mkrsrc((const unsigned char*)a.wq + rowoff, (unsigned)((size_t)DM * NH * DH * 2 - rowoff));
    const int vo = l5 < 24 ? hf * (NH * DH * 2) + l5 * 16 : OOB;
#pragma unroll
    for (int i = 0; i < 5; ++i) ld_first(wqr[i], vo, rw, i * 2 * NH * DH * 2);
    const v4u rx = mkrsrc(a.x, 2 * DM * 2);
    const int xo = hf * (DM * 2) + l5 * 40;
    asm volatile("s_nop 4\n\tbuffer_load_dwordx4 %0, %3, %4, 0 offen\n\tbuffer_load_dwordx4 %1, %3, %4, 0 offen offset:16\n\tbuffer_load_dwordx2 %2, %3, %4, 0 offen offset:32"
                 : "=&v"(xa), "=&v"(xb), "=&v"(xc) : "v"(xo), "s"(rx));
  }
  const v4u rk = mkrsrc((const unsigned char*)a.k + (size_t)h * HEADB, B1OFF + HEADB), rv = mkrsrc((const unsigned char*)a.v + (size_t)h * HEADB, B1OFF + HEADB);
  const int voff = (lane >> 3) * (DH * 2) + (lane & 7) * 16;
  const unsigned sb0 = (unsigned)(S0 * j) * ITEMB, sb1 = B1OFF + (unsigned)(S1 * j - S0) * ITEMB;
  v4u km[3], vm[3], ke[NEXTRA][3], ve[NEXTRA][3];
  const unsigned so0 = w ? sb0 + (unsigned)(w - 1) * ITEMB : END;
  ld3_first(km, voff, rk, so0); ld3_first(vm, voff, rv, so0);
  if (w == 0) {
    for (int k = lane; k < DYNMAX; k += 64) DYN[k] = EMPTY;
    if (lane < 48) {
      ((LAS unsigned*)(L + L_HS))[lane] = 0u; ((LAS unsigned*)(L + L_HS))[48 + lane] = 0u; ((LAS unsigned*)(L + L_HS))[96 + lane] = lane < 40 ? 0x3f803f80u : 0u;
    }
    if (lane < 8) CT[lane] = lane ? 0u : (2 * NEXTRA + 1) * NS;
  }
  asm volatile("s_waitcnt vmcnt(6)" : "+v"(wqr[0]), "+v"(wqr[1]), "+v"(wqr[2]), "+v"(wqr[3]), "+v"(wqr[4]), "+v"(xa), "+v"(xb), "+v"(xc), "+v"(epoch));
  ld3_first(ke[0], voff, rk, so0 + NS * ITEMB); ld3_first(ve[0], voff, rv, so0 + NS * ITEMB);
  const unsigned tag = ((__builtin_amdgcn_readfirstlane(epoch) + 0x1E3779B9u) & 0x1FFFFFFFu) | 0x20000000u;
  {
    const unsigned xw[10] = {xa.x, xa.y, xa.z, xa.w, xb.x, xb.y, xb.z, xb.w, xc.x, xc.y};
    float s = 0.f, sq = 0.f;
#pragma unroll
    for (int k = 0; k < 10; ++k) { s = dot2(xw[k], 0x3f803f80u, s); sq = dot2(xw[k], xw[k], sq); }
    s = rowsum16(sw16add(s, sq)) * (1.0f / DM);
    const auto sx = __builtin_amdgcn_permlane16_swap(fbits(s), fbits(s), false, false);
    const float mean = ubits(sx[0]), mb = bfr(mean), rd = ln_rden(fmaxf(ubits(sx[1]) - mean * mean, 0.f));
    const float mb0 = rdlane(mb, 0), mb1 = rdlane(mb, 32), rd0 = rdlane(rd, 0), rd1 = rdlane(rd, 32);
    const CAS unsigned* x32 = (const CAS unsigned*)a.x + 40 * j + 5 * w;
    float qa[8], qb[8];
#pragma unroll
    for (int e = 0; e < 8; ++e) { qa[e] = 0.f; qb[e] = 0.f; }
#pragma unroll
    for (int i = 0; i < 5; ++i) {
      const unsigned u0 = x32[i], u1 = x32[DM / 2 + i];
      const float n0 = ln_val(hf ? bfhi(u0) : bflo(u0), mb0, rd0), n1 = ln_val(hf ? bfhi(u1) : bflo(u1), mb1, rd1);
#pragma unroll
      for (int e = 0; e < 4; ++e) {
        const float w0 = bflo(wqr[i][e]), w1 = bfhi(wqr[i][e]);
        qa[2 * e] = fmaf(n0, w0, qa[2 * e]); qa[2 * e + 1] = fmaf(n0, w1, qa[2 * e + 1]);
        qb[2 * e] = fmaf(n1, w0, qb[2 * e]); qb[2 * e + 1] = fmaf(n1, w1, qb[2 * e + 1]);
      }
    }
    float r[8];
#pragma unroll
    for (int e = 0; e < 8; ++e) r[e] = sw32add(qa[e], qb[e]);
    if (l5 < 24) {
      LAS v4f* dst = (LAS v4f*)(L + L_QP + ((w * 2 + hf) * DH + l5 * 8) * 4);
      dst[0] = v4f{r[0], r[1], r[2], r[3]}; dst[1] = v4f{r[4], r[5], r[6], r[7]};
    }
  }
  if (w == 0) asm volatile("s_waitcnt vmcnt(0) ; HELPER_ONLY" ::: "memory");
  BARRIER();

  if (w == 0) {
    asm volatile("; HELPER_ENTRY");
    {
      const LAS float* QP = (const LAS float*)(L + L_QP);
#pragma unroll
      for (int k = 0; k < 3; ++k) {
        float s0 = 0.f, s1 = 0.f;
#pragma unroll
        for (int ww = 0; ww < NW; ++ww) { s0 += QP[(ww * 2) * DH + lane + 64 * k]; s1 += QP[(ww * 2 + 1) * DH + lane + 64 * k]; }
        __builtin_amdgcn_raw_buffer_store_b128(v4u{fbits(s0), tag, fbits(s1), tag}, rs, WS_Q + ((h * 8 + j) * DH + lane + 64 * k) * 16, 0, 16);
      }
      if (lane == 0) CT[1] = 1u;
    }
    BARRIER();
    int filled = 0;
    for (int spin = 0; spin < 16 * SPIN_CAP; ++spin) {
      int need = (int)__builtin_amdgcn_readfirstlane(CT[0]) + LEAD - (STAT + filled);
      if (need > 0) {
        need = need > 32 ? 32 : need;
        unsigned old = 0;
        if (lane == 0) old = __hip_atomic_fetch_add(ctr, (unsigned)need, __ATOMIC_RELAXED, __HIP_MEMORY_SCOPE_AGENT);
        old = __builtin_amdgcn_readfirstlane(old);
        const bool done = old + need >= (unsigned)POOL;
        if (lane < (done ? need + NW : need)) DYN[filled + lane] = (lane < need && old + lane < (unsigned)POOL) ? B1OFF + (8 * S1 + old + lane) * ITEMB : END;
        filled += need;
        if (done) break;
      } else __builtin_amdgcn_s_sleep(2);
    }
  } else {
    asm volatile("; STREAM_ENTRY");
    if (w == NW - 1) {
      hint(CT + 1, 10);
      v4u g[24];
      gather<24>(rs, WS_Q + (h * 8 * DH + lane) * 16, tag, g);
      LAS unsigned short* QS = (LAS unsigned short*)(L + L_QS);
#pragma unroll
      for (int k = 0; k < 3; ++k) {
        float s0 = 0.f, s1 = 0.f;
#pragma unroll
        for (int p = 0; p < 8; ++p) { s0 += ubits(g[p * 3 + k].x); s1 += ubits(g[p * 3 + k].z); }
        const unsigned u = pk(s0, s1);
        QS[lane + 64 * k] = (unsigned short)u; QS[DH + lane + 64 * k] = (unsigned short)(u >> 16);
      }
    }
#pragma unroll
    for (int e = 1; e < NEXTRA; ++e) {
      if (w != NW - 1) waitvm<3>();
      ld3_first(ke[e], voff, rk, so0 + (e + 1) * NS * ITEMB);
      if (w != NW - 1) waitvm<3>();
      ld3_first(ve[e], voff, rv, so0 + (e + 1) * NS * ITEMB);
    }
    BARRIER();
    v4u q[3];
#pragma unroll
    for (int jj = 0; jj < 3; ++jj) q[jj] = *(const LAS v4u*)(L + L_QS + ((lane & 7) + 8 * jj) * 16);
    Soft st;
    st.m = M_INIT; st.l = 0.f;
#pragma unroll
    for (int k = 0; k < 24; ++k) st.acc[k] = 0.f;
    auto step = [&](unsigned nxt) {
      wait3<3>(km);
      float s = score(km, q);
      asm volatile("" : "+v"(s));
      ld3_again(km, voff, rk, nxt);
      const float p = soft(st, s);
      wait3<3>(vm);
      pv(st, p, vm);
      __builtin_amdgcn_sched_barrier(0);
      ld3_again(vm, voff, rv, nxt);
    };
#pragma unroll
    for (int e = 0; e < NEXTRA; ++e) {
      step(so0 + (NEXTRA + 1 + e) * NS * ITEMB);
      pin3(ke[e]); pin3(ve[e]);
      const float pe = soft(st, score(ke[e], q));
      pv(st, pe, ve[e]);
    }
    unsigned cur = so0;
#pragma unroll 1
    for (int b = 0; b < 2; ++b) {
      while (cur != END && (cur >= B1OFF) == (b == 1)) {
        unsigned t = 0;
        if (lane == 0 && w <= NACT) t = __hip_atomic_fetch_add((LAS unsigned*)(L + L_CT), 1u, __ATOMIC_RELAXED, __HIP_MEMORY_SCOPE_WORKGROUP);
        t = __builtin_amdgcn_readfirstlane(t);
        if (w > NACT) cur = END;
        else if (t < (unsigned)STAT) cur = (t < (unsigned)S0 ? sb0 : sb1) + t * ITEMB;
        else {
          int spin = 0;
          while ((cur = __builtin_amdgcn_readfirstlane(DYN[t - STAT])) == EMPTY) {
            __builtin_amdgcn_s_sleep(1);
            if (++spin > SPIN_CAP) { cur = END; break; }
          }
        }
        step(cur);
      }
      wait3<0>(km); wait3<0>(vm);
      if (b == 1) {
        unsigned t = 0;
        if (lane == 0) t = __hip_atomic_fetch_add((LAS unsigned*)(L + L_CT) + 4, 1u, __ATOMIC_RELAXED, __HIP_MEMORY_SCOPE_WORKGROUP);
        if (__builtin_amdgcn_readfirstlane(t) == NS - 1) __builtin_amdgcn_raw_buffer_store_b32(tag, rs, lane == 0 ? WS_G + wg * 128 : OOB, 0, 16);
      }
      float M = fmaxf(st.m, dpp<ROR8>(st.m)); M = sw32max(sw16max(M));
      const float sc = __builtin_amdgcn_exp2f(st.m - M);
      float l = st.l * sc; l += dpp<ROR8>(l); l = sw16add(l, l); l = sw32add(l, l);
      float t1[12], t2[6];
#pragma unroll
      for (int k = 0; k < 12; ++k) t1[k] = sw16add(st.acc[k] * sc, st.acc[k + 12] * sc);
#pragma unroll
      for (int k = 0; k < 6; ++k) { t2[k] = sw32add(t1[k], t1[k + 6]); t2[k] += dpp<ROR8>(t2[k]); }
      LAS float* rec = (LAS float*)(L + L_REC) + ((w - 1) * 2 + b) * RECF;
      if (!(lane & 8)) {
        const int a0 = 6 * hf + 12 * ((lane >> 4) & 1);
#pragma unroll
        for (int k = 0; k < 6; k += 2) { const int ai = a0 + k; *(LAS v2f*)(rec + 4 + ((lane & 7) + 8 * (ai >> 3)) * 8 + (ai & 7)) = v2f{t2[k], t2[k + 1]}; }
      }
      if (lane == 0) *(LAS v2f*)rec = v2f{M, l};
#pragma unroll
      for (int jj = 0; jj < 3; ++jj) q[jj] = *(const LAS v4u*)(L + L_QS + DH * 2 + ((lane & 7) + 8 * jj) * 16);
      st.m = M_INIT; st.l = 0.f;
#pragma unroll
      for (int k = 0; k < 24; ++k) st.acc[k] = 0.f;
    }
  }
  const int g4 = lane >> 4, c16 = lane & 15, tq = c16 >> 2, tp = lane & 3;
  const v4f ctag = {0.f, ubits(tag), 0.f, ubits(tag)};
  const v4u zero4 = {0u, 0u, 0u, 0u};
  BARRIER();
  const bool tilew = w == 0 || w >= 4;
  const int tile = w ? w - 3 : 0;
  unsigned xv0 = 0, xv1 = 0;
  v4u bw[6], bg[3];
  auto gu_frags = [&]() {
    landed(CT + 3);
#pragma unroll
    for (int k = 0; k < 3; ++k) {
      const int row = 32 * k + 8 * g4 + tq;
      bg[k] = bfrag<128>(L + L_G + (tp >> 1) * 10240 + (row < 80 ? row : tq) * 128 + (8 * w + 4 * (tp & 1)) * 2);
    }
  };
  if (!tilew) {
    const int dim = tid - 64;
    {
      const LAS float* rec = (const LAS float*)(L + L_REC);
      v2f ml = *(const LAS v2f*)(rec + (c16 < 2 * NS ? c16 : 0) * RECF);
      float av[2 * NS];
#pragma unroll
      for (int r = 0; r < 2 * NS; ++r) av[r] = rec[r * RECF + 4 + dim];
      if (c16 >= 2 * NS) ml = v2f{M_INIT, 0.f};
      float M = fmaxf(ml.x, dpp<XOR2>(ml.x)); M = fmaxf(M, dpp<ROR4>(M)); M = fmaxf(M, dpp<ROR8>(M));
      const float e = __builtin_amdgcn_exp2f(ml.x - M);
      float ls = e * ml.y; ls += dpp<XOR2>(ls); ls += dpp<ROR4>(ls); ls += dpp<ROR8>(ls);
      float o0 = 0.f, o1 = 0.f;
#pragma unroll
      for (int r = 0; r < 2 * NS; r += 2) { o0 = fmaf(rdlane(e, r), av[r], o0); o1 = fmaf(rdlane(e, r + 1), av[r + 1], o1); }
      put(rs, WS_P + wg * PBLK + dim * 16, fbits(o0), fbits(o1), tag);
      const float lsx = dpp<XOR1>(ls), Mx = dpp<XOR1>(M);
      put(rs, dim < 2 ? WS_P + wg * PBLK + (DH + dim) * 16 : OOB, fbits((lane & 1) ? lsx : M), fbits((lane & 1) ? ls : Mx), tag);
    }
    v4u g[8], gm = zero4;
#pragma unroll
    for (int p = 0; p < 8; ++p) g[p] = zero4;
    const int mp = (lane >> 1) & 7, po = WS_P + h * 8 * PBLK + dim * 16, pm = WS_P + (h * 8 + mp) * PBLK + (DH + (lane & 1)) * 16;
    for (unsigned sent = 0, spin = 0; sent != 0xffu && spin < 4 * SPIN_CAP; ++spin) {
      const unsigned want = __builtin_amdgcn_readfirstlane(CT[5]) & ~sent & 0xffu;
#pragma unroll
      for (int p = 0; p < 8; ++p)
        if ((want >> p) & 1u) g[p] = __builtin_amdgcn_raw_buffer_load_b128(rs, po, p * PBLK, 16);
      if ((want >> mp) & 1u) gm = __builtin_amdgcn_raw_buffer_load_b128(rs, pm, 0, 16);
      sent |= want;
    }
    for (int spin = 0; spin < SPIN_CAP; ++spin) {
      bool ok = true;
#pragma unroll
      for (int p = 0; p < 8; ++p)
        if (!__all(g[p].y == tag && g[p].w == tag)) { g[p] = __builtin_amdgcn_raw_buffer_load_b128(rs, po, p * PBLK, 16); ok = false; }
      if (!__all(gm.y == tag && gm.w == tag)) { gm = __builtin_amdgcn_raw_buffer_load_b128(rs, pm, 0, 16); ok = false; }
      if (ok) break;
      asm volatile("" ::: "memory");
    }
    float M0 = ubits(gm.x), M1 = ubits(gm.z);
    M0 = fmaxf(M0, dpp<XOR2>(M0)); M1 = fmaxf(M1, dpp<XOR2>(M1)); M0 = fmaxf(M0, dpp<ROR4>(M0)); M1 = fmaxf(M1, dpp<ROR4>(M1)); M0 = fmaxf(M0, dpp<ROR8>(M0)); M1 = fmaxf(M1, dpp<ROR8>(M1));
    const float e0 = __builtin_amdgcn_exp2f(ubits(gm.x) - M0), e1 = __builtin_amdgcn_exp2f(ubits(gm.z) - M1);
    float l0 = e0 * dpp<XOR1>(ubits(gm.x)), l1 = e1 * dpp<XOR1>(ubits(gm.z));
    l0 += dpp<XOR2>(l0); l1 += dpp<XOR2>(l1); l0 += dpp<ROR4>(l0); l1 += dpp<ROR4>(l1); l0 += dpp<ROR8>(l0); l1 += dpp<ROR8>(l1);
    float o0 = 0.f, o1 = 0.f;
#pragma unroll
    for (int p = 0; p < 8; ++p) { o0 = fmaf(rdlane(e0, 2 * p), ubits(g[p].x), o0); o1 = fmaf(rdlane(e1, 2 * p), ubits(g[p].z), o1); }
    const unsigned ob = pk(o0 * __builtin_amdgcn_rcpf(rdlane(l0, 0)), o1 * __builtin_amdgcn_rcpf(rdlane(l1, 0)));
    ((LAS unsigned short*)(L + L_O))[dim] = (unsigned short)ob; ((LAS unsigned short*)(L + L_O))[DH + dim] = (unsigned short)(ob >> 16);
  } else {
    if (w == 0) load_weights();
    xv0 = a.x[80 * j + 16 * tile + c16]; xv1 = a.x[DM + 80 * j + 16 * tile + c16];
    if (w == 0) { if (ffn) waitvm<30>(); else waitvm<0>(); if (lane == 0) CT[2] = 1u; }
    if (w == NW - 1) watch<8>(rs, WS_G + h * 8 * 128, 128, lane, tag, CT + 5);
    landed(CT + 2);
#pragma unroll
    for (int k = 0; k < 6; ++k) bw[k] = bfrag<160>(L + L_WO + (32 * k + 8 * g4 + tq) * 160 + (16 * tile + 4 * tp) * 2);
  }
  BARRIER();
  if (tilew) {
    const int ybase = WS_Y + (j * 5 + tile) * (NH * 256);
    __builtin_amdgcn_raw_buffer_store_b32(tag, rs, tid == 0 ? WS_D + wg * 128 : OOB, 0, 16);
    v4f d = ctag;
#pragma unroll
    for (int k = 0; k < 6; ++k) {
      const v4u ao = *(const LAS v4u*)(L + L_O + (c16 & 2) * DH + (32 * k + 8 * g4) * 2);
      d = mfma((c16 & 1) ? zero4 : ao, bw[k], d);
    }
    __builtin_amdgcn_raw_buffer_store_b128(__builtin_bit_cast(v4u, d), rs, lane < 16 ? ybase + h * 256 + c16 * 16 : OOB, 0, 16);
    if (ffn) {
      if (w == 0) { waitvm<0>(); if (lane == 0) CT[3] = 1u; }
      gu_frags();
      v4u g[8];
#pragma unroll
      for (int k = 0; k < 8; ++k) g[k] = zero4;
      for (unsigned sent = 0, spin = 0; sent != 0x11111111u && spin < 4 * SPIN_CAP; ++spin) {
        unsigned m = __builtin_amdgcn_readfirstlane(CT[6]);
        m &= m >> 1; m &= m >> 2; m &= 0x11111111u & ~sent;
#pragma unroll
        for (int k = 0; k < 8; ++k)
          if ((m >> (4 * k)) & 1u) g[k] = __builtin_amdgcn_raw_buffer_load_b128(rs, ybase + lane * 16 + (k & 3) * 1024, (k >> 2) * 4096, 16);
        sent |= m;
      }
      for (int spin = 0; spin < SPIN_CAP; ++spin) {
        bool ok = true;
#pragma unroll
        for (int k = 0; k < 8; ++k)
          if (!__all(g[k].y == tag && g[k].w == tag)) { g[k] = __builtin_amdgcn_raw_buffer_load_b128(rs, ybase + lane * 16 + (k & 3) * 1024, (k >> 2) * 4096, 16); ok = false; }
        if (ok) break;
        asm volatile("" ::: "memory");
      }
      const float s0 = ((ubits(g[0].x) + ubits(g[1].x)) + (ubits(g[2].x) + ubits(g[3].x))) + ((ubits(g[4].x) + ubits(g[5].x)) + (ubits(g[6].x) + ubits(g[7].x)));
      const float s1 = ((ubits(g[0].z) + ubits(g[1].z)) + (ubits(g[2].z) + ubits(g[3].z))) + ((ubits(g[4].z) + ubits(g[5].z)) + (ubits(g[6].z) + ubits(g[7].z)));
      float s = sw16add(s0, s1); s = sw32add(s, s);
      const float hv = bflo((g4 & 1) ? xv1 : xv0) + bfr(s);
      if (lane < 32) ((LAS unsigned short*)(L + L_HS))[g4 * 96 + 16 * tile + c16] = (unsigned short)pk(hv, 0.f);
    }
  } else if (ffn) {
    if (w == 1) watch<32>(rs, WS_D + j * 128, 8 * 128, lane, tag, CT + 6);
    gu_frags();
  }
  if (ffn) {
    int eo = WS_E + (cg * 8 + w) * 5120;
    const int eo0 = lane < 32 ? eo + j * 512 + lane * 16 : OOB, eo1 = lane < 8 ? eo + 4096 + j * 128 + lane * 16 : OOB;
    BARRIER();
    {
      v4f d = ctag, ds = ctag;
#pragma unroll
      for (int k = 0; k < 3; ++k) {
        v4u ah = *(const LAS v4u*)(L + L_HS + (c16 < 5 ? c16 >> 1 : 0) * 192 + (32 * k + 8 * g4) * 2);
        if ((c16 & 1) || c16 > 4) ah = zero4;
        d = mfma(ah, bg[k], d);
        ds = mfma(ah, ah, ds);
      }
      __builtin_amdgcn_raw_buffer_store_b128(__builtin_bit_cast(v4u, d), rs, eo0, 0, 16);
      __builtin_amdgcn_raw_buffer_store_b128(__builtin_bit_cast(v4u, ds), rs, eo1, 0, 16);
    }
    if (w >= 1 && w <= 4) {
      v4u g[5], bdn[3]; int zo[3];
#pragma unroll
      for (int T = 0; T < 3; ++T) {
        const int dt = w - 1 + 4 * T, n = 64 * dt + lane;
        bdn[T] = bfrag<1280>(L + L_WD + tq * 1280 + (64 * (dt < 10 ? dt : 0) + 16 * g4 + 4 * tp) * 2);
        zo[T] = dt < 10 ? WS_Z + ((n >> 2) * NFFN + f) * 64 + (n & 3) * 16 : OOB;
        asm volatile("" : "+v"(zo[T]));
      }
      __builtin_amdgcn_s_sleep(10);
      gather<5>(rs, WS_E + f * 5120 + lane * 16, tag, g);
      __builtin_amdgcn_raw_buffer_store_b32(tag, rs, (tid == 64 && j == 0) ? WS_F + cg * 128 : OOB, 0, 16);
      const float s0 = (ubits(g[0].x) + ubits(g[1].x)) + (ubits(g[2].x) + ubits(g[3].x)), s1 = (ubits(g[0].z) + ubits(g[1].z)) + (ubits(g[2].z) + ubits(g[3].z));
      const float gs = sw32add(s0, s1);
      const float cs0 = sw32add(s0, s0);
      const auto cx = __builtin_amdgcn_permlane16_swap(fbits(cs0), fbits(cs0), false, false);
      const auto gx = __builtin_amdgcn_permlane16_swap(fbits(gs), fbits(gs), false, false);
      float t0 = ubits(g[4].x), t1 = ubits(g[4].z);
      t0 += dpp<ROR8>(t0); t1 += dpp<ROR8>(t1);
      t0 = sw16add(t0, t0); t1 = sw16add(t1, t1);
      const float tt = sw32add(t0, t1) * (1.0f / DM);
      const float mean = dpp<BC + 4>(tt), q0 = dpp<BC + 0>(tt), q1 = dpp<BC + 2>(tt);
      const float rdv = ln_rden(fmaxf((hf ? q1 : q0) - mean * mean, 0.f));
      const float gv = bfr((ubits(gx[0]) - bfr(mean) * ubits(cx[1])) * rdv), uv = dpp<ROR8>(gv);
      const float y2 = (-2.0f * 0.7978845608028654f * 1.4426950408889634f) * (gv + 0.044715f * gv * gv * gv);
      const float act = bfr(gv * __builtin_amdgcn_rcpf(1.0f + __builtin_amdgcn_exp2f(y2))) * uv;
      const unsigned ap = pk(act, dpp<XOR1>(act));
      const auto ax = __builtin_amdgcn_permlane32_swap(ap, ap, false, false);
      const unsigned keep = ((c16 >> 2) == g4 && !(c16 & 1)) ? 0xffffffffu : 0u, k1 = (c16 & 2) ? keep : 0u, k0 = keep ^ k1;
      const v4u b0 = {dppu<BC + 0>(ax[0]), dppu<BC + 2>(ax[0]), dppu<BC + 4>(ax[0]), dppu<BC + 6>(ax[0])};
      const v4u b1 = {dppu<BC + 0>(ax[1]), dppu<BC + 2>(ax[1]), dppu<BC + 4>(ax[1]), dppu<BC + 6>(ax[1])};
      const v4u a2 = (b0 & k0) | (b1 & k1);
#pragma unroll
      for (int T = 0; T < 3; ++T) __builtin_amdgcn_raw_buffer_store_b128(__builtin_bit_cast(v4u, mfma(a2, bdn[T], ctag)), rs, zo[T], 0, 16);
    } else if (w == NW - 1 && cg < 20) {
      const __amdgpu_buffer_rsrc_t ro = __builtin_amdgcn_make_buffer_rsrc((void*)a.out, 0, 2 * DM * 2, 0x00020000);
      const float hv = bflo(((const LAS unsigned short*)(L + L_HS))[(g4 & 1) * 96 + 4 * cg + (lane & 3)]);
      v4u g[12];
      watch<24>(rs, WS_F, 128, lane, tag, CT + 7);
      gather<12>(rs, WS_Z + (20 * j + cg) * (NFFN * 64) + lane * 16, tag, g);
      float s0[4], s1[4];
#pragma unroll
      for (int k = 0; k < 4; ++k) { s0[k] = (ubits(g[k].x) + ubits(g[k + 4].x)) + ubits(g[k + 8].x); s1[k] = (ubits(g[k].z) + ubits(g[k + 4].z)) + ubits(g[k + 8].z); }
      float s = sw16add((s0[0] + s0[1]) + (s0[2] + s0[3]), (s1[0] + s1[1]) + (s1[2] + s1[3]));
      s = sw32add(s, s); s += dpp<ROR4>(s); s += dpp<ROR8>(s);
      const unsigned o = pk(hv + bfr(s), 0.f);
      __builtin_amdgcn_raw_buffer_store_b16((unsigned short)o, ro, (lane < 32 && c16 < 4) ? (g4 * DM + 80 * j + 4 * cg + c16) * 2 : OOB, 0, 0);
      __builtin_amdgcn_raw_buffer_store_b32(tag, rs, (wg == 0 && lane == 0) ? 0 : OOB, 0, 16);
    }
  }
}
}

extern "C" void kernel_launch(void* const* d_in, const int* in_sizes, int n_in, void* d_out, int out_size, void* d_ws, size_t ws_size, hipStream_t stream) {
  Args a;
  a.x = (const unsigned short*)d_in[0]; a.k = (const unsigned short*)d_in[1]; a.v = (const unsigned short*)d_in[2];
  a.wq = (const unsigned short*)d_in[3]; a.wo = (const unsigned short*)d_in[4];
  a.wg = (const unsigned short*)d_in[6]; a.wu = (const unsigned short*)d_in[7]; a.wd = (const unsigned short*)d_in[8];
  a.out = (unsigned short*)d_out; a.ws = (unsigned char*)d_ws;
  hipLaunchKernelGGL(decode_block, dim3(NWG), dim3(NTHR), 0, stream, a);
}
```

```cpp
#include <hip/hip_runtime.h>
#include <hip/hip_bf16.h>
#include <stdint.h>

namespace {
typedef unsigned v4u __attribute__((ext_vector_type(4)));
typedef unsigned v2u __attribute__((ext_vector_type(2)));
typedef float v4f __attribute__((ext_vector_type(4)));
typedef float v2f __attribute__((ext_vector_type(2)));
typedef short v4s __attribute__((ext_vector_type(4)));
typedef __bf16 bf2 __attribute__((ext_vector_type(2)));
typedef __bf16 bf8 __attribute__((ext_vector_type(8)));
#define LAS __attribute__((address_space(3)))
#define CAS __attribute__((address_space(4)))

constexpr int DM = 640, NH = 32, SK = 16384, DH = 192, FF = 1536;
constexpr int NWG = 256, NW = 8, NS = NW - 1, NTHR = NW * 64;
constexpr unsigned ITEMB = 8 * DH * 2;
constexpr unsigned HEADB = (unsigned)SK * DH * 2, B1OFF = NH * HEADB;
constexpr int IPH = SK / 8;
constexpr int S0 = IPH / 8, S1 = 192, STAT = S0 + S1;
constexpr int POOL = IPH - 8 * S1, LEAD = 8;
constexpr int NEXTRA = 4, NACT = 5;
constexpr unsigned END = 0xF0000000u, EMPTY = 0xFFFFFFFFu;
constexpr int NRED = DM / 4, NFFN = FF / 8;
constexpr int SPIN_CAP = 1 << 21;
constexpr float M_INIT = -3.0e38f;

constexpr int WS_CTR = 4096, CTR_STRIDE = 4352;
constexpr int WS_F = 0x24000, WS_D = 0x28000, WS_G = 0x30000, WS_QB = 0x38000, WS_Q = 0x50000, WS_P = 0x150000, PBLK = 3200, WS_Y = 0x220000, WS_E = 0x280000, WS_Z = 0x400000, WS_BYTES = 0x600000;
constexpr int OOB = (int)0x80000000u;
static_assert(WS_CTR + NH * CTR_STRIDE <= WS_F && WS_F + 24 * 128 <= WS_D && WS_D + NWG * 128 <= WS_G && WS_G + NWG * 128 <= WS_QB && WS_QB + NWG * 256 <= WS_Q && WS_Q + NWG * 4096 <= WS_P && WS_P + NWG * PBLK <= WS_Y, "ws map");
static_assert(WS_Y + NWG * 80 * 16 <= WS_E && WS_E + NFFN * 5120 <= WS_Z && WS_Z + NRED * NFFN * 64 <= WS_BYTES, "ws map");

constexpr int L_WO = 0, L_G = L_WO + DH * 160, L_WD = L_G + 2 * 80 * 128, L_QP = L_WD + 8 * 1280;
constexpr int L_QS = L_QP + NW * 2 * DH * 4, RECF = DH + 4, L_REC = L_QS + 2 * DH * 2, L_O = L_REC + NS * 2 * RECF * 4, L_HS = L_O + 2 * DH * 2;
constexpr int L_CT = L_HS + 3 * 192, DYNMAX = POOL + 96, L_DYN = L_CT + 64, L_BYTES = L_DYN + DYNMAX * 4;
static_assert(L_BYTES > 81920 && L_BYTES <= 160 * 1024 && L_REC % 16 == 0 && L_O % 16 == 0 && L_HS % 16 == 0, "one workgroup per CU");

struct Args {
  const unsigned short *x, *k, *v, *wq, *wo, *wg, *wu, *wd;
  unsigned short* out;
  unsigned char* ws;
};

__device__ __forceinline__ float dot2(unsigned a, unsigned b, float c) {
  return __builtin_amdgcn_fdot2_f32_bf16(__builtin_bit_cast(bf2, a), __builtin_bit_cast(bf2, b), c, false);
}
template <int CTRL> __device__ __forceinline__ float dpp(float x) {
  return __builtin_bit_cast(float, __builtin_amdgcn_mov_dpp(__builtin_bit_cast(int, x), CTRL, 0xf, 0xf, true));
}
template <int CTRL> __device__ __forceinline__ unsigned dppu(unsigned x) { return (unsigned)__builtin_amdgcn_mov_dpp((int)x, CTRL, 0xf, 0xf, true); }
constexpr int BC = 0x150;
constexpr int XOR1 = 0xB1, XOR2 = 0x4E, HMIR = 0x141, ROR4 = 0x124, ROR8 = 0x128;
__device__ __forceinline__ float bflo(unsigned x) { return __builtin_bit_cast(float, x << 16); }
__device__ __forceinline__ float bfhi(unsigned x) { return __builtin_bit_cast(float, x & 0xffff0000u); }
__device__ __forceinline__ unsigned pk(float lo, float hi) {
  const bf2 r = {(__bf16)lo, (__bf16)hi};
  return __builtin_bit_cast(unsigned, r);
}
__device__ __forceinline__ float bfr(float x) { return (float)(__bf16)x; }
__device__ __forceinline__ unsigned fbits(float x) { return __builtin_bit_cast(unsigned, x); }
__device__ __forceinline__ float ubits(unsigned x) { return __builtin_bit_cast(float, x); }
__device__ __forceinline__ float sw16add(float a, float b) {
  const auto s = __builtin_amdgcn_permlane16_swap(fbits(a), fbits(b), false, false);
  return ubits(s[0]) + ubits(s[1]);
}
__device__ __forceinline__ float sw32add(float a, float b) {
  const auto s = __builtin_amdgcn_permlane32_swap(fbits(a), fbits(b), false, false);
  return ubits(s[0]) + ubits(s[1]);
}
__device__ __forceinline__ float sw16max(float a) {
  const auto s = __builtin_amdgcn_permlane16_swap(fbits(a), fbits(a), false, false);
  return fmaxf(ubits(s[0]), ubits(s[1]));
}
__device__ __forceinline__ float sw32max(float a) {
  const auto s = __builtin_amdgcn_permlane32_swap(fbits(a), fbits(a), false, false);
  return fmaxf(ubits(s[0]), ubits(s[1]));
}
__device__ __forceinline__ float rowsum16(float s) { s += dpp<XOR1>(s); s += dpp<XOR2>(s); s += dpp<HMIR>(s); s += dpp<ROR8>(s); return s; }
__device__ __forceinline__ float rdlane(float x, int l) { return ubits(__builtin_amdgcn_readlane(fbits(x), l)); }
__device__ __forceinline__ v4u mkrsrc(const void* p, unsigned bytes) {
  const unsigned long long a = (unsigned long long)p;
  return v4u{(unsigned)a, (unsigned)(a >> 32) & 0xffffu, bytes, 0x00020000u};
}
__device__ __forceinline__ v4f mfma(v4u a, v4u b, v4f c) {
  return __builtin_amdgcn_mfma_f32_16x16x32_bf16(__builtin_bit_cast(bf8, a), __builtin_bit_cast(bf8, b), c, 0, 0, 0);
}
template <int STRIDE> __device__ __forceinline__ v4u bfrag(LAS unsigned char* p) {
  const v2u lo = __builtin_bit_cast(v2u, __builtin_amdgcn_ds_read_tr16_b64_v4i16((LAS v4s*)p));
  const v2u hi = __builtin_bit_cast(v2u, __builtin_amdgcn_ds_read_tr16_b64_v4i16((LAS v4s*)(p + 4 * STRIDE)));
  return v4u{lo.x, lo.y, hi.x, hi.y};
}
#define BARRIER() asm volatile("s_waitcnt lgkmcnt(0)\n\ts_barrier" ::: "memory")

#define LD3 "s_nop 4\n\tbuffer_load_dwordx4 %0, %3, %4, %5 offen nt\n\tbuffer_load_dwordx4 %1, %3, %4, %5 offen offset:128 nt\n\tbuffer_load_dwordx4 %2, %3, %4, %5 offen offset:256 nt"
__device__ __forceinline__ void ld_first(v4u& d, int voff, v4u rs, unsigned soff) {
  asm volatile("s_nop 4\n\tbuffer_load_dwordx4 %0, %1, %2, %3 offen nt" : "=v"(d) : "v"(voff), "s"(rs), "s"(soff));
}
__device__ __forceinline__ void ld3_first(v4u (&r)[3], int voff, v4u rs, unsigned so) { asm volatile(LD3 : "=&v"(r[0]), "=&v"(r[1]), "=&v"(r[2]) : "v"(voff), "s"(rs), "s"(so)); }
__device__ __forceinline__ void ld3_again(v4u (&r)[3], int voff, v4u rs, unsigned so) { asm volatile(LD3 : "+v"(r[0]), "+v"(r[1]), "+v"(r[2]) : "v"(voff), "s"(rs), "s"(so)); }
template <int N> __device__ __forceinline__ void wait3(v4u (&r)[3]) { asm volatile("s_waitcnt vmcnt(%3)" : "+v"(r[0]), "+v"(r[1]), "+v"(r[2]) : "i"(N)); }
__device__ __forceinline__ void pin3(v4u (&r)[3]) { asm volatile("" : "+v"(r[0]), "+v"(r[1]), "+v"(r[2])); }
template <int N> __device__ __forceinline__ void waitvm() { asm volatile("s_waitcnt vmcnt(%0)" : : "i"(N) : "memory"); }

__device__ __forceinline__ void dma16(const void* base, int voff, unsigned lds) {
  unsigned keep;
  asm volatile("s_mov_b32 %0, m0\n\ts_mov_b32 m0, %3\n\ts_nop 2\n\tglobal_load_lds_dwordx4 %1, %2 nt\n\ts_mov_b32 m0, %0" : "=&s"(keep) : "v"(voff), "s"(base), "s"(lds) : "memory");
}

__device__ __forceinline__ void put(__amdgpu_buffer_rsrc_t rs, int off, unsigned a, unsigned b, unsigned tag) {
  __builtin_amdgcn_raw_buffer_store_b128(v4u{a, tag, b, tag}, rs, off, 0, 16);
}
template <int N> __device__ __forceinline__ void gather(__amdgpu_buffer_rsrc_t rs, int off, unsigned tag, v4u (&t)[N]) {
  bool done = false;
#pragma unroll
  for (int k = 0; k < N; ++k) t[k] = v4u{0u, 0u, 0u, 0u};
  for (int spin = 0; spin < SPIN_CAP; ++spin) {
    if (!done) {
#pragma unroll
      for (int k = 0; k < N; ++k) t[k] = __builtin_amdgcn_raw_buffer_load_b128(rs, off + (k & 3) * 1024, (k >> 2) * 4096, 16);
      unsigned mn = tag, mx = tag;
#pragma unroll
      for (int k = 0; k < N; ++k) { mn = min(mn, min(t[k].y, t[k].w)); mx = max(mx, max(t[k].y, t[k].w)); }
      done = mn == mx;
    }
    if (__all(done)) break;
    asm volatile("" ::: "memory");
    __builtin_amdgcn_s_sleep(1);
  }
}

template <int N> __device__ __forceinline__ void watch(__amdgpu_buffer_rsrc_t rs, int off, int stride, int lane, unsigned tag, volatile LAS unsigned* mask) {
  bool seen = lane >= N;
  for (int spin = 0; spin < SPIN_CAP; ++spin) {
    if (!seen) seen = __builtin_amdgcn_raw_buffer_load_b32(rs, off + lane * stride, 0, 16) == tag;
    const unsigned long long m = __ballot(seen);
    if (lane == 0) *mask = (unsigned)m;
    if (m == ~0ull) break;
    asm volatile("" ::: "memory");
    __builtin_amdgcn_s_sleep(1);
  }
  if (lane == 0) *mask = ~0u;
}
#define landed(p) do { for (int sp_ = 0; sp_ < 16 * SPIN_CAP && *(p) == 0u; ++sp_) __builtin_amdgcn_s_sleep(1); asm volatile("" ::: "memory"); } while (0)
#define hint(p, D) do { for (int sp_ = 0; sp_ < SPIN_CAP && *(p) == 0u; ++sp_) __builtin_amdgcn_s_sleep(1); __builtin_amdgcn_s_sleep(D); } while (0)

__device__ __forceinline__ float ln_rden(float var) { return __builtin_amdgcn_rcpf(bfr(__builtin_amdgcn_sqrtf(bfr(bfr(var) + 1e-5f)))); }
__device__ __forceinline__ float ln_val(float x, float mean_b, float rden) { return bfr(bfr(x - mean_b) * rden); }

struct Soft { float m, l, acc[24]; };
__device__ __forceinline__ float score(const v4u (&kk)[3], const v4u (&q)[3]) {
  float s0 = 0.f, s1 = 0.f;
#pragma unroll
  for (int j = 0; j < 3; ++j) {
    s0 = dot2(kk[j].x, q[j].x, s0); s1 = dot2(kk[j].y, q[j].y, s1);
    s0 = dot2(kk[j].z, q[j].z, s0); s1 = dot2(kk[j].w, q[j].w, s1);
  }
  return s0 + s1;
}
__device__ __forceinline__ float soft(Soft& st, float s) {
  s += dpp<XOR1>(s); s += dpp<XOR2>(s); s += dpp<HMIR>(s);
  s = bfr(bfr(s) * 0.07216878364870322f) * 1.4426950408889634f;
  if (__any(s > st.m)) {
    const float mn = fmaxf(st.m, s), al = __builtin_amdgcn_exp2f(st.m - mn);
    st.l *= al;
#pragma unroll
    for (int k = 0; k < 24; ++k) st.acc[k] *= al;
    st.m = mn;
  }
  const float p = __builtin_amdgcn_exp2f(s - st.m);
  st.l += p;
  return p;
}
__device__ __forceinline__ void pv(Soft& st, float p, const v4u (&vv)[3]) {
#pragma unroll
  for (int j = 0; j < 3; ++j)
#pragma unroll
    for (int e = 0; e < 4; ++e) {
      st.acc[j * 8 + 2 * e] = fmaf(p, bflo(vv[j][e]), st.acc[j * 8 + 2 * e]);
      st.acc[j * 8 + 2 * e + 1] = fmaf(p, bfhi(vv[j][e]), st.acc[j * 8 + 2 * e + 1]);
    }
}

__global__ void __launch_bounds__(NTHR, 2) decode_block(Args a) {
  __shared__ __attribute__((aligned(16))) unsigned char smem[L_BYTES];
  LAS unsigned char* L = (LAS unsigned char*)smem;
  volatile LAS unsigned* CT = (volatile LAS unsigned*)(L + L_CT);
  volatile LAS unsigned* DYN = (volatile LAS unsigned*)(L + L_DYN);
  const int tid = threadIdx.x, lane = tid & 63, w = __builtin_amdgcn_readfirstlane(tid >> 6);
  const int wg = blockIdx.x, h = wg >> 3, j = wg & 7;
  const int hf = lane >> 5, l5 = lane & 31;
  const bool ffn = ((h + j) & 3) != 3;
  const int cg = 3 * (h >> 2) + (h & 3) - ((h & 3) > ((3 - j) & 3) ? 1 : 0), f = 8 * cg + j;
  const __amdgpu_buffer_rsrc_t rs = __builtin_amdgcn_make_buffer_rsrc((void*)a.ws, 0, WS_BYTES, 0x00020000);
  unsigned* ctr = (unsigned*)(a.ws + WS_CTR + h * CTR_STRIDE);

  const unsigned lb = (unsigned)(uintptr_t)L;
  auto load_wo = [&]() {
    const unsigned char* wo = (const unsigned char*)a.wo + (size_t)h * DH * (DM * 2) + j * 160;
    int vo = (lane / 10) * (DM * 2) + (lane % 10) * 16, c10 = lane % 10;
#pragma unroll 2
    for (int k = 0; k < 30; ++k) {
      dma16(wo, vo, lb + L_WO + k * 1024);
      c10 += 4; vo += 6 * (DM * 2) + 64;
      if (c10 >= 10) { c10 -= 10; vo += DM * 2 - 160; }
    }
  };
  auto load_ffn = [&]() {
    const size_t o0 = (size_t)80 * j * (FF * 2) + cg * 128;
    const int vg = (lane >> 3) * (FF * 2) + (lane & 7) * 16;
#pragma unroll 1
    for (int k = 0; k < 10; ++k) {
      dma16((const unsigned char*)a.wg + o0 + k * 8 * FF * 2, vg, lb + L_G + k * 1024);
      dma16((const unsigned char*)a.wu + o0 + k * 8 * FF * 2, vg, lb + L_G + 10240 + k * 1024);
      dma16((const unsigned char*)a.wd + (size_t)f * 8 * (DM * 2) + k * 1024, lane * 16, lb + L_WD + k * 1024);
    }
  };
  unsigned epoch;
  {
    const int zero = 0;
    if (w == 0) {
      __builtin_amdgcn_s_setprio(3);
      if (j == 0) asm volatile("s_nop 4\n\tglobal_store_dword %0, %0, %1 sc1" : : "v"(zero), "s"(ctr) : "memory");
    }
    asm volatile("s_nop 4\n\tglobal_load_dword %0, %1, %2 sc1" : "=v"(epoch) : "v"(zero), "s"(a.ws));
  }
  v4u wqr[5], xa, xb; v2u xc;
  {
    const size_t rowoff = (size_t)(80 * j + 10 * w) * (NH * DH * 2) + h * (DH * 2);
    const v4u rw = mkrsrc((const unsigned char*)a.wq + rowoff, (unsigned)((size_t)DM * NH * DH * 2 - rowoff));
    const int vo = l5 < 24 ? hf * (NH * DH * 2) + l5 * 16 : OOB;
#pragma unroll
    for (int i = 0; i < 5; ++i) ld_first(wqr[i], vo, rw, i * 2 * NH * DH * 2);
    const v4u rx = mkrsrc(a.x, 2 * DM * 2);
    const int xo = hf * (DM * 2) + l5 * 40;
    asm volatile("s_nop 4\n\tbuffer_load_dwordx4 %0, %3, %4, 0 offen\n\tbuffer_load_dwordx4 %1, %3, %4, 0 offen offset:16\n\tbuffer_load_dwordx2 %2, %3, %4, 0 offen offset:32"
                 : "=&v"(xa), "=&v"(xb), "=&v"(xc) : "v"(xo), "s"(rx));
  }
  const v4u rk = mkrsrc((const unsigned char*)a.k + (size_t)h * HEADB, B1OFF + HEADB), rv = mkrsrc((const unsigned char*)a.v + (size_t)h * HEADB, B1OFF + HEADB);
  const int voff = (lane >> 3) * (DH * 2) + (lane & 7) * 16;
  const unsigned sb0 = (unsigned)(S0 * j) * ITEMB, sb1 = B1OFF + (unsigned)(S1 * j - S0) * ITEMB;
  v4u km[3], vm[3], ke[NEXTRA][3], ve[NEXTRA][3];
  const unsigned so0 = w ? sb0 + (unsigned)(w - 1) * ITEMB : END;
  asm volatile("s_barrier" ::: "memory");
  ld3_first(km, voff, rk, so0); ld3_first(vm, voff, rv, so0);
  if (w == 0) {
    for (int k = lane; k < DYNMAX; k += 64) DYN[k] = EMPTY;
    if (lane < 48) {
      ((LAS unsigned*)(L + L_HS))[lane] = 0u; ((LAS unsigned*)(L + L_HS))[48 + lane] = 0u; ((LAS unsigned*)(L + L_HS))[96 + lane] = lane < 40 ? 0x3f803f80u : 0u;
    }
    if (lane < 8) CT[lane] = lane ? 0u : (2 * NEXTRA + 1) * NS;
  }
  asm volatile("s_waitcnt vmcnt(6)" : "+v"(wqr[0]), "+v"(wqr[1]), "+v"(wqr[2]), "+v"(wqr[3]), "+v"(wqr[4]), "+v"(xa), "+v"(xb), "+v"(xc), "+v"(epoch));
  ld3_first(ke[0], voff, rk, so0 + NS * ITEMB); ld3_first(ve[0], voff, rv, so0 + NS * ITEMB);
  const unsigned tag = ((__builtin_amdgcn_readfirstlane(epoch) + 0x1E3779B9u) & 0x1FFFFFFFu) | 0x20000000u;
  {
    const unsigned xw[10] = {xa.x, xa.y, xa.z, xa.w, xb.x, xb.y, xb.z, xb.w, xc.x, xc.y};
    float s = 0.f, sq = 0.f;
#pragma unroll
    for (int k = 0; k < 10; ++k) { s = dot2(xw[k], 0x3f803f80u, s); sq = dot2(xw[k], xw[k], sq); }
    s = rowsum16(sw16add(s, sq)) * (1.0f / DM);
    const auto sx = __builtin_amdgcn_permlane16_swap(fbits(s), fbits(s), false, false);
    const float mean = ubits(sx[0]), mb = bfr(mean), rd = ln_rden(fmaxf(ubits(sx[1]) - mean * mean, 0.f));
    const float mb0 = rdlane(mb, 0), mb1 = rdlane(mb, 32), rd0 = rdlane(rd, 0), rd1 = rdlane(rd, 32);
    const CAS unsigned* x32 = (const CAS unsigned*)a.x + 40 * j + 5 * w;
    float qa[8], qb[8];
#pragma unroll
    for (int e = 0; e < 8; ++e) { qa[e] = 0.f; qb[e] = 0.f; }
#pragma unroll
    for (int i = 0; i < 5; ++i) {
      const unsigned u0 = x32[i], u1 = x32[DM / 2 + i];
      const float n0 = ln_val(hf ? bfhi(u0) : bflo(u0), mb0, rd0), n1 = ln_val(hf ? bfhi(u1) : bflo(u1), mb1, rd1);
#pragma unroll
      for (int e = 0; e < 4; ++e) {
        const float w0 = bflo(wqr[i][e]), w1 = bfhi(wqr[i][e]);
        qa[2 * e] = fmaf(n0, w0, qa[2 * e]); qa[2 * e + 1] = fmaf(n0, w1, qa[2 * e + 1]);
        qb[2 * e] = fmaf(n1, w0, qb[2 * e]); qb[2 * e + 1] = fmaf(n1, w1, qb[2 * e + 1]);
      }
    }
    float r[8];
#pragma unroll
    for (int e = 0; e < 8; ++e) r[e] = sw32add(qa[e], qb[e]);
    if (l5 < 24) {
      LAS v4f* dst = (LAS v4f*)(L + L_QP + ((w * 2 + hf) * DH + l5 * 8) * 4);
      dst[0] = v4f{r[0], r[1], r[2], r[3]}; dst[1] = v4f{r[4], r[5], r[6], r[7]};
    }
  }
  if (w == 0) asm volatile("s_waitcnt vmcnt(0) ; HELPER_ONLY" ::: "memory");
  BARRIER();

  if (w == 0) {
    asm volatile("; HELPER_ENTRY");
    {
      const LAS float* QP = (const LAS float*)(L + L_QP) + 24 * hf + (l5 < 24 ? l5 : 0);
#pragma unroll
      for (int k = 0; k < 4; ++k) {
        float s0 = 0.f, s1 = 0.f;
#pragma unroll
        for (int ww = 0; ww < NW; ++ww) { s0 += QP[(ww * 2) * DH + 48 * k]; s1 += QP[(ww * 2 + 1) * DH + 48 * k]; }
        put(rs, l5 < 24 ? WS_Q + ((h * 8 + 2 * k + hf) * 8 + j) * 512 + l5 * 16 : OOB, fbits(s0), fbits(s1), tag);
      }
      if (lane == 0) CT[1] = 1u;
    }
    BARRIER();
    int filled = 0;
    for (int spin = 0; spin < 16 * SPIN_CAP; ++spin) {
      int need = (int)__builtin_amdgcn_readfirstlane(CT[0]) + LEAD - (STAT + filled);
      if (need > 0) {
        need = need > 32 ? 32 : need;
        unsigned old = 0;
        if (lane == 0) old = __hip_atomic_fetch_add(ctr, (unsigned)need, __ATOMIC_RELAXED, __HIP_MEMORY_SCOPE_AGENT);
        old = __builtin_amdgcn_readfirstlane(old);
        const bool done = old + need >= (unsigned)POOL;
        if (lane < (done ? need + NW : need)) DYN[filled + lane] = (lane < need && old + lane < (unsigned)POOL) ? B1OFF + (8 * S1 + old + lane) * ITEMB : END;
        filled += need;
        if (done) break;
      } else __builtin_amdgcn_s_sleep(2);
    }
  } else {
    asm volatile("; STREAM_ENTRY");
    if (w == NW - 1) {
      hint(CT + 1, 10);
      v4u g[4];
      gather<4>(rs, WS_Q + wg * 4096 + hf * 512 + (l5 < 24 ? l5 : 0) * 16, tag, g);
      const float qv = sw32add((ubits(g[0].x) + ubits(g[1].x)) + (ubits(g[2].x) + ubits(g[3].x)), (ubits(g[0].z) + ubits(g[1].z)) + (ubits(g[2].z) + ubits(g[3].z)));
      const unsigned qp = pk(qv, dpp<XOR1>(qv));
      const auto qx = __builtin_amdgcn_permlane32_swap(qp, qp, false, false);
      put(rs, (lane < 24 && !(lane & 1)) ? WS_QB + wg * 256 + lane * 8 : OOB, qx[0], qx[1], tag);
      waitvm<0>();
      __builtin_amdgcn_s_sleep(16);
      v4u gq[2];
      gather<2>(rs, WS_QB + h * 2048 + (lane >> 4) * 256 + ((lane & 15) < 12 ? lane & 15 : 0) * 16, tag, gq);
      if ((lane & 15) < 12) {
        LAS unsigned* QS = (LAS unsigned*)(L + L_QS) + (lane >> 4) * 12 + (lane & 15);
        QS[0] = gq[0].x; QS[DH / 2] = gq[0].z; QS[48] = gq[1].x; QS[DH / 2 + 48] = gq[1].z;
      }
    }
#pragma unroll
    for (int e = 1; e < NEXTRA; ++e) {
      if (w != NW - 1) waitvm<0>();
      ld3_first(ke[e], voff, rk, so0 + (e + 1) * NS * ITEMB);
      if (w != NW - 1) waitvm<0>();
      ld3_first(ve[e], voff, rv, so0 + (e + 1) * NS * ITEMB);
    }
    BARRIER();
    v4u q[3];
#pragma unroll
    for (int jj = 0; jj < 3; ++jj) q[jj] = *(const LAS v4u*)(L + L_QS + ((lane & 7) + 8 * jj) * 16);
    Soft st;
    st.m = M_INIT; st.l = 0.f;
#pragma unroll
    for (int k = 0; k < 24; ++k) st.acc[k] = 0.f;
    auto step = [&](unsigned nxt) {
      wait3<3>(km);
      float s = score(km, q);
      asm volatile("" : "+v"(s));
      ld3_again(km, voff, rk, nxt);
      const float p = soft(st, s);
      wait3<3>(vm);
      pv(st, p, vm);
      __builtin_amdgcn_sched_barrier(0);
      ld3_again(vm, voff, rv, nxt);
    };
#pragma unroll
    for (int e = 0; e < NEXTRA; ++e) {
      step(so0 + (NEXTRA + 1 + e) * NS * ITEMB);
      pin3(ke[e]); pin3(ve[e]);
      const float pe = soft(st, score(ke[e], q));
      pv(st, pe, ve[e]);
    }
    unsigned cur = so0;
#pragma unroll 1
    for (int b = 0; b < 2; ++b) {
      while (cur != END && (cur >= B1OFF) == (b == 1)) {
        unsigned t = 0;
        if (lane == 0 && w <= NACT) t = __hip_atomic_fetch_add((LAS unsigned*)(L + L_CT), 1u, __ATOMIC_RELAXED, __HIP_MEMORY_SCOPE_WORKGROUP);
        t = __builtin_amdgcn_readfirstlane(t);
        if (w > NACT) cur = END;
        else if (t < (unsigned)STAT) cur = (t < (unsigned)S0 ? sb0 : sb1) + t * ITEMB;
        else {
          int spin = 0;
          while ((cur = __builtin_amdgcn_readfirstlane(DYN[t - STAT])) == EMPTY) {
            __builtin_amdgcn_s_sleep(1);
            if (++spin > SPIN_CAP) { cur = END; break; }
          }
        }
        step(cur);
      }
      wait3<0>(km); wait3<0>(vm);
      if (b == 1) {
        unsigned t = 0;
        if (lane == 0) t = __hip_atomic_fetch_add((LAS unsigned*)(L + L_CT) + 4, 1u, __ATOMIC_RELAXED, __HIP_MEMORY_SCOPE_WORKGROUP);
        if (__builtin_amdgcn_readfirstlane(t) == NS - 1) __builtin_amdgcn_raw_buffer_store_b32(tag, rs, lane == 0 ? WS_G + wg * 128 : OOB, 0, 16);
      }
      float M = fmaxf(st.m, dpp<ROR8>(st.m)); M = sw32max(sw16max(M));
      const float sc = __builtin_amdgcn_exp2f(st.m - M);
      float l = st.l * sc; l += dpp<ROR8>(l); l = sw16add(l, l); l = sw32add(l, l);
      float t1[12], t2[6];
#pragma unroll
      for (int k = 0; k < 12; ++k) t1[k] = sw16add(st.acc[k] * sc, st.acc[k + 12] * sc);
#pragma unroll
      for (int k = 0; k < 6; ++k) { t2[k] = sw32add(t1[k], t1[k + 6]); t2[k] += dpp<ROR8>(t2[k]); }
      LAS float* rec = (LAS float*)(L + L_REC) + ((w - 1) * 2 + b) * RECF;
      if (!(lane & 8)) {
        const int a0 = 6 * hf + 12 * ((lane >> 4) & 1);
#pragma unroll
        for (int k = 0; k < 6; k += 2) { const int ai = a0 + k; *(LAS v2f*)(rec + 4 + ((lane & 7) + 8 * (ai >> 3)) * 8 + (ai & 7)) = v2f{t2[k], t2[k + 1]}; }
      }
      if (lane == 0) *(LAS v2f*)rec = v2f{M, l};
#pragma unroll
      for (int jj = 0; jj < 3; ++jj) q[jj] = *(const LAS v4u*)(L + L_QS + DH * 2 + ((lane & 7) + 8 * jj) * 16);
      st.m = M_INIT; st.l = 0.f;
#pragma unroll
      for (int k = 0; k < 24; ++k) st.acc[k] = 0.f;
    }
  }
  const int g4 = lane >> 4, c16 = lane & 15, tq = c16 >> 2, tp = lane & 3;
  const v4f ctag = {0.f, ubits(tag), 0.f, ubits(tag)};
  const v4u zero4 = {0u, 0u, 0u, 0u};
  BARRIER();
  const bool tilew = w == 0 || w >= 4;
  const int tile = w ? w - 3 : 0;
  unsigned xv0 = 0, xv1 = 0;
  v4u bw[6], bg[3];
  auto gu_frags = [&]() {
    landed(CT + 3);
#pragma unroll
    for (int k = 0; k < 3; ++k) {
      const int row = 32 * k + 8 * g4 + tq;
      bg[k] = bfrag<128>(L + L_G + (tp >> 1) * 10240 + (row < 80 ? row : tq) * 128 + (8 * w + 4 * (tp & 1)) * 2);
    }
  };
  if (!tilew) {
    const int dim = tid - 64;
    {
      const LAS float* rec = (const LAS float*)(L + L_REC);
      v2f ml = *(const LAS v2f*)(rec + (c16 < 2 * NS ? c16 : 0) * RECF);
      float av[2 * NS];
#pragma unroll
      for (int r = 0; r < 2 * NS; ++r) av[r] = rec[r * RECF + 4 + dim];
      if (c16 >= 2 * NS) ml = v2f{M_INIT, 0.f};
      float M = fmaxf(ml.x, dpp<XOR2>(ml.x)); M = fmaxf(M, dpp<ROR4>(M)); M = fmaxf(M, dpp<ROR8>(M));
      const float e = __builtin_amdgcn_exp2f(ml.x - M);
      float ls = e * ml.y; ls += dpp<XOR2>(ls); ls += dpp<ROR4>(ls); ls += dpp<ROR8>(ls);
      float o0 = 0.f, o1 = 0.f;
#pragma unroll
      for (int r = 0; r < 2 * NS; r += 2) { o0 = fmaf(rdlane(e, r), av[r], o0); o1 = fmaf(rdlane(e, r + 1), av[r + 1], o1); }
      put(rs, WS_P + wg * PBLK + dim * 16, fbits(o0), fbits(o1), tag);
      const float lsx = dpp<XOR1>(ls), Mx = dpp<XOR1>(M);
      put(rs, dim < 2 ? WS_P + wg * PBLK + (DH + dim) * 16 : OOB, fbits((lane & 1) ? lsx : M), fbits((lane & 1) ? ls : Mx), tag);
    }
    v4u g[8], gm = zero4;
#pragma unroll
    for (int p = 0; p < 8; ++p) g[p] = zero4;
    const int mp = (lane >> 1) & 7, po = WS_P + h * 8 * PBLK + dim * 16, pm = WS_P + (h * 8 + mp) * PBLK + (DH + (lane & 1)) * 16;
    for (unsigned sent = 0, spin = 0; sent != 0xffu && spin < 4 * SPIN_CAP; ++spin) {
      const unsigned want = __builtin_amdgcn_readfirstlane(CT[5]) & ~sent & 0xffu;
#pragma unroll
      for (int p = 0; p < 8; ++p)
        if ((want >> p) & 1u) g[p] = __builtin_amdgcn_raw_buffer_load_b128(rs, po, p * PBLK, 16);
      if ((want >> mp) & 1u) gm = __builtin_amdgcn_raw_buffer_load_b128(rs, pm, 0, 16);
      sent |= want;
    }
    for (int spin = 0; spin < SPIN_CAP; ++spin) {
      bool ok = true;
#pragma unroll
      for (int p = 0; p < 8; ++p)
        if (!__all(g[p].y == tag && g[p].w == tag)) { g[p] = __builtin_amdgcn_raw_buffer_load_b128(rs, po, p * PBLK, 16); ok = false; }
      if (!__all(gm.y == tag && gm.w == tag)) { gm = __builtin_amdgcn_raw_buffer_load_b128(rs, pm, 0, 16); ok = false; }
      if (ok) break;
      asm volatile("" ::: "memory");
    }
    float M0 = ubits(gm.x), M1 = ubits(gm.z);
    M0 = fmaxf(M0, dpp<XOR2>(M0)); M1 = fmaxf(M1, dpp<XOR2>(M1)); M0 = fmaxf(M0, dpp<ROR4>(M0)); M1 = fmaxf(M1, dpp<ROR4>(M1)); M0 = fmaxf(M0, dpp<ROR8>(M0)); M1 = fmaxf(M1, dpp<ROR8>(M1));
    const float e0 = __builtin_amdgcn_exp2f(ubits(gm.x) - M0), e1 = __builtin_amdgcn_exp2f(ubits(gm.z) - M1);
    float l0 = e0 * dpp<XOR1>(ubits(gm.x)), l1 = e1 * dpp<XOR1>(ubits(gm.z));
    l0 += dpp<XOR2>(l0); l1 += dpp<XOR2>(l1); l0 += dpp<ROR4>(l0); l1 += dpp<ROR4>(l1); l0 += dpp<ROR8>(l0); l1 += dpp<ROR8>(l1);
    float o0 = 0.f, o1 = 0.f;
#pragma unroll
    for (int p = 0; p < 8; ++p) { o0 = fmaf(rdlane(e0, 2 * p), ubits(g[p].x), o0); o1 = fmaf(rdlane(e1, 2 * p), ubits(g[p].z), o1); }
    const unsigned ob = pk(o0 * __builtin_amdgcn_rcpf(rdlane(l0, 0)), o1 * __builtin_amdgcn_rcpf(rdlane(l1, 0)));
    ((LAS unsigned short*)(L + L_O))[dim] = (unsigned short)ob; ((LAS unsigned short*)(L + L_O))[DH + dim] = (unsigned short)(ob >> 16);
  } else {
    if (w == 0) load_wo();
    xv0 = a.x[80 * j + 16 * tile + c16]; xv1 = a.x[DM + 80 * j + 16 * tile + c16];
    if (w == 0) { waitvm<0>(); if (lane == 0) CT[2] = 1u; }
    if (w == NW - 1) watch<8>(rs, WS_G + h * 8 * 128, 128, lane, tag, CT + 5);
    landed(CT + 2);
#pragma unroll
    for (int k = 0; k < 6; ++k) bw[k] = bfrag<160>(L + L_WO + (32 * k + 8 * g4 + tq) * 160 + (16 * tile + 4 * tp) * 2);
    if (w == 0 && ffn) load_ffn();
  }
  BARRIER();
  if (tilew) {
    const int ybase = WS_Y + (j * 5 + tile) * (NH * 256);
    __builtin_amdgcn_raw_buffer_store_b32(tag, rs, tid == 0 ? WS_D + wg * 128 : OOB, 0, 16);
    v4f d = ctag;
#pragma unroll
    for (int k = 0; k < 6; ++k) {
      const v4u ao = *(const LAS v4u*)(L + L_O + (c16 & 2) * DH + (32 * k + 8 * g4) * 2);
      d = mfma((c16 & 1) ? zero4 : ao, bw[k], d);
    }
    __builtin_amdgcn_raw_buffer_store_b128(__builtin_bit_cast(v4u, d), rs, lane < 16 ? ybase + h * 256 + c16 * 16 : OOB, 0, 16);
    if (ffn) {
      if (w == 0) { waitvm<0>(); if (lane == 0) CT[3] = 1u; }
      gu_frags();
      v4u g[8];
#pragma unroll
      for (int k = 0; k < 8; ++k) g[k] = zero4;
      for (unsigned sent = 0, spin = 0; sent != 0x11111111u && spin < 4 * SPIN_CAP; ++spin) {
        unsigned m = __builtin_amdgcn_readfirstlane(CT[6]);
        m &= m >> 1; m &= m >> 2; m &= 0x11111111u & ~sent;
#pragma unroll
        for (int k = 0; k < 8; ++k)
          if ((m >> (4 * k)) & 1u) g[k] = __builtin_amdgcn_raw_buffer_load_b128(rs, ybase + lane * 16 + (k & 3) * 1024, (k >> 2) * 4096, 16);
        sent |= m;
      }
      for (int spin = 0; spin < SPIN_CAP; ++spin) {
        bool ok = true;
#pragma unroll
        for (int k = 0; k < 8; ++k)
          if (!__all(g[k].y == tag && g[k].w == tag)) { g[k] = __builtin_amdgcn_raw_buffer_load_b128(rs, ybase + lane * 16 + (k & 3) * 1024, (k >> 2) * 4096, 16); ok = false; }
        if (ok) break;
        asm volatile("" ::: "memory");
      }
      const float s0 = ((ubits(g[0].x) + ubits(g[1].x)) + (ubits(g[2].x) + ubits(g[3].x))) + ((ubits(g[4].x) + ubits(g[5].x)) + (ubits(g[6].x) + ubits(g[7].x)));
      const float s1 = ((ubits(g[0].z) + ubits(g[1].z)) + (ubits(g[2].z) + ubits(g[3].z))) + ((ubits(g[4].z) + ubits(g[5].z)) + (ubits(g[6].z) + ubits(g[7].z)));
      float s = sw16add(s0, s1); s = sw32add(s, s);
      const float hv = bflo((g4 & 1) ? xv1 : xv0) + bfr(s);
      if (lane < 32) ((LAS unsigned short*)(L + L_HS))[g4 * 96 + 16 * tile + c16] = (unsigned short)pk(hv, 0.f);
    }
  } else if (ffn) {
    if (w == 1) watch<32>(rs, WS_D + j * 128, 8 * 128, lane, tag, CT + 6);
    gu_frags();
  }
  if (ffn) {
    int eo = WS_E + (cg * 8 + w) * 5120;
    const int eo0 = lane < 32 ? eo + j * 512 + lane * 16 : OOB, eo1 = lane < 8 ? eo + 4096 + j * 128 + lane * 16 : OOB;
    BARRIER();
    {
      v4f d = ctag, ds = ctag;
#pragma unroll
      for (int k = 0; k < 3; ++k) {
        v4u ah = *(const LAS v4u*)(L + L_HS + (c16 < 5 ? c16 >> 1 : 0) * 192 + (32 * k + 8 * g4) * 2);
        if ((c16 & 1) || c16 > 4) ah = zero4;
        d = mfma(ah, bg[k], d);
        ds = mfma(ah, ah, ds);
      }
      __builtin_amdgcn_raw_buffer_store_b128(__builtin_bit_cast(v4u, d), rs, eo0, 0, 16);
      __builtin_amdgcn_raw_buffer_store_b128(__builtin_bit_cast(v4u, ds), rs, eo1, 0, 16);
    }
    if (w >= 1 && w <= 4) {
      v4u g[5], bdn[3]; int zo[3];
#pragma unroll
      for (int T = 0; T < 3; ++T) {
        const int dt = w - 1 + 4 * T, n = 64 * dt + lane;
        bdn[T] = bfrag<1280>(L + L_WD + tq * 1280 + (64 * (dt < 10 ? dt : 0) + 16 * g4 + 4 * tp) * 2);
        zo[T] = dt < 10 ? WS_Z + ((n >> 2) * NFFN + f) * 64 + (n & 3) * 16 : OOB;
        asm volatile("" : "+v"(zo[T]));
      }
      __builtin_amdgcn_s_sleep(10);
      gather<5>(rs, WS_E + f * 5120 + lane * 16, tag, g);
      __builtin_amdgcn_raw_buffer_store_b32(tag, rs, (tid == 64 && j == 0) ? WS_F + cg * 128 : OOB, 0, 16);
      const float s0 = (ubits(g[0].x) + ubits(g[1].x)) + (ubits(g[2].x) + ubits(g[3].x)), s1 = (ubits(g[0].z) + ubits(g[1].z)) + (ubits(g[2].z) + ubits(g[3].z));
      const float gs = sw32add(s0, s1);
      const float cs0 = sw32add(s0, s0);
      const auto cx = __builtin_amdgcn_permlane16_swap(fbits(cs0), fbits(cs0), false, false);
      const auto gx = __builtin_amdgcn_permlane16_swap(fbits(gs), fbits(gs), false, false);
      float t0 = ubits(g[4].x), t1 = ubits(g[4].z);
      t0 += dpp<ROR8>(t0); t1 += dpp<ROR8>(t1);
      t0 = sw16add(t0, t0); t1 = sw16add(t1, t1);
      const float tt = sw32add(t0, t1) * (1.0f / DM);
      const float mean = dpp<BC + 4>(tt), q0 = dpp<BC + 0>(tt), q1 = dpp<BC + 2>(tt);
      const float rdv = ln_rden(fmaxf((hf ? q1 : q0) - mean * mean, 0.f));
      const float gv = bfr((ubits(gx[0]) - bfr(mean) * ubits(cx[1])) * rdv), uv = dpp<ROR8>(gv);
      const float y2 = (-2.0f * 0.7978845608028654f * 1.4426950408889634f) * (gv + 0.044715f * gv * gv * gv);
      const float act = bfr(gv * __builtin_amdgcn_rcpf(1.0f + __builtin_amdgcn_exp2f(y2))) * uv;
      const unsigned ap = pk(act, dpp<XOR1>(act));
      const auto ax = __builtin_amdgcn_permlane32_swap(ap, ap, false, false);
      const unsigned keep = ((c16 >> 2) == g4 && !(c16 & 1)) ? 0xffffffffu : 0u, k1 = (c16 & 2) ? keep : 0u, k0 = keep ^ k1;
      const v4u b0 = {dppu<BC + 0>(ax[0]), dppu<BC + 2>(ax[0]), dppu<BC + 4>(ax[0]), dppu<BC + 6>(ax[0])};
      const v4u b1 = {dppu<BC + 0>(ax[1]), dppu<BC + 2>(ax[1]), dppu<BC + 4>(ax[1]), dppu<BC + 6>(ax[1])};
      const v4u a2 = (b0 & k0) | (b1 & k1);
#pragma unroll
      for (int T = 0; T < 3; ++T) __builtin_amdgcn_raw_buffer_store_b128(__builtin_bit_cast(v4u, mfma(a2, bdn[T], ctag)), rs, zo[T], 0, 16);
    } else if (w == NW - 1 && cg < 20) {
      const __amdgpu_buffer_rsrc_t ro = __builtin_amdgcn_make_buffer_rsrc((void*)a.out, 0, 2 * DM * 2, 0x00020000);
      const float hv = bflo(((const LAS unsigned short*)(L + L_HS))[(g4 & 1) * 96 + 4 * cg + (lane & 3)]);
      v4u g[12];
      watch<24>(rs, WS_F, 128, lane, tag, CT + 7);
      gather<12>(rs, WS_Z + (20 * j + cg) * (NFFN * 64) + lane * 16, tag, g);
      float s0[4], s1[4];
#pragma unroll
      for (int k = 0; k < 4; ++k) { s0[k] = (ubits(g[k].x) + ubits(g[k + 4].x)) + ubits(g[k + 8].x); s1[k] = (ubits(g[k].z) + ubits(g[k + 4].z)) + ubits(g[k + 8].z); }
      float s = sw16add((s0[0] + s0[1]) + (s0[2] + s0[3]), (s1[0] + s1[1]) + (s1[2] + s1[3]));
      s = sw32add(s, s); s += dpp<ROR4>(s); s += dpp<ROR8>(s);
      const unsigned o = pk(hv + bfr(s), 0.f);
      __builtin_amdgcn_raw_buffer_store_b16((unsigned short)o, ro, (lane < 32 && c16 < 4) ? (g4 * DM + 80 * j + 4 * cg + c16) * 2 : OOB, 0, 0);
      __builtin_amdgcn_raw_buffer_store_b32(tag, rs, (wg == 0 && lane == 0) ? 0 : OOB, 0, 16);
    }
  }
}
}

extern "C" void kernel_launch(void* const* d_in, const int* in_sizes, int n_in, void* d_out, int out_size, void* d_ws, size_t ws_size, hipStream_t stream) {
  Args a;
  a.x = (const unsigned short*)d_in[0]; a.k = (const unsigned short*)d_in[1]; a.v = (const unsigned short*)d_in[2];
  a.wq = (const unsigned short*)d_in[3]; a.wo = (const unsigned short*)d_in[4];
  a.wg = (const unsigned short*)d_in[6]; a.wu = (const unsigned short*)d_in[7]; a.wd = (const unsigned short*)d_in[8];
  a.out = (unsigned short*)d_out; a.ws = (unsigned char*)d_ws;
  hipLaunchKernelGGL(decode_block, dim3(NWG), dim3(NTHR), 0, stream, a);
}
```

```cpp
#include <hip/hip_runtime.h>
#include <hip/hip_bf16.h>
#include <stdint.h>

namespace {
typedef unsigned v4u __attribute__((ext_vector_type(4)));
typedef unsigned v2u __attribute__((ext_vector_type(2)));
typedef float v4f __attribute__((ext_vector_type(4)));
typedef float v2f __attribute__((ext_vector_type(2)));
typedef short v4s __attribute__((ext_vector_type(4)));
typedef __bf16 bf2 __attribute__((ext_vector_type(2)));
typedef __bf16 bf8 __attribute__((ext_vector_type(8)));
#define LAS __attribute__((address_space(3)))
#define CAS __attribute__((address_space(4)))

constexpr int DM = 640, NH = 32, SK = 16384, DH = 192, FF = 1536;
constexpr int NWG = 256, NW = 8, NS = NW - 1, NTHR = NW * 64;
constexpr unsigned ITEMB = 8 * DH * 2;
constexpr unsigned HEADB = (unsigned)SK * DH * 2, B1OFF = NH * HEADB;
constexpr int IPH = SK / 8;
constexpr int S0 = IPH / 8, S1 = 208, STAT = S0 + S1;
constexpr int POOL = IPH - 8 * S1, LEAD = 8;
constexpr int NEXTRA = 4, NACT = 5;
constexpr unsigned END = 0xF0000000u, EMPTY = 0xFFFFFFFFu;
constexpr int NRED = DM / 4, NFFN = FF / 8;
constexpr int SPIN_CAP = 1 << 21;
constexpr float M_INIT = -3.0e38f;

constexpr int WS_CTR = 4096, CTR_STRIDE = 4352;
constexpr int WS_F = 0x24000, WS_D = 0x28000, WS_G = 0x30000, WS_QB = 0x38000, WS_Q = 0x50000, WS_P = 0x150000, PBLK = 3200, WS_Y = 0x220000, WS_E = 0x280000, WS_Z = 0x400000, WS_BYTES = 0x600000;
constexpr int OOB = (int)0x80000000u;
static_assert(WS_CTR + NH * CTR_STRIDE <= WS_F && WS_F + 24 * 128 <= WS_D && WS_D + NWG * 128 <= WS_G && WS_G + NWG * 128 <= WS_QB && WS_QB + NWG * 256 <= WS_Q && WS_Q + NWG * 4096 <= WS_P && WS_P + NWG * PBLK <= WS_Y, "ws map");
static_assert(WS_Y + NWG * 80 * 16 <= WS_E && WS_E + NFFN * 5120 <= WS_Z && WS_Z + NRED * NFFN * 64 <= WS_BYTES, "ws map");

constexpr int L_WO = 0, L_G = L_WO + DH * 160, L_WD = L_G + 2 * 80 * 128, L_QP = L_WD + 8 * 1280;
constexpr int L_QS = L_QP + NW * 2 * DH * 4, RECF = DH + 4, L_REC = L_QS + 2 * DH * 2, L_O = L_REC + NS * 2 * RECF * 4, L_HS = L_O + 2 * DH * 2;
constexpr int L_CT = L_HS + 3 * 192, DYNMAX = POOL + 96, L_DYN = L_CT + 64, L_BYTES = L_DYN + DYNMAX * 4;
static_assert(L_BYTES > 81920 && L_BYTES <= 160 * 1024 && L_REC % 16 == 0 && L_O % 16 == 0 && L_HS % 16 == 0, "one workgroup per CU");

struct Args {
  const unsigned short *x, *k, *v, *wq, *wo, *wg, *wu, *wd;
  unsigned short* out;
  unsigned char* ws;
};

__device__ __forceinline__ float dot2(unsigned a, unsigned b, float c) {
  return __builtin_amdgcn_fdot2_f32_bf16(__builtin_bit_cast(bf2, a), __builtin_bit_cast(bf2, b), c, false);
}
template <int CTRL> __device__ __forceinline__ float dpp(float x) {
  return __builtin_bit_cast(float, __builtin_amdgcn_mov_dpp(__builtin_bit_cast(int, x), CTRL, 0xf, 0xf, true));
}
template <int CTRL> __device__ __forceinline__ unsigned dppu(unsigned x) { return (unsigned)__builtin_amdgcn_mov_dpp((int)x, CTRL, 0xf, 0xf, true); }
constexpr int BC = 0x150;
constexpr int XOR1 = 0xB1, XOR2 = 0x4E, HMIR = 0x141, ROR4 = 0x124, ROR8 = 0x128;
__device__ __forceinline__ float bflo(unsigned x) { return __builtin_bit_cast(float, x << 16); }
__device__ __forceinline__ float bfhi(unsigned x) { return __builtin_bit_cast(float, x & 0xffff0000u); }
__device__ __forceinline__ unsigned pk(float lo, float hi) {
  const bf2 r = {(__bf16)lo, (__bf16)hi};
  return __builtin_bit_cast(unsigned, r);
}
__device__ __forceinline__ float bfr(float x) { return (float)(__bf16)x; }
__device__ __forceinline__ unsigned fbits(float x) { return __builtin_bit_cast(unsigned, x); }
__device__ __forceinline__ float ubits(unsigned x) { return __builtin_bit_cast(float, x); }
__device__ __forceinline__ float sw16add(float a, float b) {
  const auto s = __builtin_amdgcn_permlane16_swap(fbits(a), fbits(b), false, false);
  return ubits(s[0]) + ubits(s[1]);
}
__device__ __forceinline__ float sw32add(float a, float b) {
  const auto s = __builtin_amdgcn_permlane32_swap(fbits(a), fbits(b), false, false);
  return ubits(s[0]) + ubits(s[1]);
}
__device__ __forceinline__ float sw16max(float a) {
  const auto s = __builtin_amdgcn_permlane16_swap(fbits(a), fbits(a), false, false);
  return fmaxf(ubits(s[0]), ubits(s[1]));
}
__device__ __forceinline__ float sw32max(float a) {
  const auto s = __builtin_amdgcn_permlane32_swap(fbits(a), fbits(a), false, false);
  return fmaxf(ubits(s[0]), ubits(s[1]));
}
__device__ __forceinline__ float rowsum16(float s) { s += dpp<XOR1>(s); s += dpp<XOR2>(s); s += dpp<HMIR>(s); s += dpp<ROR8>(s); return s; }
__device__ __forceinline__ float rdlane(float x, int l) { return ubits(__builtin_amdgcn_readlane(fbits(x), l)); }
__device__ __forceinline__ v4u mkrsrc(const void* p, unsigned bytes) {
  const unsigned long long a = (unsigned long long)p;
  return v4u{(unsigned)a, (unsigned)(a >> 32) & 0xffffu, bytes, 0x00020000u};
}
__device__ __forceinline__ v4f mfma(v4u a, v4u b, v4f c) {
  return __builtin_amdgcn_mfma_f32_16x16x32_bf16(__builtin_bit_cast(bf8, a), __builtin_bit_cast(bf8, b), c, 0, 0, 0);
}
template <int STRIDE> __device__ __forceinline__ v4u bfrag(LAS unsigned char* p) {
  const v2u lo = __builtin_bit_cast(v2u, __builtin_amdgcn_ds_read_tr16_b64_v4i16((LAS v4s*)p));
  const v2u hi = __builtin_bit_cast(v2u, __builtin_amdgcn_ds_read_tr16_b64_v4i16((LAS v4s*)(p + 4 * STRIDE)));
  return v4u{lo.x, lo.y, hi.x, hi.y};
}
#define BARRIER() asm volatile("s_waitcnt lgkmcnt(0)\n\ts_barrier" ::: "memory")

#define LD3 "s_nop 4\n\tbuffer_load_dwordx4 %0, %3, %4, %5 offen nt\n\tbuffer_load_dwordx4 %1, %3, %4, %5 offen offset:128 nt\n\tbuffer_load_dwordx4 %2, %3, %4, %5 offen offset:256 nt"
__device__ __forceinline__ void ld_first(v4u& d, int voff, v4u rs, unsigned soff) {
  asm volatile("s_nop 4\n\tbuffer_load_dwordx4 %0, %1, %2, %3 offen nt" : "=v"(d) : "v"(voff), "s"(rs), "s"(soff));
}
__device__ __forceinline__ void ld3_first(v4u (&r)[3], int voff, v4u rs, unsigned so) { asm volatile(LD3 : "=&v"(r[0]), "=&v"(r[1]), "=&v"(r[2]) : "v"(voff), "s"(rs), "s"(so)); }
__device__ __forceinline__ void ld3_again(v4u (&r)[3], int voff, v4u rs, unsigned so) { asm volatile(LD3 : "+v"(r[0]), "+v"(r[1]), "+v"(r[2]) : "v"(voff), "s"(rs), "s"(so)); }
template <int N> __device__ __forceinline__ void wait3(v4u (&r)[3]) { asm volatile("s_waitcnt vmcnt(%3)" : "+v"(r[0]), "+v"(r[1]), "+v"(r[2]) : "i"(N)); }
__device__ __forceinline__ void pin3(v4u (&r)[3]) { asm volatile("" : "+v"(r[0]), "+v"(r[1]), "+v"(r[2])); }
template <int N> __device__ __forceinline__ void waitvm() { asm volatile("s_waitcnt vmcnt(%0)" : : "i"(N) : "memory"); }

__device__ __forceinline__ void dma16(const void* base, int voff, unsigned lds) {
  unsigned keep;
  asm volatile("s_mov_b32 %0, m0\n\ts_mov_b32 m0, %3\n\ts_nop 2\n\tglobal_load_lds_dwordx4 %1, %2 nt\n\ts_mov_b32 m0, %0" : "=&s"(keep) : "v"(voff), "s"(base), "s"(lds) : "memory");
}

__device__ __forceinline__ void put(__amdgpu_buffer_rsrc_t rs, int off, unsigned a, unsigned b, unsigned tag) {
  __builtin_amdgcn_raw_buffer_store_b128(v4u{a, tag, b, tag}, rs, off, 0, 16);
}
template <int N> __device__ __forceinline__ void gather(__amdgpu_buffer_rsrc_t rs, int off, unsigned tag, v4u (&t)[N]) {
  bool done = false;
#pragma unroll
  for (int k = 0; k < N; ++k) t[k] = v4u{0u, 0u, 0u, 0u};
  for (int spin = 0; spin < SPIN_CAP; ++spin) {
    if (!done) {
#pragma unroll
      for (int k = 0; k < N; ++k) t[k] = __builtin_amdgcn_raw_buffer_load_b128(rs, off + (k & 3) * 1024, (k >> 2) * 4096, 16);
      unsigned mn = tag, mx = tag;
#pragma unroll
      for (int k = 0; k < N; ++k) { mn = min(mn, min(t[k].y, t[k].w)); mx = max(mx, max(t[k].y, t[k].w)); }
      done = mn == mx;
    }
    if (__all(done)) break;
    asm volatile("" ::: "memory");
    __builtin_amdgcn_s_sleep(1);
  }
}

template <int N> __device__ __forceinline__ void watch(__amdgpu_buffer_rsrc_t rs, int off, int stride, int lane, unsigned tag, volatile LAS unsigned* mask) {
  bool seen = lane >= N;
  for (int spin = 0; spin < SPIN_CAP; ++spin) {
    if (!seen) seen = __builtin_amdgcn_raw_buffer_load_b32(rs, off + lane * stride, 0, 16) == tag;
    const unsigned long long m = __ballot(seen);
    if (lane == 0) *mask = (unsigned)m;
    if (m == ~0ull) break;
    asm volatile("" ::: "memory");
    __builtin_amdgcn_s_sleep(1);
  }
  if (lane == 0) *mask = ~0u;
}
#define landed(p) do { for (int sp_ = 0; sp_ < 16 * SPIN_CAP && *(p) == 0u; ++sp_) __builtin_amdgcn_s_sleep(1); asm volatile("" ::: "memory"); } while (0)
#define hint(p, D) do { for (int sp_ = 0; sp_ < SPIN_CAP && *(p) == 0u; ++sp_) __builtin_amdgcn_s_sleep(1); __builtin_amdgcn_s_sleep(D); } while (0)

__device__ __forceinline__ float ln_rden(float var) { return __builtin_amdgcn_rcpf(bfr(__builtin_amdgcn_sqrtf(bfr(bfr(var) + 1e-5f)))); }
__device__ __forceinline__ float ln_val(float x, float mean_b, float rden) { return bfr(bfr(x - mean_b) * rden); }

struct Soft { float m, l, acc[24]; };
__device__ __forceinline__ float score(const v4u (&kk)[3], const v4u (&q)[3]) {
  float s0 = 0.f, s1 = 0.f;
#pragma unroll
  for (int j = 0; j < 3; ++j) {
    s0 = dot2(kk[j].x, q[j].x, s0); s1 = dot2(kk[j].y, q[j].y, s1);
    s0 = dot2(kk[j].z, q[j].z, s0); s1 = dot2(kk[j].w, q[j].w, s1);
  }
  return s0 + s1;
}
__device__ __forceinline__ float soft(Soft& st, float s) {
  s += dpp<XOR1>(s); s += dpp<XOR2>(s); s += dpp<HMIR>(s);
  s = bfr(bfr(s) * 0.07216878364870322f) * 1.4426950408889634f;
  if (__any(s > st.m)) {
    const float mn = fmaxf(st.m, s), al = __builtin_amdgcn_exp2f(st.m - mn);
    st.l *= al;
#pragma unroll
    for (int k = 0; k < 24; ++k) st.acc[k] *= al;
    st.m = mn;
  }
  const float p = __builtin_amdgcn_exp2f(s - st.m);
  st.l += p;
  return p;
}
__device__ __forceinline__ void pv(Soft& st, float p, const v4u (&vv)[3]) {
#pragma unroll
  for (int j = 0; j < 3; ++j)
#pragma unroll
    for (int e = 0; e < 4; ++e) {
      st.acc[j * 8 + 2 * e] = fmaf(p, bflo(vv[j][e]), st.acc[j * 8 + 2 * e]);
      st.acc[j * 8 + 2 * e + 1] = fmaf(p, bfhi(vv[j][e]), st.acc[j * 8 + 2 * e + 1]);
    }
}

__global__ void __launch_bounds__(NTHR, 2) decode_block(Args a) {
  __shared__ __attribute__((aligned(16))) unsigned char smem[L_BYTES];
  LAS unsigned char* L = (LAS unsigned char*)smem;
  volatile LAS unsigned* CT = (volatile LAS unsigned*)(L + L_CT);
  volatile LAS unsigned* DYN = (volatile LAS unsigned*)(L + L_DYN);
  const int tid = threadIdx.x, lane = tid & 63, w = __builtin_amdgcn_readfirstlane(tid >> 6);
  const int wg = blockIdx.x, h = wg >> 3, j = wg & 7;
  const int hf = lane >> 5, l5 = lane & 31;
  const bool ffn = ((h + j) & 3) != 3;
  const int cg = 3 * (h >> 2) + (h & 3) - ((h & 3) > ((3 - j) & 3) ? 1 : 0), f = 8 * cg + j;
  const __amdgpu_buffer_rsrc_t rs = __builtin_amdgcn_make_buffer_rsrc((void*)a.ws, 0, WS_BYTES, 0x00020000);
  unsigned* ctr = (unsigned*)(a.ws + WS_CTR + h * CTR_STRIDE);

  const unsigned lb = (unsigned)(uintptr_t)L;
  auto load_wo = [&]() {
    const unsigned char* wo = (const unsigned char*)a.wo + (size_t)h * DH * (DM * 2) + j * 160;
    int vo = (lane / 10) * (DM * 2) + (lane % 10) * 16, c10 = lane % 10;
#pragma unroll 2
    for (int k = 0; k < 30; ++k) {
      dma16(wo, vo, lb + L_WO + k * 1024);
      c10 += 4; vo += 6 * (DM * 2) + 64;
      if (c10 >= 10) { c10 -= 10; vo += DM * 2 - 160; }
    }
  };
  auto load_ffn = [&]() {
    const size_t o0 = (size_t)80 * j * (FF * 2) + cg * 128;
    const int vg = (lane >> 3) * (FF * 2) + (lane & 7) * 16;
#pragma unroll 1
    for (int k = 0; k < 10; ++k) {
      dma16((const unsigned char*)a.wg + o0 + k * 8 * FF * 2, vg, lb + L_G + k * 1024);
      dma16((const unsigned char*)a.wu + o0 + k * 8 * FF * 2, vg, lb + L_G + 10240 + k * 1024);
      dma16((const unsigned char*)a.wd + (size_t)f * 8 * (DM * 2) + k * 1024, lane * 16, lb + L_WD + k * 1024);
    }
  };
  unsigned epoch;
  {
    const int zero = 0;
    if (w == 0) {
      __builtin_amdgcn_s_setprio(3);
      if (j == 0) asm volatile("s_nop 4\n\tglobal_store_dword %0, %0, %1 sc1" : : "v"(zero), "s"(ctr) : "memory");
    }
    asm volatile("s_nop 4\n\tglobal_load_dword %0, %1, %2 sc1" : "=v"(epoch) : "v"(zero), "s"(a.ws));
  }
  v4u wqr[5], xa, xb; v2u xc;
  {
    const size_t rowoff = (size_t)(80 * j + 10 * w) * (NH * DH * 2) + h * (DH * 2);
    const v4u rw = mkrsrc((const unsigned char*)a.wq + rowoff, (unsigned)((size_t)DM * NH * DH * 2 - rowoff));
    const int vo = l5 < 24 ? hf * (NH * DH * 2) + l5 * 16 : OOB;
#pragma unroll
    for (int i = 0; i < 5; ++i) ld_first(wqr[i], vo, rw, i * 2 * NH * DH * 2);
    const v4u rx = mkrsrc(a.x, 2 * DM * 2);
    const int xo = hf * (DM * 2) + l5 * 40;
    asm volatile("s_nop 4\n\tbuffer_load_dwordx4 %0, %3, %4, 0 offen\n\tbuffer_load_dwordx4 %1, %3, %4, 0 offen offset:16\n\tbuffer_load_dwordx2 %2, %3, %4, 0 offen offset:32"
                 : "=&v"(xa), "=&v"(xb), "=&v"(xc) : "v"(xo), "s"(rx));
  }
  const v4u rk = mkrsrc((const unsigned char*)a.k + (size_t)h * HEADB, B1OFF + HEADB), rv = mkrsrc((const unsigned char*)a.v + (size_t)h * HEADB, B1OFF + HEADB);
  const int voff = (lane >> 3) * (DH * 2) + (lane & 7) * 16;
  const unsigned sb0 = (unsigned)(S0 * j) * ITEMB, sb1 = B1OFF + (unsigned)(S1 * j) * ITEMB, rot = 21 * j;
  auto item0 = [&](unsigned t) { return sb0 + ((t + rot) & (S0 - 1)) * ITEMB; };
  static_assert((S0 & (S0 - 1)) == 0 && 21 * 7 < S1, "wraps");
  v4u km[3], vm[3], ke[NEXTRA][3], ve[NEXTRA][3];
  auto first = [&](int k) { return w ? item0(w - 1 + k * NS) : END; };
  asm volatile("s_barrier" ::: "memory");
  ld3_first(km, voff, rk, first(0)); ld3_first(vm, voff, rv, first(0));
  if (w == 0) {
    for (int k = lane; k < DYNMAX; k += 64) DYN[k] = EMPTY;
    if (lane < 48) {
      ((LAS unsigned*)(L + L_HS))[lane] = 0u; ((LAS unsigned*)(L + L_HS))[48 + lane] = 0u; ((LAS unsigned*)(L + L_HS))[96 + lane] = lane < 40 ? 0x3f803f80u : 0u;
    }
    if (lane < 8) CT[lane] = lane ? 0u : (2 * NEXTRA + 1) * NS;
  }
  asm volatile("s_waitcnt vmcnt(6)" : "+v"(wqr[0]), "+v"(wqr[1]), "+v"(wqr[2]), "+v"(wqr[3]), "+v"(wqr[4]), "+v"(xa), "+v"(xb), "+v"(xc), "+v"(epoch));
  ld3_first(ke[0], voff, rk, first(1)); ld3_first(ve[0], voff, rv, first(1));
  const unsigned tag = ((__builtin_amdgcn_readfirstlane(epoch) + 0x1E3779B9u) & 0x1FFFFFFFu) | 0x20000000u;
  {
    const unsigned xw[10] = {xa.x, xa.y, xa.z, xa.w, xb.x, xb.y, xb.z, xb.w, xc.x, xc.y};
    float s = 0.f, sq = 0.f;
#pragma unroll
    for (int k = 0; k < 10; ++k) { s = dot2(xw[k], 0x3f803f80u, s); sq = dot2(xw[k], xw[k], sq); }
    s = rowsum16(sw16add(s, sq)) * (1.0f / DM);
    const auto sx = __builtin_amdgcn_permlane16_swap(fbits(s), fbits(s), false, false);
    const float mean = ubits(sx[0]), mb = bfr(mean), rd = ln_rden(fmaxf(ubits(sx[1]) - mean * mean, 0.f));
    const float mb0 = rdlane(mb, 0), mb1 = rdlane(mb, 32), rd0 = rdlane(rd, 0), rd1 = rdlane(rd, 32);
    const CAS unsigned* x32 = (const CAS unsigned*)a.x + 40 * j + 5 * w;
    float qa[8], qb[8];
#pragma unroll
    for (int e = 0; e < 8; ++e) { qa[e] = 0.f; qb[e] = 0.f; }
#pragma unroll
    for (int i = 0; i < 5; ++i) {
      const unsigned u0 = x32[i], u1 = x32[DM / 2 + i];
      const float n0 = ln_val(hf ? bfhi(u0) : bflo(u0), mb0, rd0), n1 = ln_val(hf ? bfhi(u1) : bflo(u1), mb1, rd1);
#pragma unroll
      for (int e = 0; e < 4; ++e) {
        const float w0 = bflo(wqr[i][e]), w1 = bfhi(wqr[i][e]);
        qa[2 * e] = fmaf(n0, w0, qa[2 * e]); qa[2 * e + 1] = fmaf(n0, w1, qa[2 * e + 1]);
        qb[2 * e] = fmaf(n1, w0, qb[2 * e]); qb[2 * e + 1] = fmaf(n1, w1, qb[2 * e + 1]);
      }
    }
    float r[8];
#pragma unroll
    for (int e = 0; e < 8; ++e) r[e] = sw32add(qa[e], qb[e]);
    if (l5 < 24) {
      LAS v4f* dst = (LAS v4f*)(L + L_QP + ((w * 2 + hf) * DH + l5 * 8) * 4);
      dst[0] = v4f{r[0], r[1], r[2], r[3]}; dst[1] = v4f{r[4], r[5], r[6], r[7]};
    }
  }
  if (w == 0) asm volatile("s_waitcnt vmcnt(0) ; HELPER_ONLY" ::: "memory");
  BARRIER();

  if (w == 0) {
    asm volatile("; HELPER_ENTRY");
    {
      const LAS float* QP = (const LAS float*)(L + L_QP) + 24 * hf + (l5 < 24 ? l5 : 0);
#pragma unroll
      for (int k = 0; k < 4; ++k) {
        float s0 = 0.f, s1 = 0.f;
#pragma unroll
        for (int ww = 0; ww < NW; ++ww) { s0 += QP[(ww * 2) * DH + 48 * k]; s1 += QP[(ww * 2 + 1) * DH + 48 * k]; }
        put(rs, l5 < 24 ? WS_Q + ((h * 8 + 2 * k + hf) * 8 + j) * 512 + l5 * 16 : OOB, fbits(s0), fbits(s1), tag);
      }
      if (lane == 0) CT[1] = 1u;
    }
    BARRIER();
    int filled = 0;
    for (int spin = 0; spin < 16 * SPIN_CAP; ++spin) {
      int need = (int)__builtin_amdgcn_readfirstlane(CT[0]) + LEAD - (STAT + filled);
      if (need > 0) {
        need = need > 32 ? 32 : need;
        unsigned old = 0;
        if (lane == 0) old = __hip_atomic_fetch_add(ctr, (unsigned)need, __ATOMIC_RELAXED, __HIP_MEMORY_SCOPE_AGENT);
        old = __builtin_amdgcn_readfirstlane(old);
        const bool done = old + need >= (unsigned)POOL;
        if (lane < (done ? need + NW : need)) DYN[filled + lane] = (lane < need && old + lane < (unsigned)POOL) ? B1OFF + (8 * S1 + old + lane) * ITEMB : END;
        filled += need;
        if (done) break;
      } else __builtin_amdgcn_s_sleep(2);
    }
    for (int spin = 0; spin < 16 * SPIN_CAP && CT[4] < (unsigned)NS; ++spin) __builtin_amdgcn_s_sleep(1);
    load_wo();
  } else {
    asm volatile("; STREAM_ENTRY");
    if (w == NW - 1) {
      hint(CT + 1, 10);
      v4u g[4];
      gather<4>(rs, WS_Q + wg * 4096 + hf * 512 + (l5 < 24 ? l5 : 0) * 16, tag, g);
      const float qv = sw32add((ubits(g[0].x) + ubits(g[1].x)) + (ubits(g[2].x) + ubits(g[3].x)), (ubits(g[0].z) + ubits(g[1].z)) + (ubits(g[2].z) + ubits(g[3].z)));
      const unsigned qp = pk(qv, dpp<XOR1>(qv));
      const auto qx = __builtin_amdgcn_permlane32_swap(qp, qp, false, false);
      put(rs, (lane < 24 && !(lane & 1)) ? WS_QB + wg * 256 + lane * 8 : OOB, qx[0], qx[1], tag);
      waitvm<0>();
      __builtin_amdgcn_s_sleep(16);
      v4u gq[2];
      gather<2>(rs, WS_QB + h * 2048 + (lane >> 4) * 256 + ((lane & 15) < 12 ? lane & 15 : 0) * 16, tag, gq);
      if ((lane & 15) < 12) {
        LAS unsigned* QS = (LAS unsigned*)(L + L_QS) + (lane >> 4) * 12 + (lane & 15);
        QS[0] = gq[0].x; QS[DH / 2] = gq[0].z; QS[48] = gq[1].x; QS[DH / 2 + 48] = gq[1].z;
      }
    }
#pragma unroll
    for (int e = 1; e < NEXTRA; ++e) {
      if (w != NW - 1) waitvm<0>();
      ld3_first(ke[e], voff, rk, first(e + 1));
      if (w != NW - 1) waitvm<0>();
      ld3_first(ve[e], voff, rv, first(e + 1));
    }
    BARRIER();
    v4u q[3];
#pragma unroll
    for (int jj = 0; jj < 3; ++jj) q[jj] = *(const LAS v4u*)(L + L_QS + ((lane & 7) + 8 * jj) * 16);
    Soft st;
    st.m = M_INIT; st.l = 0.f;
#pragma unroll
    for (int k = 0; k < 24; ++k) st.acc[k] = 0.f;
    auto step = [&](unsigned nxt) {
      wait3<3>(km);
      float s = score(km, q);
      asm volatile("" : "+v"(s));
      ld3_again(km, voff, rk, nxt);
      const float p = soft(st, s);
      wait3<3>(vm);
      pv(st, p, vm);
      __builtin_amdgcn_sched_barrier(0);
      ld3_again(vm, voff, rv, nxt);
    };
#pragma unroll
    for (int e = 0; e < NEXTRA; ++e) {
      step(first(NEXTRA + 1 + e));
      pin3(ke[e]); pin3(ve[e]);
      const float pe = soft(st, score(ke[e], q));
      pv(st, pe, ve[e]);
    }
    unsigned cur = 0;
#pragma unroll 1
    for (int b = 0; b < 2; ++b) {
      while (cur != END && (cur >= B1OFF) == (b == 1)) {
        unsigned t = 0;
        if (lane == 0 && w <= NACT) t = __hip_atomic_fetch_add((LAS unsigned*)(L + L_CT), 1u, __ATOMIC_RELAXED, __HIP_MEMORY_SCOPE_WORKGROUP);
        t = __builtin_amdgcn_readfirstlane(t);
        if (w > NACT) cur = END;
        else if (t < (unsigned)S0) cur = item0(t);
        else if (t < (unsigned)STAT) { t += rot - S0; cur = sb1 + (t < (unsigned)S1 ? t : t - S1) * ITEMB; }
        else {
          int spin = 0;
          while ((cur = __builtin_amdgcn_readfirstlane(DYN[t - STAT])) == EMPTY) {
            __builtin_amdgcn_s_sleep(1);
            if (++spin > SPIN_CAP) { cur = END; break; }
          }
        }
        step(cur);
      }
      wait3<0>(km); wait3<0>(vm);
      if (b == 1) {
        unsigned t = 0;
        if (lane == 0) t = __hip_atomic_fetch_add((LAS unsigned*)(L + L_CT) + 4, 1u, __ATOMIC_RELAXED, __HIP_MEMORY_SCOPE_WORKGROUP);
        if (__builtin_amdgcn_readfirstlane(t) == NS - 1) __builtin_amdgcn_raw_buffer_store_b32(tag, rs, lane == 0 ? WS_G + wg * 128 : OOB, 0, 16);
      }
      float M = fmaxf(st.m, dpp<ROR8>(st.m)); M = sw32max(sw16max(M));
      const float sc = __builtin_amdgcn_exp2f(st.m - M);
      float l = st.l * sc; l += dpp<ROR8>(l); l = sw16add(l, l); l = sw32add(l, l);
      float t1[12], t2[6];
#pragma unroll
      for (int k = 0; k < 12; ++k) t1[k] = sw16add(st.acc[k] * sc, st.acc[k + 12] * sc);
#pragma unroll
      for (int k = 0; k < 6; ++k) { t2[k] = sw32add(t1[k], t1[k + 6]); t2[k] += dpp<ROR8>(t2[k]); }
      LAS float* rec = (LAS float*)(L + L_REC) + ((w - 1) * 2 + b) * RECF;
      if (!(lane & 8)) {
        const int a0 = 6 * hf + 12 * ((lane >> 4) & 1);
#pragma unroll
        for (int k = 0; k < 6; k += 2) { const int ai = a0 + k; *(LAS v2f*)(rec + 4 + ((lane & 7) + 8 * (ai >> 3)) * 8 + (ai & 7)) = v2f{t2[k], t2[k + 1]}; }
      }
      if (lane == 0) *(LAS v2f*)rec = v2f{M, l};
#pragma unroll
      for (int jj = 0; jj < 3; ++jj) q[jj] = *(const LAS v4u*)(L + L_QS + DH * 2 + ((lane & 7) + 8 * jj) * 16);
      st.m = M_INIT; st.l = 0.f;
#pragma unroll
      for (int k = 0; k < 24; ++k) st.acc[k] = 0.f;
    }
  }
  const int g4 = lane >> 4, c16 = lane & 15, tq = c16 >> 2, tp = lane & 3;
  const v4f ctag = {0.f, ubits(tag), 0.f, ubits(tag)};
  const v4u zero4 = {0u, 0u, 0u, 0u};
  BARRIER();
  const bool tilew = w == 0 || w >= 4;
  const int tile = w ? w - 3 : 0;
  unsigned xv0 = 0, xv1 = 0;
  v4u bw[6], bg[3];
  auto gu_frags = [&]() {
    landed(CT + 3);
#pragma unroll
    for (int k = 0; k < 3; ++k) {
      const int row = 32 * k + 8 * g4 + tq;
      bg[k] = bfrag<128>(L + L_G + (tp >> 1) * 10240 + (row < 80 ? row : tq) * 128 + (8 * w + 4 * (tp & 1)) * 2);
    }
  };
  if (!tilew) {
    const int dim = tid - 64;
    {
      const LAS float* rec = (const LAS float*)(L + L_REC);
      v2f ml = *(const LAS v2f*)(rec + (c16 < 2 * NS ? c16 : 0) * RECF);
      float av[2 * NS];
#pragma unroll
      for (int r = 0; r < 2 * NS; ++r) av[r] = rec[r * RECF + 4 + dim];
      if (c16 >= 2 * NS) ml = v2f{M_INIT, 0.f};
      float M = fmaxf(ml.x, dpp<XOR2>(ml.x)); M = fmaxf(M, dpp<ROR4>(M)); M = fmaxf(M, dpp<ROR8>(M));
      const float e = __builtin_amdgcn_exp2f(ml.x - M);
      float ls = e * ml.y; ls += dpp<XOR2>(ls); ls += dpp<ROR4>(ls); ls += dpp<ROR8>(ls);
      float o0 = 0.f, o1 = 0.f;
#pragma unroll
      for (int r = 0; r < 2 * NS; r += 2) { o0 = fmaf(rdlane(e, r), av[r], o0); o1 = fmaf(rdlane(e, r + 1), av[r + 1], o1); }
      put(rs, WS_P + wg * PBLK + dim * 16, fbits(o0), fbits(o1), tag);
      const float lsx = dpp<XOR1>(ls), Mx = dpp<XOR1>(M);
      put(rs, dim < 2 ? WS_P + wg * PBLK + (DH + dim) * 16 : OOB, fbits((lane & 1) ? lsx : M), fbits((lane & 1) ? ls : Mx), tag);
    }
    v4u g[8], gm = zero4;
#pragma unroll
    for (int p = 0; p < 8; ++p) g[p] = zero4;
    const int mp = (lane >> 1) & 7, po = WS_P + h * 8 * PBLK + dim * 16, pm = WS_P + (h * 8 + mp) * PBLK + (DH + (lane & 1)) * 16;
    for (unsigned sent = 0, spin = 0; sent != 0xffu && spin < 4 * SPIN_CAP; ++spin) {
      const unsigned want = __builtin_amdgcn_readfirstlane(CT[5]) & ~sent & 0xffu;
#pragma unroll
      for (int p = 0; p < 8; ++p)
        if ((want >> p) & 1u) g[p] = __builtin_amdgcn_raw_buffer_load_b128(rs, po, p * PBLK, 16);
      if ((want >> mp) & 1u) gm = __builtin_amdgcn_raw_buffer_load_b128(rs, pm, 0, 16);
      sent |= want;
    }
    for (int spin = 0; spin < SPIN_CAP; ++spin) {
      bool ok = true;
#pragma unroll
      for (int p = 0; p < 8; ++p)
        if (!__all(g[p].y == tag && g[p].w == tag)) { g[p] = __builtin_amdgcn_raw_buffer_load_b128(rs, po, p * PBLK, 16); ok = false; }
      if (!__all(gm.y == tag && gm.w == tag)) { gm = __builtin_amdgcn_raw_buffer_load_b128(rs, pm, 0, 16); ok = false; }
      if (ok) break;
      asm volatile("" ::: "memory");
    }
    float M0 = ubits(gm.x), M1 = ubits(gm.z);
    M0 = fmaxf(M0, dpp<XOR2>(M0)); M1 = fmaxf(M1, dpp<XOR2>(M1)); M0 = fmaxf(M0, dpp<ROR4>(M0)); M1 = fmaxf(M1, dpp<ROR4>(M1)); M0 = fmaxf(M0, dpp<ROR8>(M0)); M1 = fmaxf(M1, dpp<ROR8>(M1));
    const float e0 = __builtin_amdgcn_exp2f(ubits(gm.x) - M0), e1 = __builtin_amdgcn_exp2f(ubits(gm.z) - M1);
    float l0 = e0 * dpp<XOR1>(ubits(gm.x)), l1 = e1 * dpp<XOR1>(ubits(gm.z));
    l0 += dpp<XOR2>(l0); l1 += dpp<XOR2>(l1); l0 += dpp<ROR4>(l0); l1 += dpp<ROR4>(l1); l0 += dpp<ROR8>(l0); l1 += dpp<ROR8>(l1);
    float o0 = 0.f, o1 = 0.f;
#pragma unroll
    for (int p = 0; p < 8; ++p) { o0 = fmaf(rdlane(e0, 2 * p), ubits(g[p].x), o0); o1 = fmaf(rdlane(e1, 2 * p), ubits(g[p].z), o1); }
    const unsigned ob = pk(o0 * __builtin_amdgcn_rcpf(rdlane(l0, 0)), o1 * __builtin_amdgcn_rcpf(rdlane(l1, 0)));
    ((LAS unsigned short*)(L + L_O))[dim] = (unsigned short)ob; ((LAS unsigned short*)(L + L_O))[DH + dim] = (unsigned short)(ob >> 16);
  } else {
    xv0 = a.x[80 * j + 16 * tile + c16]; xv1 = a.x[DM + 80 * j + 16 * tile + c16];
    if (w == 0) { waitvm<0>(); if (lane == 0) CT[2] = 1u; }
    if (w == NW - 1) watch<8>(rs, WS_G + h * 8 * 128, 128, lane, tag, CT + 5);
    landed(CT + 2);
#pragma unroll
    for (int k = 0; k < 6; ++k) bw[k] = bfrag<160>(L + L_WO + (32 * k + 8 * g4 + tq) * 160 + (16 * tile + 4 * tp) * 2);
    if (w == 0 && ffn) load_ffn();
  }
  BARRIER();
  if (tilew) {
    const int ybase = WS_Y + (j * 5 + tile) * (NH * 256);
    __builtin_amdgcn_raw_buffer_store_b32(tag, rs, tid == 0 ? WS_D + wg * 128 : OOB, 0, 16);
    v4f d = ctag;
#pragma unroll
    for (int k = 0; k < 6; ++k) {
      const v4u ao = *(const LAS v4u*)(L + L_O + (c16 & 2) * DH + (32 * k + 8 * g4) * 2);
      d = mfma((c16 & 1) ? zero4 : ao, bw[k], d);
    }
    __builtin_amdgcn_raw_buffer_store_b128(__builtin_bit_cast(v4u, d), rs, lane < 16 ? ybase + h * 256 + c16 * 16 : OOB, 0, 16);
    if (ffn) {
      if (w == 0) { waitvm<0>(); if (lane == 0) CT[3] = 1u; }
      gu_frags();
      v4u g[8];
#pragma unroll
      for (int k = 0; k < 8; ++k) g[k] = zero4;
      for (unsigned sent = 0, spin = 0; sent != 0x11111111u && spin < 4 * SPIN_CAP; ++spin) {
        unsigned m = __builtin_amdgcn_readfirstlane(CT[6]);
        m &= m >> 1; m &= m >> 2; m &= 0x11111111u & ~sent;
#pragma unroll
        for (int k = 0; k < 8; ++k)
          if ((m >> (4 * k)) & 1u) g[k] = __builtin_amdgcn_raw_buffer_load_b128(rs, ybase + lane * 16 + (k & 3) * 1024, (k >> 2) * 4096, 16);
        sent |= m;
      }
      for (int spin = 0; spin < SPIN_CAP; ++spin) {
        bool ok = true;
#pragma unroll
        for (int k = 0; k < 8; ++k)
          if (!__all(g[k].y == tag && g[k].w == tag)) { g[k] = __builtin_amdgcn_raw_buffer_load_b128(rs, ybase + lane * 16 + (k & 3) * 1024, (k >> 2) * 4096, 16); ok = false; }
        if (ok) break;
        asm volatile("" ::: "memory");
      }
      const float s0 = ((ubits(g[0].x) + ubits(g[1].x)) + (ubits(g[2].x) + ubits(g[3].x))) + ((ubits(g[4].x) + ubits(g[5].x)) + (ubits(g[6].x) + ubits(g[7].x)));
      const float s1 = ((ubits(g[0].z) + ubits(g[1].z)) + (ubits(g[2].z) + ubits(g[3].z))) + ((ubits(g[4].z) + ubits(g[5].z)) + (ubits(g[6].z) + ubits(g[7].z)));
      float s = sw16add(s0, s1); s = sw32add(s, s);
      const float hv = bflo((g4 & 1) ? xv1 : xv0) + bfr(s);
      if (lane < 32) ((LAS unsigned short*)(L + L_HS))[g4 * 96 + 16 * tile + c16] = (unsigned short)pk(hv, 0.f);
    }
  } else if (ffn) {
    if (w == 1) watch<32>(rs, WS_D + j * 128, 8 * 128, lane, tag, CT + 6);
    gu_frags();
  }
  if (ffn) {
    int eo = WS_E + (cg * 8 + w) * 5120;
    const int eo0 = lane < 32 ? eo + j * 512 + lane * 16 : OOB, eo1 = lane < 8 ? eo + 4096 + j * 128 + lane * 16 : OOB;
    BARRIER();
    {
      v4f d = ctag, ds = ctag;
#pragma unroll
      for (int k = 0; k < 3; ++k) {
        v4u ah = *(const LAS v4u*)(L + L_HS + (c16 < 5 ? c16 >> 1 : 0) * 192 + (32 * k + 8 * g4) * 2);
        if ((c16 & 1) || c16 > 4) ah = zero4;
        d = mfma(ah, bg[k], d);
        ds = mfma(ah, ah, ds);
      }
      __builtin_amdgcn_raw_buffer_store_b128(__builtin_bit_cast(v4u, d), rs, eo0, 0, 16);
      __builtin_amdgcn_raw_buffer_store_b128(__builtin_bit_cast(v4u, ds), rs, eo1, 0, 16);
    }
    if (w >= 1 && w <= 4) {
      v4u g[5], bdn[3]; int zo[3];
#pragma unroll
      for (int T = 0; T < 3; ++T) {
        const int dt = w - 1 + 4 * T, n = 64 * dt + lane;
        bdn[T] = bfrag<1280>(L + L_WD + tq * 1280 + (64 * (dt < 10 ? dt : 0) + 16 * g4 + 4 * tp) * 2);
        zo[T] = dt < 10 ? WS_Z + ((n >> 2) * NFFN + f) * 64 + (n & 3) * 16 : OOB;
        asm volatile("" : "+v"(zo[T]));
      }
      __builtin_amdgcn_s_sleep(10);
      gather<5>(rs, WS_E + f * 5120 + lane * 16, tag, g);
      __builtin_amdgcn_raw_buffer_store_b32(tag, rs, (tid == 64 && j == 0) ? WS_F + cg * 128 : OOB, 0, 16);
      const float s0 = (ubits(g[0].x) + ubits(g[1].x)) + (ubits(g[2].x) + ubits(g[3].x)), s1 = (ubits(g[0].z) + ubits(g[1].z)) + (ubits(g[2].z) + ubits(g[3].z));
      const float gs = sw32add(s0, s1);
      const float cs0 = sw32add(s0, s0);
      const auto cx = __builtin_amdgcn_permlane16_swap(fbits(cs0), fbits(cs0), false, false);
      const auto gx = __builtin_amdgcn_permlane16_swap(fbits(gs), fbits(gs), false, false);
      float t0 = ubits(g[4].x), t1 = ubits(g[4].z);
      t0 += dpp<ROR8>(t0); t1 += dpp<ROR8>(t1);
      t0 = sw16add(t0, t0); t1 = sw16add(t1, t1);
      const float tt = sw32add(t0, t1) * (1.0f / DM);
      const float mean = dpp<BC + 4>(tt), q0 = dpp<BC + 0>(tt), q1 = dpp<BC + 2>(tt);
      const float rdv = ln_rden(fmaxf((hf ? q1 : q0) - mean * mean, 0.f));
      const float gv = bfr((ubits(gx[0]) - bfr(mean) * ubits(cx[1])) * rdv), uv = dpp<ROR8>(gv);
      const float y2 = (-2.0f * 0.7978845608028654f * 1.4426950408889634f) * (gv + 0.044715f * gv * gv * gv);
      const float act = bfr(gv * __builtin_amdgcn_rcpf(1.0f + __builtin_amdgcn_exp2f(y2))) * uv;
      const unsigned ap = pk(act, dpp<XOR1>(act));
      const auto ax = __builtin_amdgcn_permlane32_swap(ap, ap, false, false);
      const unsigned keep = ((c16 >> 2) == g4 && !(c16 & 1)) ? 0xffffffffu : 0u, k1 = (c16 & 2) ? keep : 0u, k0 = keep ^ k1;
      const v4u b0 = {dppu<BC + 0>(ax[0]), dppu<BC + 2>(ax[0]), dppu<BC + 4>(ax[0]), dppu<BC + 6>(ax[0])};
      const v4u b1 = {dppu<BC + 0>(ax[1]), dppu<BC + 2>(ax[1]), dppu<BC + 4>(ax[1]), dppu<BC + 6>(ax[1])};
      const v4u a2 = (b0 & k0) | (b1 & k1);
#pragma unroll
      for (int T = 0; T < 3; ++T) __builtin_amdgcn_raw_buffer_store_b128(__builtin_bit_cast(v4u, mfma(a2, bdn[T], ctag)), rs, zo[T], 0, 16);
    } else if (w == NW - 1 && cg < 20) {
      const __amdgpu_buffer_rsrc_t ro = __builtin_amdgcn_make_buffer_rsrc((void*)a.out, 0, 2 * DM * 2, 0x00020000);
      const float hv = bflo(((const LAS unsigned short*)(L + L_HS))[(g4 & 1) * 96 + 4 * cg + (lane & 3)]);
      v4u g[12];
      watch<24>(rs, WS_F, 128, lane, tag, CT + 7);
      gather<12>(rs, WS_Z + (20 * j + cg) * (NFFN * 64) + lane * 16, tag, g);
      float s0[4], s1[4];
#pragma unroll
      for (int k = 0; k < 4; ++k) { s0[k] = (ubits(g[k].x) + ubits(g[k + 4].x)) + ubits(g[k + 8].x); s1[k] = (ubits(g[k].z) + ubits(g[k + 4].z)) + ubits(g[k + 8].z); }
      float s = sw16add((s0[0] + s0[1]) + (s0[2] + s0[3]), (s1[0] + s1[1]) + (s1[2] + s1[3]));
      s = sw32add(s, s); s += dpp<ROR4>(s); s += dpp<ROR8>(s);
      const unsigned o = pk(hv + bfr(s), 0.f);
      __builtin_amdgcn_raw_buffer_store_b16((unsigned short)o, ro, (lane < 32 && c16 < 4) ? (g4 * DM + 80 * j + 4 * cg + c16) * 2 : OOB, 0, 0);
      __builtin_amdgcn_raw_buffer_store_b32(tag, rs, (wg == 0 && lane == 0) ? 0 : OOB, 0, 16);
    }
  }
}
}

extern "C" void kernel_launch(void* const* d_in, const int* in_sizes, int n_in, void* d_out, int out_size, void* d_ws, size_t ws_size, hipStream_t stream) {
  Args a;
  a.x = (const unsigned short*)d_in[0]; a.k = (const unsigned short*)d_in[1]; a.v = (const unsigned short*)d_in[2];
  a.wq = (const unsigned short*)d_in[3]; a.wo = (const unsigned short*)d_in[4];
  a.wg = (const unsigned short*)d_in[6]; a.wu = (const unsigned short*)d_in[7]; a.wd = (const unsigned short*)d_in[8];
  a.out = (unsigned short*)d_out; a.ws = (unsigned char*)d_ws;
  hipLaunchKernelGGL(decode_block, dim3(NWG), dim3(NTHR), 0, stream, a);
}
```
